```python
import torch

def _softmax(x, axis=-1):
    return torch.softmax(x, dim=axis)
def _gelu(x):
    return torch.nn.functional.gelu(x, approximate="tanh")
def _silu(x):
    return torch.nn.functional.silu(x)
def _sigmoid(x):
    return torch.sigmoid(x)
def _layernorm(x, axis=-1, eps=1e-5):
    m = x.mean(dim=axis, keepdim=True)
    v = x.var(dim=axis, keepdim=True, unbiased=False)
    return (x - m) / torch.sqrt(v + eps)
def _scatter_add(tgt, idx, src, axis=0):
    return tgt.clone().index_add_(axis, idx, src)
def _segment_sum(src, seg, n):
    out = torch.zeros((n,) + src.shape[1:], dtype=src.dtype)
    return out.index_add_(0, seg, src)
def _pad(x, pw):
    flat = tuple(v for pair in reversed(pw) for v in pair)
    return torch.nn.functional.pad(x, flat)

kernel_name = 'recurrent_scan__linear_attn_block__swiglu_ffn__prompt'

def setup_inputs():

    _w = (lambda a: (lambda t: t.to(torch.bfloat16) if t.is_floating_point() else t)(torch.as_tensor(a)))
    def _A_setup():
        B, S, D = 1, 2048, 1024
        return {
            "x": _w((lambda *s: torch.randn(*s, dtype=torch.bfloat16) * 0.05)(B, S, D)),
            "wqkv": _w((lambda *s: torch.randn(*s, dtype=torch.bfloat16) * 0.1)(D, 3 * D)),
            "wo": _w((lambda *s: torch.randn(*s, dtype=torch.bfloat16) * 0.05)(D, D)),
        }
    _A_in = _A_setup()

    def _B_setup():
        B, S, D, F = 1, 2048, 1024, 3584
        return {
            "x": _w((lambda *s: torch.randn(*s, dtype=torch.bfloat16) * 0.05)(B, S, D)),
            "w_gate": _w((lambda *s: torch.randn(*s, dtype=torch.bfloat16) * 0.05)(D, F)),
            "w_up": _w((lambda *s: torch.randn(*s, dtype=torch.bfloat16) * 0.05)(D, F)),
            "w_down": _w((lambda *s: torch.randn(*s, dtype=torch.bfloat16) * 0.05)(F, D)),
        }
    _B_in = _B_setup()

    return {
        "A_x": _A_in["x"],
        "A_wqkv": _A_in["wqkv"],
        "A_wo": _A_in["wo"],
        "B_x": _B_in["x"],
        "B_w_gate": _B_in["w_gate"],
        "B_w_up": _B_in["w_up"],
        "B_w_down": _B_in["w_down"],
    }

def reference(A_x, A_wqkv, A_wo, B_x, B_w_gate, B_w_up, B_w_down):
    def _A_ref(x, wqkv, wo):
        B, S, D = x.shape
        H = 32; Dh = D // H
        qkv = (lambda x, *d: x.permute(*d))((x @ wqkv).reshape(B, S, 3, H, Dh), 2, 0, 3, 1, 4)
        q, k, v = qkv[0], qkv[1], qkv[2]
        phi = lambda t: torch.where(t > 0, t + 1, torch.exp(t)) + 1e-6
        pq, pk = phi(q), phi(k)
        kv = torch.einsum("bhsd,bhse->bhde", pk, v)
        z = torch.einsum("bhsd->bhd", pk)
        num = torch.einsum("bhqd,bhde->bhqe", pq, kv)
        den = torch.einsum("bhqd,bhd->bhq", pq, z)[..., None] + 1e-6
        o = num / den
        return (lambda x, *d: x.permute(*d))(o, 0, 2, 1, 3).reshape(B, S, D) @ wo

    def _B_ref(x, w_gate, w_up, w_down):
        g = _silu(x @ w_gate)
        u = x @ w_up
        return (g * u) @ w_down

    x = A_x
    xn = _layernorm(x, axis=-1)
    s = _A_ref(xn, A_wqkv, A_wo)
    g = _silu(_B_ref(xn, B_w_gate, B_w_up, B_w_down))
    return x + s * g
```

```cpp
#include <hip/hip_runtime.h>
#include <hip/hip_bf16.h>
typedef unsigned short u16;
__device__ __forceinline__ float bf2f(u16 v){ return __uint_as_float(((unsigned)v)<<16); }
__device__ __forceinline__ u16 f2bf(float f){ unsigned u=__float_as_uint(f); u += 0x7fffu + ((u>>16)&1u); return (u16)(u>>16); }
constexpr int S=2048,D=1024,F=3584,H=32,DH=32;

__global__ void k_ln(const u16* x, u16* xn){
  int r=blockIdx.x; __shared__ float red[256];
  float v[4]; float s=0; for(int i=0;i<4;i++){v[i]=bf2f(x[r*D+threadIdx.x+256*i]); s+=v[i];}
  red[threadIdx.x]=s; __syncthreads(); for(int o=128;o>0;o>>=1){ if(threadIdx.x<o) red[threadIdx.x]+=red[threadIdx.x+o]; __syncthreads(); }
  float m=red[0]/D; __syncthreads(); float q=0; for(int i=0;i<4;i++){float d=v[i]-m; q+=d*d;}
  red[threadIdx.x]=q; __syncthreads(); for(int o=128;o>0;o>>=1){ if(threadIdx.x<o) red[threadIdx.x]+=red[threadIdx.x+o]; __syncthreads(); }
  float rs=rsqrtf(red[0]/D+1e-5f);
  for(int i=0;i<4;i++) xn[r*D+threadIdx.x+256*i]=f2bf((v[i]-m)*rs);
}
__global__ void k_gemm(const u16* A,const u16* B,float* C,int M,int N,int K){
  __shared__ float As[32][33],Bs[32][33];
  int tx=threadIdx.x&31,ty=threadIdx.x>>5; int row0=blockIdx.y*32,col0=blockIdx.x*32;
  float acc[4]={0,0,0,0};
  for(int k0=0;k0<K;k0+=32){
    for(int i=0;i<4;i++){ As[ty+8*i][tx]=bf2f(A[(size_t)(row0+ty+8*i)*K+k0+tx]); Bs[ty+8*i][tx]=bf2f(B[(size_t)(k0+ty+8*i)*N+col0+tx]); }
    __syncthreads();
    for(int k=0;k<32;k++){ float b=Bs[k][tx]; for(int i=0;i<4;i++) acc[i]+=As[ty+8*i][k]*b; }
    __syncthreads();
  }
  for(int i=0;i<4;i++) C[(size_t)(row0+ty+8*i)*N+col0+tx]=acc[i];
}
__device__ __forceinline__ float phi(float t){ return (t>0.f? t+1.f: __expf(t))+1e-6f; }
__global__ void k_kv(const float* qkv,float* kv,float* z){
  int h=blockIdx.x,d=threadIdx.x>>5,e=threadIdx.x&31; float s=0,zz=0;
  for(int t=0;t<S;t++){ float pk=phi(qkv[(size_t)t*3072+1024+h*32+d]); s+=pk*qkv[(size_t)t*3072+2048+h*32+e]; zz+=pk; }
  kv[h*1024+d*32+e]=s; if(e==0) z[h*32+d]=zz;
}
__global__ void k_o(const float* qkv,const float* kv,const float* z,u16* o){
  int r=blockIdx.x; int c=threadIdx.x; int h=c>>5,e=c&31; float num=0,den=0;
  for(int d=0;d<32;d++){ float pq=phi(qkv[(size_t)r*3072+h*32+d]); num+=pq*kv[h*1024+d*32+e]; den+=pq*z[h*32+d]; }
  o[r*D+c]=f2bf(num/(den+1e-6f));
}
__global__ void k_h(const float* g,const float* u,u16* hh){ size_t i=blockIdx.x*256+threadIdx.x; float a=g[i]; hh[i]=f2bf(a/(1.f+__expf(-a))*u[i]); }
__global__ void k_out(const u16* x,const float* s,const float* d,u16* out){ size_t i=blockIdx.x*256+threadIdx.x; float a=d[i]; out[i]=f2bf(bf2f(x[i])+s[i]*(a/(1.f+__expf(-a)))); }

extern "C" void kernel_launch(void* const* d_in, const int* in_sizes, int n_in, void* d_out, int out_size, void* d_ws, size_t ws_size, hipStream_t stream){
  const u16* x=(const u16*)d_in[0]; const u16* wqkv=(const u16*)d_in[1]; const u16* wo=(const u16*)d_in[2];
  const u16* wg=(const u16*)d_in[4]; const u16* wu=(const u16*)d_in[5]; const u16* wd=(const u16*)d_in[6];
  char* ws=(char*)d_ws; size_t off=0;
  u16* xn=(u16*)(ws+off); off+=(size_t)S*D*2;
  float* qkv=(float*)(ws+off); off+=(size_t)S*3072*4;
  float* g=(float*)(ws+off); off+=(size_t)S*F*4;
  float* u=(float*)(ws+off); off+=(size_t)S*F*4;
  u16* hh=(u16*)(ws+off); off+=(size_t)S*F*2;
  float* kv=(float*)(ws+off); off+=32*1024*4;
  float* z=(float*)(ws+off); off+=1024*4;
  u16* o=(u16*)(ws+off); off+=(size_t)S*D*2;
  float* s=(float*)(ws+off); off+=(size_t)S*D*4;
  float* dd=(float*)(ws+off); off+=(size_t)S*D*4;
  k_ln<<<S,256,0,stream>>>(x,xn);
  k_gemm<<<dim3(3072/32,S/32),256,0,stream>>>(xn,wqkv,qkv,S,3072,D);
  k_gemm<<<dim3(F/32,S/32),256,0,stream>>>(xn,wg,g,S,F,D);
  k_gemm<<<dim3(F/32,S/32),256,0,stream>>>(xn,wu,u,S,F,D);
  k_h<<<S*F/256,256,0,stream>>>(g,u,hh);
  k_kv<<<32,1024,0,stream>>>(qkv,kv,z);
  k_o<<<S,1024,0,stream>>>(qkv,kv,z,o);
  k_gemm<<<dim3(D/32,S/32),256,0,stream>>>(o,wo,s,S,D,D);
  k_gemm<<<dim3(D/32,S/32),256,0,stream>>>(hh,wd,dd,S,D,F);
  k_out<<<S*D/256,256,0,stream>>>(x,s,dd,(u16*)d_out);
}
```

```cpp
#include <hip/hip_runtime.h>
#include <hip/hip_bf16.h>
typedef unsigned short u16;
typedef unsigned long long u64;
typedef float f32x2 __attribute__((ext_vector_type(2)));
typedef float f32x4 __attribute__((ext_vector_type(4)));
typedef float f32x32 __attribute__((ext_vector_type(32)));
typedef __bf16 bf16x2 __attribute__((ext_vector_type(2)));
typedef __bf16 bf16x8 __attribute__((ext_vector_type(8)));
typedef unsigned u32x2 __attribute__((ext_vector_type(2)));
typedef unsigned u32x4 __attribute__((ext_vector_type(4)));
typedef unsigned u32x8 __attribute__((ext_vector_type(8)));
typedef unsigned u32x16 __attribute__((ext_vector_type(16)));
typedef unsigned u32x32 __attribute__((ext_vector_type(32)));
typedef __amdgpu_buffer_rsrc_t rsrc_t;
#define DI __device__ __forceinline__
#define RFL(x) __builtin_amdgcn_readfirstlane(x)
#define LAS __attribute__((address_space(3)))
#define GAS __attribute__((address_space(1)))
typedef const __attribute__((address_space(4))) u64* KARG;
#define G1_ASM_T0 \
"s_movk_i32 s74, 0x800\n" \
"s_movk_i32 s75, 0x400\n" \
"s_mov_b32 m0, %3\n" \
"s_cmp_lg_u32 %2, 0\n" \
"s_cbranch_scc1 L_t0_0_%=\n" \
"buffer_load_dword %0, s[40:43], 0 offen lds\n" \
"buffer_load_dword %1, s[56:59], 0 offen lds\n" \
"L_t0_0_%=:\n" \
"s_cmp_lg_u32 %2, 1\n" \
"s_cbranch_scc1 L_t0_1_%=\n" \
"buffer_load_dword %0, s[44:47], s74 offen lds\n" \
"buffer_load_dword %1, s[60:63], s75 offen lds\n" \
"L_t0_1_%=:\n" \
"s_cmp_lg_u32 %2, 2\n" \
"s_cbranch_scc1 L_t0_2_%=\n" \
"buffer_load_dword %0, s[48:51], 0 offen lds\n" \
"buffer_load_dword %1, s[56:59], 0 offen lds\n" \
"L_t0_2_%=:\n" \
"s_cmp_lg_u32 %2, 3\n" \
"s_cbranch_scc1 L_t0_3_%=\n" \
"buffer_load_dword %0, s[52:55], s74 offen lds\n" \
"buffer_load_dword %1, s[60:63], s75 offen lds\n" \
"L_t0_3_%=:\n"
#define ASM_CODE \
"s_getpc_b64 %0\n" \
"L_code_here:\n" \
"s_mov_b32 m0, %2\n" \
"v_min_u32 %1, L_code_end-L_code_here-4, %1\n" \
"global_load_lds_dword %1, %0\n"
#define G1_ASM_TI \
"s_mov_b32 m0, %2\n" \
"s_nop 0\n" \
"buffer_load_dword %0, %1, 0 offen sc1 lds\n"
#define G1_ASM_X \
"s_nop 4\n" \
"buffer_load_dwordx4 v[32:35], %1, %2, 0 offen offset:0\n" \
"buffer_load_dwordx4 v[36:39], %1, %2, 0 offen offset:128\n" \
"buffer_load_dwordx4 v[40:43], %1, %2, 0 offen offset:256\n" \
"buffer_load_dwordx4 v[44:47], %1, %2, 0 offen offset:384\n"
#define G1_ASM_PRO \
"s_nop 4\n" \
"s_sub_u32 s74, s66, s68\n" \
"s_add_u32 m0, s71, 0\n" \
"s_nop 0\n" \
"buffer_load_dwordx4 v8, s[40:43], 0 offen offset:0 lds\n" \
"buffer_load_dwordx4 v10, s[40:43], s74 offen offset:1024 lds\n" \
"buffer_load_dwordx4 v8, s[44:47], 0 offen offset:2048 lds\n" \
"buffer_load_dwordx4 v10, s[44:47], s74 offen offset:3072 lds\n" \
"s_add_u32 m0, s71, 4096\n" \
"s_nop 0\n" \
"buffer_load_dwordx4 v8, s[48:51], 0 offen offset:0 lds\n" \
"buffer_load_dwordx4 v10, s[48:51], s74 offen offset:1024 lds\n" \
"buffer_load_dwordx4 v8, s[52:55], 0 offen offset:2048 lds\n" \
"buffer_load_dwordx4 v10, s[52:55], s74 offen offset:3072 lds\n" \
"s_add_u32 m0, s72, 0\n" \
"s_nop 0\n" \
"buffer_load_dwordx4 v9, s[56:59], 0 offen offset:0 lds\n" \
"buffer_load_dwordx4 v9, s[60:63], 0 offen offset:1024 lds\n" \
"s_add_u32 m0, s71, 32768\n" \
"s_nop 0\n" \
"buffer_load_dwordx4 v8, s[40:43], s65 offen offset:0 lds\n" \
"buffer_load_dwordx4 v10, s[40:43], s66 offen offset:1024 lds\n"
#define G1_ASM_XWAIT "s_waitcnt vmcnt(2)"
#define G1_ASM_SWAIT "s_waitcnt vmcnt(0)"
#define PRO_LATE 2
#define MAIN_ASM \
"s_nop 4\n" \
"buffer_load_dwordx2 v[22:23], v21, s[96:99], 0 offen sc1\n" \
"s_load_dwordx2 s[4:5], s[12:13], 0x10\n" \
"s_load_dwordx4 s[8:11], s[12:13], 0x28\n" \
"s_mov_b32 s78, s69\n" \
"s_mov_b32 s69, s67\n" \
"s_add_u32 s70, s71, 0x10000\n" \
"s_lshr_b32 s79, s71, 1\n" \
"s_lshr_b32 s77, s71, 5\n" \
"s_add_u32 s77, s77, 155648\n" \
"s_mov_b32 s34, 0x7d2f6481\n" \
"s_mov_b32 s35, 0x5be9c3a1\n" \
"s_mov_b32 s33, 0\n" \
"s_and_b32 s6, s15, 1\n" \
"s_lshr_b32 s7, s15, 1\n" \
"s_lshr_b32 s3, s14, 5\n" \
"s_and_b32 s2, s14, 31\n" \
"v_mbcnt_lo_u32_b32 v20, -1, 0\n" \
"v_mbcnt_hi_u32_b32 v20, -1, v20\n" \
"v_mov_b32 v24, 0\n" \
"v_mov_b32 v25, 0\n" \
"v_mov_b32 v26, 0\n" \
"v_mov_b32 v27, 0\n" \
"v_mov_b32 v28, s34\n" \
"v_mov_b32 v29, s35\n" \
"v_mfma_f32_32x32x16_bf16 a[160:175], v[24:27], v[24:27], 0\n" \
"v_mfma_f32_32x32x16_bf16 a[176:191], v[24:27], v[24:27], 0\n" \
"v_mfma_f32_32x32x16_bf16 a[192:207], v[24:27], v[24:27], 0\n" \
"v_mfma_f32_32x32x16_bf16 a[208:223], v[24:27], v[24:27], 0\n" \
"v_mfma_f32_32x32x16_bf16 a[224:239], v[24:27], v[24:27], 0\n" \
"v_mfma_f32_32x32x16_bf16 a[240:255], v[24:27], v[24:27], 0\n" \
"v_mfma_f32_32x32x16_bf16 v[32:47], v[24:27], v[24:27], 0\n" \
"v_lshlrev_b32 v14, 4, v20\n" \
"v_add_u32 v7, s71, v14\n" \
"s_lshl_b32 s74, s7, 14\n" \
"v_mfma_f32_32x32x16_bf16 v[48:63], v[24:27], v[24:27], 0\n" \
"s_add_u32 s74, s74, 0x10000\n" \
"v_add_u32 v0, s74, v14\n" \
"v_and_b32 v30, 3, v20\n" \
"v_mfma_f32_32x32x16_bf16 v[64:79], v[24:27], v[24:27], 0\n" \
"v_lshlrev_b32 v30, 3, v30\n" \
"v_and_b32 v12, 24, v20\n" \
"v_lshl_or_b32 v13, v12, 2, v30\n" \
"v_mfma_f32_32x32x16_bf16 v[80:95], v[24:27], v[24:27], 0\n" \
"v_and_b32 v12, 12, v20\n" \
"v_lshl_or_b32 v1, v12, 5, v13\n" \
"v_lshl_or_b32 v5, v12, 4, v30\n" \
"v_mfma_f32_32x32x16_bf16 v[96:111], v[24:27], v[24:27], 0\n" \
"v_and_b32 v12, 16, v20\n" \
"v_lshl_or_b32 v1, v12, 6, v1\n" \
"v_lshl_or_b32 v5, v12, 5, v5\n" \
"v_mfma_f32_32x32x16_bf16 v[112:127], v[24:27], v[24:27], 0\n" \
"v_lshl_or_b32 v5, v12, 1, v5\n" \
"v_and_b32 v12, 32, v20\n" \
"v_lshl_or_b32 v1, v12, 8, v1\n" \
"v_mfma_f32_32x32x16_bf16 v[128:143], v[24:27], v[24:27], 0\n" \
"v_lshl_or_b32 v5, v12, 6, v5\n" \
"v_lshl_or_b32 v17, v12, 6, v13\n" \
"s_lshl_b32 s74, s6, 12\n" \
"v_mfma_f32_32x32x16_bf16 v[144:159], v[24:27], v[24:27], 0\n" \
"v_add_u32 v1, s74, v1\n" \
"v_xor_b32 v2, 32, v1\n" \
"v_xor_b32 v3, 64, v1\n" \
"v_mfma_f32_32x32x16_bf16 v[160:175], v[24:27], v[24:27], 0\n" \
"v_xor_b32 v4, 0x60, v1\n" \
"s_lshl_b32 s74, s6, 10\n" \
"s_lshl_b32 s76, s6, 5\n" \
"v_mfma_f32_32x32x16_bf16 v[176:191], v[24:27], v[24:27], 0\n" \
"s_cmp_lg_u32 s78, 0\n" \
"s_cselect_b32 s74, s74, s76\n" \
"s_cselect_b32 s76, 32, 0x400\n" \
"v_mfma_f32_32x32x16_bf16 v[192:207], v[24:27], v[24:27], 0\n" \
"s_add_u32 s74, s74, 0x20000\n" \
"v_xor_b32 v5, s74, v5\n" \
"v_xor_b32 v6, s76, v5\n" \
"v_mfma_f32_32x32x16_bf16 v[208:223], v[24:27], v[24:27], 0\n" \
"v_and_b32 v12, 28, v20\n" \
"v_lshl_or_b32 v17, v12, 5, v17\n" \
"s_lshl_b32 s74, s6, 10\n" \
"v_mfma_f32_32x32x16_bf16 v[224:239], v[24:27], v[24:27], 0\n" \
"v_add_u32 v17, s74, v17\n" \
"v_mfma_f32_32x32x16_bf16 v[240:255], v[24:27], v[24:27], 0\n" \
"s_mov_b32 s27, 64\n" \
"s_waitcnt lgkmcnt(0)\n" \
"s_cmp_eq_u32 s15, 0\n" \
"s_cbranch_scc1 L_t0_%=\n" \
"s_cmp_eq_u32 s15, 3\n" \
"s_cbranch_scc1 L_t3_%=\n" \
"s_mov_b32 s75, 15\n" \
"s_lshr_b32 s74, s14, 3\n" \
"s_cmp_eq_u32 s15, 1\n" \
"s_cbranch_scc0 L_t2_%=\n" \
"s_and_b32 s76, s14, 1\n" \
"s_mul_i32 s30, s76, 20\n" \
"s_lshl_b32 s31, s76, 3\n" \
"s_add_u32 s31, s31, 20\n" \
"s_cmp_lt_u32 s74, 20\n" \
"s_cselect_b32 s30, s30, s31\n" \
"s_min_u32 s74, s74, 27\n" \
"s_add_u32 s30, s30, s74\n" \
"s_lshl_b32 s30, s30, 17\n" \
"s_and_b32 s76, s14, 6\n" \
"s_lshl_b32 s76, s76, 8\n" \
"s_add_u32 s76, s76, s30\n" \
"s_add_u32 s30, s8, s76\n" \
"s_addc_u32 s31, s9, 0\n" \
"v_and_b32 v12, 3, v20\n" \
"v_lshlrev_b32 v12, 7, v12\n" \
"v_and_b32 v13, 12, v20\n" \
"v_lshl_or_b32 v12, v13, 9, v12\n" \
"v_mov_b32 v11, 0x2000\n" \
"s_branch L_tadd_%=\n" \
"L_t2_%=:\n" \
"s_lshl_b32 s74, s74, 16\n" \
"s_and_b32 s76, s14, 7\n" \
"s_lshl_b32 s76, s76, 8\n" \
"s_add_u32 s76, s76, s74\n" \
"s_add_u32 s30, s4, s76\n" \
"s_addc_u32 s31, s5, 0\n" \
"v_and_b32 v12, 1, v20\n" \
"v_lshlrev_b32 v12, 7, v12\n" \
"v_and_b32 v13, 2, v20\n" \
"v_lshl_or_b32 v12, v13, 10, v12\n" \
"v_mov_b32 v11, 0x1000\n" \
"s_branch L_tadd_%=\n" \
"L_t3_%=:\n" \
"s_getpc_b64 s[30:31]\n" \
"L_rt_here:\n" \
"s_mov_b32 s75, 16\n" \
"s_mov_b32 s27, 4\n" \
"s_mov_b32 s74, L_epi_gu-L_rt_here\n" \
"s_cmp_eq_u32 s78, 1\n" \
"s_cselect_b32 s74, L_epi_kv-L_rt_here, s74\n" \
"v_lshlrev_b32 v12, 7, v20\n" \
"v_mov_b32 v11, ((L_code_end-L_g2_code-6144)/16)*4\n" \
"v_mov_b32 v13, L_g2_code-L_rt_here-2048\n" \
"s_mov_b64 exec, 0xffff\n" \
"v_mov_b32 v11, 0x800\n" \
"v_mov_b32 v13, s74\n" \
"s_mov_b64 exec, -1\n" \
"v_add_u32 v12, v12, v13\n" \
"s_branch L_tadd_%=\n" \
"L_t0_%=:\n" \
"s_mov_b32 s75, 14\n" \
"s_lshl_b32 s74, s3, 3\n" \
"s_add_u32 s74, s74, 64\n" \
"v_and_b32 v12, 7, v20\n" \
"v_add_u32 v12, s74, v12\n" \
"v_mov_b32 v11, s68\n" \
"v_mov_b32 v15, s40\n" \
"v_mov_b32 v13, s41\n" \
"v_mov_b32 v16, 0\n" \
"s_mov_b32 exec_lo, 0xff00\n" \
"s_mov_b32 exec_hi, 0\n" \
"v_mov_b32 v15, s44\n" \
"v_mov_b32 v13, s45\n" \
"v_mov_b32 v16, 2048\n" \
"s_mov_b32 exec_lo, 0xff0000\n" \
"s_mov_b32 exec_hi, 0\n" \
"v_mov_b32 v15, s48\n" \
"v_mov_b32 v13, s49\n" \
"s_mov_b32 exec_lo, 0xff000000\n" \
"s_mov_b32 exec_hi, 0\n" \
"v_mov_b32 v15, s52\n" \
"v_mov_b32 v13, s53\n" \
"v_mov_b32 v16, 2048\n" \
"s_mov_b32 exec_lo, 0\n" \
"s_mov_b32 exec_hi, 0xff\n" \
"v_mov_b32 v15, s56\n" \
"v_mov_b32 v13, s57\n" \
"v_mov_b32 v11, s69\n" \
"s_mov_b32 exec_lo, 0\n" \
"s_mov_b32 exec_hi, 0xff00\n" \
"v_mov_b32 v15, s60\n" \
"v_mov_b32 v13, s61\n" \
"v_mov_b32 v16, 1024\n" \
"v_mov_b32 v11, s69\n" \
"s_mov_b64 exec, -1\n" \
"v_lshrrev_b32 v18, 6, v11\n" \
"v_mad_u32_u24 v12, v12, v18, v16\n" \
"v_add_co_u32 v12, vcc, v15, v12\n" \
"s_nop 1\n" \
"v_addc_co_u32 v13, vcc, 0, v13, vcc\n" \
"s_branch L_tdone_%=\n" \
"L_tadd_%=:\n" \
"v_mov_b32 v13, s31\n" \
"v_add_co_u32 v12, vcc, s30, v12\n" \
"s_nop 1\n" \
"v_addc_co_u32 v13, vcc, 0, v13, vcc\n" \
"L_tdone_%=:\n" \
"s_waitcnt vmcnt(0)\n" \
"v_cmp_eq_u64 vcc, s[34:35], v[22:23]\n" \
"s_nop 1\n" \
"s_cmp_eq_u64 vcc, -1\n" \
"s_cbranch_scc1 L_okln_%=\n" \
"s_mov_b32 s73, 0x40000\n" \
"L_spln_%=:\n" \
"buffer_load_dwordx2 v[22:23], v21, s[96:99], 0 offen offset:0 sc1\n" \
"s_waitcnt vmcnt(0)\n" \
"v_cmp_eq_u64 vcc, s[34:35], v[22:23]\n" \
"s_nop 1\n" \
"s_cmp_eq_u64 vcc, -1\n" \
"s_cbranch_scc1 L_okln_%=\n" \
"s_sleep 1\n" \
"s_sub_u32 s73, s73, 1\n" \
"s_cmp_lg_u32 s73, 0\n" \
"s_cbranch_scc1 L_spln_%=\n" \
"s_mov_b32 s33, 1\n" \
"L_okln_%=:\n" \
"buffer_store_dwordx2 v[26:27], v21, s[96:99], 0 offen offset:0 sc1\n" \
"s_add_u32 m0, s70, 0\n" \
"s_nop 0\n" \
"buffer_load_dwordx4 v7, s[36:39], s64 offen offset:0 sc1 lds\n" \
"buffer_load_dwordx4 v7, s[36:39], s64 offen offset:1024 sc1 lds\n" \
"buffer_load_dwordx4 v7, s[36:39], s64 offen offset:2048 sc1 lds\n" \
"buffer_load_dwordx4 v7, s[36:39], s64 offen offset:3072 sc1 lds\n" \
"s_add_u32 m0, s70, 4096\n" \
"s_add_u32 s74, s64, 4096\n" \
"s_nop 0\n" \
"buffer_load_dwordx4 v7, s[36:39], s74 offen offset:0 sc1 lds\n" \
"buffer_load_dwordx4 v7, s[36:39], s74 offen offset:1024 sc1 lds\n" \
"buffer_load_dwordx4 v7, s[36:39], s74 offen offset:2048 sc1 lds\n" \
"buffer_load_dwordx4 v7, s[36:39], s74 offen offset:3072 sc1 lds\n" \
"s_add_u32 s64, s64, 32768\n" \
"s_mov_b32 m0, s77\n" \
"s_sub_i32 s75, s75, 1\n" \
"s_cmp_lt_u32 s75, s27\n" \
"s_cselect_b32 s76, 1, 0\n" \
"s_nop 0\n" \
"global_load_lds_dword v[12:13], off\n" \
"v_mad_u64_u32 v[12:13], vcc, v11, s76, v[12:13]\n" \
"s_mov_b32 m0, s77\n" \
"s_sub_i32 s75, s75, 1\n" \
"s_cmp_lt_u32 s75, s27\n" \
"s_cselect_b32 s76, 1, 0\n" \
"s_nop 0\n" \
"global_load_lds_dword v[12:13], off\n" \
"v_mad_u64_u32 v[12:13], vcc, v11, s76, v[12:13]\n" \
"s_add_u32 m0, s71, 32768\n" \
"s_lshl_b32 s16, s3, 1\n" \
"s_add_u32 s16, s16, s7\n" \
"s_lshl_b32 s74, s16, 18\n" \
"s_add_u32 s74, s74, 0x1600000\n" \
"s_add_u32 s84, s10, s74\n" \
"s_addc_u32 s85, s11, 0\n" \
"s_and_b32 s85, s85, 0xffff\n" \
"s_mov_b32 s86, 0x40000\n" \
"s_and_b32 s19, s14, 1\n" \
"s_mul_i32 s19, s19, 2621440\n" \
"s_lshl_b32 s20, s3, 8\n" \
"s_add_u32 s19, s19, s20\n" \
"s_sub_u32 s8, s8, 1024\n" \
"s_subb_u32 s9, s9, 0\n" \
"s_sub_u32 s4, s4, 1024\n" \
"s_subb_u32 s5, s5, 0\n" \
"s_add_u32 s88, s8, s19\n" \
"s_addc_u32 s89, s9, 0\n" \
"s_and_b32 s89, s89, 0xffff\n" \
"s_sub_u32 s90, 7341056, s19\n" \
"s_add_u32 s92, s4, s20\n" \
"s_addc_u32 s93, s5, 0\n" \
"s_and_b32 s93, s93, 0xffff\n" \
"s_sub_u32 s94, 2098176, s20\n" \
"s_mov_b32 s83, 0x20000\n" \
"s_mov_b32 s87, 0x20000\n" \
"s_mov_b32 s91, 0x20000\n" \
"s_mov_b32 s95, 0x20000\n" \
"v_and_b32 v15, 7, v20\n" \
"v_lshlrev_b32 v15, 4, v15\n" \
"v_and_b32 v16, 48, v20\n" \
"v_lshlrev_b32 v16, 1, v16\n" \
"v_xor_b32 v15, v15, v16\n" \
"v_and_b32 v16, 56, v20\n" \
"v_lshl_or_b32 v15, v16, 8, v15\n" \
"s_lshl_b32 s74, s15, 15\n" \
"v_add_u32 v15, s74, v15\n" \
"s_mul_i32 s74, s15, 6144\n" \
"s_add_u32 s74, s74, 0x8000\n" \
"v_add_u32 v19, s74, v14\n" \
"v_mov_b32 v16, 0x7ffffff0\n" \
"v_mov_b32 v18, 0x7ffffff0\n" \
"s_and_b32 s18, s2, 1\n" \
"s_lshr_b32 s21, s3, 1\n" \
"s_lshl_b32 s21, s21, 12\n" \
"s_and_b32 s22, s3, 1\n" \
"s_lshl_b32 s23, s22, 3\n" \
"s_add_u32 s23, s23, 576\n" \
"s_add_u32 s23, s23, s21\n" \
"v_and_b32 v22, 1, v20\n" \
"v_and_b32 v23, 30, v20\n" \
"v_lshlrev_b32 v23, 13, v23\n" \
"v_lshl_or_b32 v23, v22, 11, v23\n" \
"s_cmp_eq_u32 s78, 1\n" \
"s_cselect_b32 s74, 24, 8\n" \
"s_sub_u32 s25, s2, s74\n" \
"s_lshl_b32 s74, s25, 15\n" \
"s_add_u32 s23, s23, s74\n" \
"s_lshl_b32 s25, s25, 14\n" \
"s_lshl_b32 s74, s6, 10\n" \
"s_add_u32 s25, s25, s74\n" \
"s_cmp_eq_u32 s78, 1\n" \
"s_cbranch_scc1 L_sk_%=\n" \
"s_mul_i32 s74, s16, 1179648\n" \
"s_add_u32 s74, s74, 0x400000\n" \
"s_add_u32 s80, s10, s74\n" \
"s_addc_u32 s81, s11, 0\n" \
"s_and_b32 s81, s81, 0xffff\n" \
"s_mov_b32 s82, 917504\n" \
"s_lshr_b32 s17, s2, 1\n" \
"s_mul_i32 s17, s17, 5\n" \
"s_lshl_b32 s24, s18, 1\n" \
"s_add_u32 s24, s24, s17\n" \
"s_add_u32 s17, s17, 4\n" \
"s_lshl_b32 s29, s17, 14\n" \
"s_lshl_b32 s74, s18, 10\n" \
"s_add_u32 s29, s29, s74\n" \
"s_lshl_b32 s74, s6, 13\n" \
"s_add_u32 s29, s29, s74\n" \
"s_lshl_b32 s74, s15, 11\n" \
"s_add_u32 s74, s74, 0x8000\n" \
"v_lshl_add_u32 v18, v20, 3, s74\n" \
"s_lshl_b32 s74, s2, 1\n" \
"s_add_u32 s74, s74, 8\n" \
"s_cmp_eq_u32 s78, 2\n" \
"s_cselect_b32 s24, s74, s24\n" \
"s_cselect_b32 s29, s25, s29\n" \
"s_add_u32 s24, s24, s6\n" \
"s_lshl_b32 s28, s24, 14\n" \
"s_cmp_lg_u32 s15, 0\n" \
"s_cbranch_scc1 L_sdone_%=\n" \
"s_and_b32 s26, s2, 7\n" \
"s_lshr_b32 s17, s2, 3\n" \
"s_and_b32 s74, s2, 3\n" \
"s_add_u32 s74, s74, 8\n" \
"s_bfe_u32 s76, s2, 0x10002\n" \
"s_cmp_eq_u32 s78, 2\n" \
"s_cselect_b32 s26, s74, s26\n" \
"s_cselect_b32 s17, s76, s17\n" \
"s_lshl_b32 s26, s26, 3\n" \
"s_add_u32 s26, s26, 256\n" \
"s_lshl_b32 s74, s22, 15\n" \
"s_add_u32 s26, s26, s74\n" \
"s_add_u32 s26, s26, s21\n" \
"s_lshl_b32 s17, s17, 11\n" \
"s_add_u32 s26, s26, s17\n" \
"v_lshlrev_b32 v24, 14, v22\n" \
"v_and_b32 v25, 14, v20\n" \
"v_lshl_or_b32 v24, v25, 15, v24\n" \
"s_mov_b64 exec, 0xffff\n" \
"v_add_u32 v16, s26, v24\n" \
"s_cmp_eq_u32 s78, 2\n" \
"s_cselect_b32 exec_hi, 15, 0\n" \
"s_mov_b32 exec_lo, 0\n" \
"v_add_u32 v16, s23, v23\n" \
"s_mov_b64 exec, -1\n" \
"s_branch L_sdone_%=\n" \
"L_sk_%=:\n" \
"s_mul_i32 s74, s3, 196608\n" \
"s_add_u32 s74, s74, 0x1a00000\n" \
"s_add_u32 s80, s10, s74\n" \
"s_addc_u32 s81, s11, 0\n" \
"s_and_b32 s81, s81, 0xffff\n" \
"s_mov_b32 s82, 196608\n" \
"s_sub_u32 s18, s2, 24\n" \
"s_mov_b32 s28, s25\n" \
"s_lshl_b32 s29, s18, 2\n" \
"s_lshl_b32 s74, s6, 1\n" \
"s_add_u32 s29, s29, s74\n" \
"s_add_u32 s29, s29, s7\n" \
"s_mul_i32 s29, s29, 6144\n" \
"s_mov_b32 exec_lo, 0x10001\n" \
"s_mov_b32 exec_hi, 0x10001\n" \
"v_mov_b32 v18, v20\n" \
"s_mov_b64 exec, -1\n" \
"s_cmp_lg_u32 s15, 0\n" \
"s_cbranch_scc1 L_sdone_%=\n" \
"s_lshl_b32 s74, s18, 16\n" \
"s_lshl_b32 s76, s3, 3\n" \
"s_add_u32 s74, s74, s76\n" \
"s_add_u32 s74, s74, 512\n" \
"v_lshlrev_b32 v24, 11, v20\n" \
"s_mov_b64 exec, 0xffffffff\n" \
"v_add_u32 v16, s74, v24\n" \
"s_mov_b32 exec_lo, 0\n" \
"s_mov_b32 exec_hi, 15\n" \
"v_add_u32 v16, s23, v23\n" \
"s_mov_b64 exec, -1\n" \
"L_sdone_%=:\n" \
"s_waitcnt vmcnt(2)\n" \
"s_barrier\n" \
"ds_read_b64_tr_b16 a[128:129], v1 offset:0\n" \
"ds_read_b64_tr_b16 a[130:131], v1 offset:512\n" \
"ds_read_b64_tr_b16 a[132:133], v1 offset:16384\n" \
"ds_read_b64_tr_b16 a[134:135], v1 offset:16896\n" \
"ds_read_b128 a[0:3], v0 offset:0\n" \
"ds_read_b128 a[4:7], v0 offset:1024\n" \
"ds_read_b128 a[8:11], v0 offset:2048\n" \
"ds_read_b128 a[12:15], v0 offset:3072\n" \
"ds_read_b128 a[16:19], v0 offset:4096\n" \
"ds_read_b128 a[20:23], v0 offset:5120\n" \
"ds_read_b128 a[24:27], v0 offset:6144\n" \
"ds_read_b128 a[28:31], v0 offset:7168\n" \
"ds_read_b128 a[32:35], v0 offset:8192\n" \
"ds_read_b128 a[36:39], v0 offset:9216\n" \
"ds_read_b128 a[40:43], v0 offset:10240\n" \
"ds_read_b128 a[44:47], v0 offset:11264\n" \
"ds_read_b128 a[48:51], v0 offset:12288\n" \
"ds_read_b128 a[52:55], v0 offset:13312\n" \
"ds_read_b128 a[56:59], v0 offset:14336\n" \
"ds_read_b128 a[60:63], v0 offset:15360\n" \
"ds_read_b64_tr_b16 a[136:137], v2 offset:0\n" \
"ds_read_b64_tr_b16 a[138:139], v2 offset:512\n" \
"ds_read_b64_tr_b16 a[140:141], v2 offset:16384\n" \
"ds_read_b64_tr_b16 a[142:143], v2 offset:16896\n" \
"s_mov_b32 s73, 7\n" \
"s_cmp_eq_u32 s78, 1\n" \
"s_cbranch_scc1 L_kv_%=\n" \
"L_g1_%=:\n" \
"s_waitcnt lgkmcnt(4)\n" \
"v_mfma_f32_16x16x32_bf16 v[32:35], a[128:131], a[0:3], v[32:35]\n" \
"ds_read_b64_tr_b16 a[144:145], v3 offset:0\n" \
"v_mfma_f32_16x16x32_bf16 v[32:35], a[132:135], a[4:7], v[32:35]\n" \
"buffer_load_dwordx4 v8, s[44:47], s65 offen offset:2048 lds\n" \
"v_mfma_f32_16x16x32_bf16 v[36:39], a[132:135], a[12:15], v[36:39]\n" \
"ds_read_b64_tr_b16 a[146:147], v3 offset:512\n" \
"v_mfma_f32_16x16x32_bf16 v[36:39], a[128:131], a[8:11], v[36:39]\n" \
"v_mfma_f32_16x16x32_bf16 v[40:43], a[128:131], a[16:19], v[40:43]\n" \
"ds_read_b64_tr_b16 a[148:149], v3 offset:16384\n" \
"v_mfma_f32_16x16x32_bf16 v[40:43], a[132:135], a[20:23], v[40:43]\n" \
"buffer_load_dwordx4 v10, s[44:47], s66 offen offset:3072 lds\n" \
"v_mfma_f32_16x16x32_bf16 v[44:47], a[132:135], a[28:31], v[44:47]\n" \
"ds_read_b64_tr_b16 a[150:151], v3 offset:16896\n" \
"v_mfma_f32_16x16x32_bf16 v[44:47], a[128:131], a[24:27], v[44:47]\n" \
"v_mfma_f32_16x16x32_bf16 v[48:51], a[128:131], a[32:35], v[48:51]\n" \
"s_add_u32 m0, s71, 36864\n" \
"v_mfma_f32_16x16x32_bf16 v[48:51], a[132:135], a[36:39], v[48:51]\n" \
"buffer_load_dwordx4 v8, s[48:51], s65 offen offset:0 lds\n" \
"v_mfma_f32_16x16x32_bf16 v[52:55], a[132:135], a[44:47], v[52:55]\n" \
"v_mfma_f32_16x16x32_bf16 v[52:55], a[128:131], a[40:43], v[52:55]\n" \
"v_mfma_f32_16x16x32_bf16 v[56:59], a[128:131], a[48:51], v[56:59]\n" \
"v_mfma_f32_16x16x32_bf16 v[56:59], a[132:135], a[52:55], v[56:59]\n" \
"buffer_load_dwordx4 v10, s[48:51], s66 offen offset:1024 lds\n" \
"v_mfma_f32_16x16x32_bf16 v[60:63], a[132:135], a[60:63], v[60:63]\n" \
"v_mfma_f32_16x16x32_bf16 v[60:63], a[128:131], a[56:59], v[60:63]\n" \
"s_waitcnt lgkmcnt(4)\n" \
"v_mfma_f32_16x16x32_bf16 v[92:95], a[136:139], a[56:59], v[92:95]\n" \
"ds_read_b64_tr_b16 a[152:153], v4 offset:0\n" \
"v_mfma_f32_16x16x32_bf16 v[92:95], a[140:143], a[60:63], v[92:95]\n" \
"buffer_load_dwordx4 v8, s[52:55], s65 offen offset:2048 lds\n" \
"v_mfma_f32_16x16x32_bf16 v[88:91], a[140:143], a[52:55], v[88:91]\n" \
"ds_read_b64_tr_b16 a[154:155], v4 offset:512\n" \
"v_mfma_f32_16x16x32_bf16 v[88:91], a[136:139], a[48:51], v[88:91]\n" \
"v_mfma_f32_16x16x32_bf16 v[84:87], a[136:139], a[40:43], v[84:87]\n" \
"ds_read_b64_tr_b16 a[156:157], v4 offset:16384\n" \
"v_mfma_f32_16x16x32_bf16 v[84:87], a[140:143], a[44:47], v[84:87]\n" \
"buffer_load_dwordx4 v10, s[52:55], s66 offen offset:3072 lds\n" \
"v_mfma_f32_16x16x32_bf16 v[80:83], a[140:143], a[36:39], v[80:83]\n" \
"ds_read_b64_tr_b16 a[158:159], v4 offset:16896\n" \
"v_mfma_f32_16x16x32_bf16 v[80:83], a[136:139], a[32:35], v[80:83]\n" \
"v_mfma_f32_16x16x32_bf16 v[76:79], a[136:139], a[24:27], v[76:79]\n" \
"s_add_u32 m0, s72, 8192\n" \
"v_mfma_f32_16x16x32_bf16 v[76:79], a[140:143], a[28:31], v[76:79]\n" \
"buffer_load_dwordx4 v9, s[56:59], s67 offen offset:0 lds\n" \
"v_mfma_f32_16x16x32_bf16 v[72:75], a[140:143], a[20:23], v[72:75]\n" \
"v_mfma_f32_16x16x32_bf16 v[72:75], a[136:139], a[16:19], v[72:75]\n" \
"v_mfma_f32_16x16x32_bf16 v[68:71], a[136:139], a[8:11], v[68:71]\n" \
"v_mfma_f32_16x16x32_bf16 v[68:71], a[140:143], a[12:15], v[68:71]\n" \
"buffer_load_dwordx4 v9, s[60:63], s67 offen offset:1024 lds\n" \
"s_add_u32 s65, s65, s68\n" \
"s_add_u32 s66, s66, s68\n" \
"s_add_u32 s67, s67, s69\n" \
"v_mfma_f32_16x16x32_bf16 v[64:67], a[140:143], a[4:7], v[64:67]\n" \
"v_mfma_f32_16x16x32_bf16 v[64:67], a[136:139], a[0:3], v[64:67]\n" \
"s_waitcnt lgkmcnt(4)\n" \
"v_mfma_f32_16x16x32_bf16 v[96:99], a[144:147], a[0:3], v[96:99]\n" \
"ds_read_b64_tr_b16 a[128:129], v1 offset:2048\n" \
"s_add_u32 m0, s70, 32768\n" \
"v_mfma_f32_16x16x32_bf16 v[96:99], a[148:151], a[4:7], v[96:99]\n" \
"buffer_load_dwordx4 v7, s[36:39], s64 offen offset:0 sc1 lds\n" \
"v_mfma_f32_16x16x32_bf16 v[100:103], a[148:151], a[12:15], v[100:103]\n" \
"ds_read_b64_tr_b16 a[130:131], v1 offset:2560\n" \
"v_mfma_f32_16x16x32_bf16 v[100:103], a[144:147], a[8:11], v[100:103]\n" \
"v_mfma_f32_16x16x32_bf16 v[104:107], a[144:147], a[16:19], v[104:107]\n" \
"ds_read_b64_tr_b16 a[132:133], v1 offset:18432\n" \
"v_mfma_f32_16x16x32_bf16 v[104:107], a[148:151], a[20:23], v[104:107]\n" \
"buffer_load_dwordx4 v7, s[36:39], s64 offen offset:1024 sc1 lds\n" \
"v_mfma_f32_16x16x32_bf16 v[108:111], a[148:151], a[28:31], v[108:111]\n" \
"ds_read_b64_tr_b16 a[134:135], v1 offset:18944\n" \
"v_mfma_f32_16x16x32_bf16 v[108:111], a[144:147], a[24:27], v[108:111]\n" \
"v_mfma_f32_16x16x32_bf16 v[112:115], a[144:147], a[32:35], v[112:115]\n" \
"v_mfma_f32_16x16x32_bf16 v[112:115], a[148:151], a[36:39], v[112:115]\n" \
"buffer_load_dwordx4 v7, s[36:39], s64 offen offset:2048 sc1 lds\n" \
"v_mfma_f32_16x16x32_bf16 v[116:119], a[148:151], a[44:47], v[116:119]\n" \
"v_mfma_f32_16x16x32_bf16 v[116:119], a[144:147], a[40:43], v[116:119]\n" \
"v_mfma_f32_16x16x32_bf16 v[120:123], a[144:147], a[48:51], v[120:123]\n" \
"v_mfma_f32_16x16x32_bf16 v[120:123], a[148:151], a[52:55], v[120:123]\n" \
"buffer_load_dwordx4 v7, s[36:39], s64 offen offset:3072 sc1 lds\n" \
"v_mfma_f32_16x16x32_bf16 v[124:127], a[148:151], a[60:63], v[124:127]\n" \
"v_mfma_f32_16x16x32_bf16 v[124:127], a[144:147], a[56:59], v[124:127]\n" \
"s_waitcnt lgkmcnt(4)\n" \
"v_mfma_f32_16x16x32_bf16 v[156:159], a[152:155], a[56:59], v[156:159]\n" \
"ds_read_b64_tr_b16 a[136:137], v2 offset:2048\n" \
"s_add_u32 m0, s70, 36864\n" \
"s_add_u32 s74, s64, 4096\n" \
"v_mfma_f32_16x16x32_bf16 v[156:159], a[156:159], a[60:63], v[156:159]\n" \
"buffer_load_dwordx4 v7, s[36:39], s74 offen offset:0 sc1 lds\n" \
"v_mfma_f32_16x16x32_bf16 v[152:155], a[156:159], a[52:55], v[152:155]\n" \
"ds_read_b64_tr_b16 a[138:139], v2 offset:2560\n" \
"v_mfma_f32_16x16x32_bf16 v[152:155], a[152:155], a[48:51], v[152:155]\n" \
"v_mfma_f32_16x16x32_bf16 v[148:151], a[152:155], a[40:43], v[148:151]\n" \
"ds_read_b64_tr_b16 a[140:141], v2 offset:18432\n" \
"v_mfma_f32_16x16x32_bf16 v[148:151], a[156:159], a[44:47], v[148:151]\n" \
"buffer_load_dwordx4 v7, s[36:39], s74 offen offset:1024 sc1 lds\n" \
"v_mfma_f32_16x16x32_bf16 v[144:147], a[156:159], a[36:39], v[144:147]\n" \
"ds_read_b64_tr_b16 a[142:143], v2 offset:18944\n" \
"v_mfma_f32_16x16x32_bf16 v[144:147], a[152:155], a[32:35], v[144:147]\n" \
"v_mfma_f32_16x16x32_bf16 v[140:143], a[152:155], a[24:27], v[140:143]\n" \
"v_mfma_f32_16x16x32_bf16 v[140:143], a[156:159], a[28:31], v[140:143]\n" \
"buffer_load_dwordx4 v7, s[36:39], s74 offen offset:2048 sc1 lds\n" \
"v_mfma_f32_16x16x32_bf16 v[136:139], a[156:159], a[20:23], v[136:139]\n" \
"v_mfma_f32_16x16x32_bf16 v[136:139], a[152:155], a[16:19], v[136:139]\n" \
"v_mfma_f32_16x16x32_bf16 v[132:135], a[152:155], a[8:11], v[132:135]\n" \
"v_mfma_f32_16x16x32_bf16 v[132:135], a[156:159], a[12:15], v[132:135]\n" \
"buffer_load_dwordx4 v7, s[36:39], s74 offen offset:3072 sc1 lds\n" \
"s_add_u32 s64, s64, 32768\n" \
"v_mfma_f32_16x16x32_bf16 v[128:131], a[156:159], a[4:7], v[128:131]\n" \
"v_mfma_f32_16x16x32_bf16 v[128:131], a[152:155], a[0:3], v[128:131]\n" \
"s_waitcnt lgkmcnt(4)\n" \
"v_mfma_f32_16x16x32_bf16 v[160:163], a[128:131], a[0:3], v[160:163]\n" \
"ds_read_b64_tr_b16 a[144:145], v3 offset:2048\n" \
"s_mov_b32 m0, s77\n" \
"s_sub_i32 s75, s75, 1\n" \
"v_mfma_f32_16x16x32_bf16 v[160:163], a[132:135], a[4:7], v[160:163]\n" \
"s_cmp_lt_u32 s75, s27\n" \
"s_cselect_b32 s76, 1, 0\n" \
"v_mfma_f32_16x16x32_bf16 v[164:167], a[132:135], a[12:15], v[164:167]\n" \
"ds_read_b64_tr_b16 a[146:147], v3 offset:2560\n" \
"global_load_lds_dword v[12:13], off\n" \
"v_mad_u64_u32 v[12:13], vcc, v11, s76, v[12:13]\n" \
"v_mfma_f32_16x16x32_bf16 v[164:167], a[128:131], a[8:11], v[164:167]\n" \
"v_mfma_f32_16x16x32_bf16 v[168:171], a[128:131], a[16:19], v[168:171]\n" \
"ds_read_b64_tr_b16 a[148:149], v3 offset:18432\n" \
"v_mfma_f32_16x16x32_bf16 v[168:171], a[132:135], a[20:23], v[168:171]\n" \
"v_mfma_f32_16x16x32_bf16 v[172:175], a[132:135], a[28:31], v[172:175]\n" \
"ds_read_b64_tr_b16 a[150:151], v3 offset:18944\n" \
"v_mfma_f32_16x16x32_bf16 v[172:175], a[128:131], a[24:27], v[172:175]\n" \
"v_mfma_f32_16x16x32_bf16 v[176:179], a[128:131], a[32:35], v[176:179]\n" \
"v_mfma_f32_16x16x32_bf16 v[176:179], a[132:135], a[36:39], v[176:179]\n" \
"v_mfma_f32_16x16x32_bf16 v[180:183], a[132:135], a[44:47], v[180:183]\n" \
"v_mfma_f32_16x16x32_bf16 v[180:183], a[128:131], a[40:43], v[180:183]\n" \
"v_mfma_f32_16x16x32_bf16 v[184:187], a[128:131], a[48:51], v[184:187]\n" \
"v_mfma_f32_16x16x32_bf16 v[184:187], a[132:135], a[52:55], v[184:187]\n" \
"v_mfma_f32_16x16x32_bf16 v[188:191], a[132:135], a[60:63], v[188:191]\n" \
"v_mfma_f32_16x16x32_bf16 v[188:191], a[128:131], a[56:59], v[188:191]\n" \
"s_waitcnt lgkmcnt(4)\n" \
"v_mfma_f32_16x16x32_bf16 a[188:191], a[136:139], a[56:59], a[188:191]\n" \
"ds_read_b64_tr_b16 a[152:153], v4 offset:2048\n" \
"v_mfma_f32_16x16x32_bf16 a[188:191], a[140:143], a[60:63], a[188:191]\n" \
"v_mfma_f32_16x16x32_bf16 a[184:187], a[140:143], a[52:55], a[184:187]\n" \
"ds_read_b64_tr_b16 a[154:155], v4 offset:2560\n" \
"v_mfma_f32_16x16x32_bf16 a[184:187], a[136:139], a[48:51], a[184:187]\n" \
"v_mfma_f32_16x16x32_bf16 a[180:183], a[136:139], a[40:43], a[180:183]\n" \
"ds_read_b64_tr_b16 a[156:157], v4 offset:18432\n" \
"v_mfma_f32_16x16x32_bf16 a[180:183], a[140:143], a[44:47], a[180:183]\n" \
"v_mfma_f32_16x16x32_bf16 a[176:179], a[140:143], a[36:39], a[176:179]\n" \
"ds_read_b64_tr_b16 a[158:159], v4 offset:18944\n" \
"v_mfma_f32_16x16x32_bf16 a[176:179], a[136:139], a[32:35], a[176:179]\n" \
"v_mfma_f32_16x16x32_bf16 a[172:175], a[136:139], a[24:27], a[172:175]\n" \
"v_mfma_f32_16x16x32_bf16 a[172:175], a[140:143], a[28:31], a[172:175]\n" \
"v_mfma_f32_16x16x32_bf16 a[168:171], a[140:143], a[20:23], a[168:171]\n" \
"v_mfma_f32_16x16x32_bf16 a[168:171], a[136:139], a[16:19], a[168:171]\n" \
"v_mfma_f32_16x16x32_bf16 a[164:167], a[136:139], a[8:11], a[164:167]\n" \
"v_mfma_f32_16x16x32_bf16 a[164:167], a[140:143], a[12:15], a[164:167]\n" \
"v_mfma_f32_16x16x32_bf16 a[160:163], a[140:143], a[4:7], a[160:163]\n" \
"v_mfma_f32_16x16x32_bf16 a[160:163], a[136:139], a[0:3], a[160:163]\n" \
"s_waitcnt lgkmcnt(4)\n" \
"v_mfma_f32_16x16x32_bf16 a[192:195], a[144:147], a[0:3], a[192:195]\n" \
"ds_read_b64_tr_b16 a[128:129], v5 offset:0\n" \
"v_mfma_f32_16x16x32_bf16 a[192:195], a[148:151], a[4:7], a[192:195]\n" \
"v_mfma_f32_16x16x32_bf16 a[196:199], a[148:151], a[12:15], a[196:199]\n" \
"ds_read_b64_tr_b16 a[130:131], v5 offset:256\n" \
"v_mfma_f32_16x16x32_bf16 a[196:199], a[144:147], a[8:11], a[196:199]\n" \
"v_mfma_f32_16x16x32_bf16 a[200:203], a[144:147], a[16:19], a[200:203]\n" \
"ds_read_b64_tr_b16 a[132:133], v5 offset:4096\n" \
"v_mfma_f32_16x16x32_bf16 a[200:203], a[148:151], a[20:23], a[200:203]\n" \
"v_mfma_f32_16x16x32_bf16 a[204:207], a[148:151], a[28:31], a[204:207]\n" \
"ds_read_b64_tr_b16 a[134:135], v5 offset:4352\n" \
"v_mfma_f32_16x16x32_bf16 a[204:207], a[144:147], a[24:27], a[204:207]\n" \
"v_mfma_f32_16x16x32_bf16 a[208:211], a[144:147], a[32:35], a[208:211]\n" \
"v_mfma_f32_16x16x32_bf16 a[208:211], a[148:151], a[36:39], a[208:211]\n" \
"v_mfma_f32_16x16x32_bf16 a[212:215], a[148:151], a[44:47], a[212:215]\n" \
"v_mfma_f32_16x16x32_bf16 a[212:215], a[144:147], a[40:43], a[212:215]\n" \
"v_mfma_f32_16x16x32_bf16 a[216:219], a[144:147], a[48:51], a[216:219]\n" \
"v_mfma_f32_16x16x32_bf16 a[216:219], a[148:151], a[52:55], a[216:219]\n" \
"v_mfma_f32_16x16x32_bf16 a[220:223], a[148:151], a[60:63], a[220:223]\n" \
"v_mfma_f32_16x16x32_bf16 a[220:223], a[144:147], a[56:59], a[220:223]\n" \
"s_waitcnt lgkmcnt(4)\n" \
"v_mfma_f32_16x16x32_bf16 a[252:255], a[152:155], a[56:59], a[252:255]\n" \
"ds_read_b64_tr_b16 a[136:137], v6 offset:0\n" \
"v_mfma_f32_16x16x32_bf16 a[252:255], a[156:159], a[60:63], a[252:255]\n" \
"v_mfma_f32_16x16x32_bf16 a[248:251], a[156:159], a[52:55], a[248:251]\n" \
"ds_read_b64_tr_b16 a[138:139], v6 offset:256\n" \
"v_mfma_f32_16x16x32_bf16 a[248:251], a[152:155], a[48:51], a[248:251]\n" \
"v_mfma_f32_16x16x32_bf16 a[244:247], a[152:155], a[40:43], a[244:247]\n" \
"ds_read_b64_tr_b16 a[140:141], v6 offset:4096\n" \
"v_mfma_f32_16x16x32_bf16 a[244:247], a[156:159], a[44:47], a[244:247]\n" \
"v_mfma_f32_16x16x32_bf16 a[240:243], a[156:159], a[36:39], a[240:243]\n" \
"ds_read_b64_tr_b16 a[142:143], v6 offset:4352\n" \
"v_mfma_f32_16x16x32_bf16 a[240:243], a[152:155], a[32:35], a[240:243]\n" \
"v_mfma_f32_16x16x32_bf16 a[236:239], a[152:155], a[24:27], a[236:239]\n" \
"v_mfma_f32_16x16x32_bf16 a[236:239], a[156:159], a[28:31], a[236:239]\n" \
"v_mfma_f32_16x16x32_bf16 a[232:235], a[156:159], a[20:23], a[232:235]\n" \
"v_mfma_f32_16x16x32_bf16 a[232:235], a[152:155], a[16:19], a[232:235]\n" \
"v_mfma_f32_16x16x32_bf16 a[228:231], a[152:155], a[8:11], a[228:231]\n" \
"v_mfma_f32_16x16x32_bf16 a[228:231], a[156:159], a[12:15], a[228:231]\n" \
"v_mfma_f32_16x16x32_bf16 a[224:227], a[156:159], a[4:7], a[224:227]\n" \
"v_mfma_f32_16x16x32_bf16 a[224:227], a[152:155], a[0:3], a[224:227]\n" \
"s_waitcnt vmcnt(1) lgkmcnt(0)\n" \
"s_barrier\n" \
"v_mfma_f32_16x16x32_bf16 v[192:195], a[128:131], a[0:3], v[192:195]\n" \
"ds_read_b64_tr_b16 a[144:145], v1 offset:32768\n" \
"v_mfma_f32_16x16x32_bf16 v[192:195], a[132:135], a[4:7], v[192:195]\n" \
"ds_read_b64_tr_b16 a[146:147], v1 offset:33280\n" \
"v_mfma_f32_16x16x32_bf16 v[196:199], a[132:135], a[12:15], v[196:199]\n" \
"ds_read_b64_tr_b16 a[148:149], v1 offset:49152\n" \
"v_mfma_f32_16x16x32_bf16 v[196:199], a[128:131], a[8:11], v[196:199]\n" \
"ds_read_b64_tr_b16 a[150:151], v1 offset:49664\n" \
"v_mfma_f32_16x16x32_bf16 v[200:203], a[128:131], a[16:19], v[200:203]\n" \
"ds_read_b128 a[64:67], v0 offset:32768\n" \
"v_mfma_f32_16x16x32_bf16 v[200:203], a[132:135], a[20:23], v[200:203]\n" \
"ds_read_b128 a[68:71], v0 offset:33792\n" \
"v_mfma_f32_16x16x32_bf16 v[204:207], a[132:135], a[28:31], v[204:207]\n" \
"ds_read_b128 a[72:75], v0 offset:34816\n" \
"v_mfma_f32_16x16x32_bf16 v[204:207], a[128:131], a[24:27], v[204:207]\n" \
"ds_read_b128 a[76:79], v0 offset:35840\n" \
"v_mfma_f32_16x16x32_bf16 v[208:211], a[128:131], a[32:35], v[208:211]\n" \
"ds_read_b128 a[80:83], v0 offset:36864\n" \
"v_mfma_f32_16x16x32_bf16 v[208:211], a[132:135], a[36:39], v[208:211]\n" \
"ds_read_b128 a[84:87], v0 offset:37888\n" \
"v_mfma_f32_16x16x32_bf16 v[212:215], a[132:135], a[44:47], v[212:215]\n" \
"ds_read_b128 a[88:91], v0 offset:38912\n" \
"v_mfma_f32_16x16x32_bf16 v[212:215], a[128:131], a[40:43], v[212:215]\n" \
"ds_read_b128 a[92:95], v0 offset:39936\n" \
"v_mfma_f32_16x16x32_bf16 v[216:219], a[128:131], a[48:51], v[216:219]\n" \
"ds_read_b128 a[96:99], v0 offset:40960\n" \
"v_mfma_f32_16x16x32_bf16 v[216:219], a[132:135], a[52:55], v[216:219]\n" \
"ds_read_b128 a[100:103], v0 offset:41984\n" \
"v_mfma_f32_16x16x32_bf16 v[220:223], a[132:135], a[60:63], v[220:223]\n" \
"ds_read_b128 a[104:107], v0 offset:43008\n" \
"v_mfma_f32_16x16x32_bf16 v[220:223], a[128:131], a[56:59], v[220:223]\n" \
"ds_read_b128 a[108:111], v0 offset:44032\n" \
"v_mfma_f32_16x16x32_bf16 v[252:255], a[136:139], a[56:59], v[252:255]\n" \
"ds_read_b128 a[112:115], v0 offset:45056\n" \
"v_mfma_f32_16x16x32_bf16 v[252:255], a[140:143], a[60:63], v[252:255]\n" \
"ds_read_b128 a[116:119], v0 offset:46080\n" \
"v_mfma_f32_16x16x32_bf16 v[248:251], a[140:143], a[52:55], v[248:251]\n" \
"ds_read_b128 a[120:123], v0 offset:47104\n" \
"v_mfma_f32_16x16x32_bf16 v[248:251], a[136:139], a[48:51], v[248:251]\n" \
"ds_read_b128 a[124:127], v0 offset:48128\n" \
"v_mfma_f32_16x16x32_bf16 v[244:247], a[136:139], a[40:43], v[244:247]\n" \
"ds_read_b64_tr_b16 a[152:153], v2 offset:32768\n" \
"v_mfma_f32_16x16x32_bf16 v[244:247], a[140:143], a[44:47], v[244:247]\n" \
"ds_read_b64_tr_b16 a[154:155], v2 offset:33280\n" \
"v_mfma_f32_16x16x32_bf16 v[240:243], a[140:143], a[36:39], v[240:243]\n" \
"ds_read_b64_tr_b16 a[156:157], v2 offset:49152\n" \
"v_mfma_f32_16x16x32_bf16 v[240:243], a[136:139], a[32:35], v[240:243]\n" \
"ds_read_b64_tr_b16 a[158:159], v2 offset:49664\n" \
"v_mfma_f32_16x16x32_bf16 v[236:239], a[136:139], a[24:27], v[236:239]\n" \
"s_add_u32 m0, s71, 0\n" \
"v_mfma_f32_16x16x32_bf16 v[236:239], a[140:143], a[28:31], v[236:239]\n" \
"buffer_load_dwordx4 v8, s[40:43], s65 offen offset:0 lds\n" \
"v_mfma_f32_16x16x32_bf16 v[232:235], a[140:143], a[20:23], v[232:235]\n" \
"v_mfma_f32_16x16x32_bf16 v[232:235], a[136:139], a[16:19], v[232:235]\n" \
"v_mfma_f32_16x16x32_bf16 v[228:231], a[136:139], a[8:11], v[228:231]\n" \
"v_mfma_f32_16x16x32_bf16 v[228:231], a[140:143], a[12:15], v[228:231]\n" \
"buffer_load_dwordx4 v10, s[40:43], s66 offen offset:1024 lds\n" \
"v_mfma_f32_16x16x32_bf16 v[224:227], a[140:143], a[4:7], v[224:227]\n" \
"v_mfma_f32_16x16x32_bf16 v[224:227], a[136:139], a[0:3], v[224:227]\n" \
"s_waitcnt lgkmcnt(4)\n" \
"v_mfma_f32_16x16x32_bf16 v[32:35], a[144:147], a[64:67], v[32:35]\n" \
"ds_read_b64_tr_b16 a[128:129], v3 offset:32768\n" \
"v_mfma_f32_16x16x32_bf16 v[32:35], a[148:151], a[68:71], v[32:35]\n" \
"buffer_load_dwordx4 v8, s[44:47], s65 offen offset:2048 lds\n" \
"v_mfma_f32_16x16x32_bf16 v[36:39], a[148:151], a[76:79], v[36:39]\n" \
"ds_read_b64_tr_b16 a[130:131], v3 offset:33280\n" \
"v_mfma_f32_16x16x32_bf16 v[36:39], a[144:147], a[72:75], v[36:39]\n" \
"v_mfma_f32_16x16x32_bf16 v[40:43], a[144:147], a[80:83], v[40:43]\n" \
"ds_read_b64_tr_b16 a[132:133], v3 offset:49152\n" \
"v_mfma_f32_16x16x32_bf16 v[40:43], a[148:151], a[84:87], v[40:43]\n" \
"buffer_load_dwordx4 v10, s[44:47], s66 offen offset:3072 lds\n" \
"v_mfma_f32_16x16x32_bf16 v[44:47], a[148:151], a[92:95], v[44:47]\n" \
"ds_read_b64_tr_b16 a[134:135], v3 offset:49664\n" \
"v_mfma_f32_16x16x32_bf16 v[44:47], a[144:147], a[88:91], v[44:47]\n" \
"v_mfma_f32_16x16x32_bf16 v[48:51], a[144:147], a[96:99], v[48:51]\n" \
"s_add_u32 m0, s71, 4096\n" \
"v_mfma_f32_16x16x32_bf16 v[48:51], a[148:151], a[100:103], v[48:51]\n" \
"buffer_load_dwordx4 v8, s[48:51], s65 offen offset:0 lds\n" \
"v_mfma_f32_16x16x32_bf16 v[52:55], a[148:151], a[108:111], v[52:55]\n" \
"v_mfma_f32_16x16x32_bf16 v[52:55], a[144:147], a[104:107], v[52:55]\n" \
"v_mfma_f32_16x16x32_bf16 v[56:59], a[144:147], a[112:115], v[56:59]\n" \
"v_mfma_f32_16x16x32_bf16 v[56:59], a[148:151], a[116:119], v[56:59]\n" \
"buffer_load_dwordx4 v10, s[48:51], s66 offen offset:1024 lds\n" \
"v_mfma_f32_16x16x32_bf16 v[60:63], a[148:151], a[124:127], v[60:63]\n" \
"v_mfma_f32_16x16x32_bf16 v[60:63], a[144:147], a[120:123], v[60:63]\n" \
"s_waitcnt lgkmcnt(4)\n" \
"v_mfma_f32_16x16x32_bf16 v[92:95], a[152:155], a[120:123], v[92:95]\n" \
"ds_read_b64_tr_b16 a[136:137], v4 offset:32768\n" \
"v_mfma_f32_16x16x32_bf16 v[92:95], a[156:159], a[124:127], v[92:95]\n" \
"buffer_load_dwordx4 v8, s[52:55], s65 offen offset:2048 lds\n" \
"v_mfma_f32_16x16x32_bf16 v[88:91], a[156:159], a[116:119], v[88:91]\n" \
"ds_read_b64_tr_b16 a[138:139], v4 offset:33280\n" \
"v_mfma_f32_16x16x32_bf16 v[88:91], a[152:155], a[112:115], v[88:91]\n" \
"v_mfma_f32_16x16x32_bf16 v[84:87], a[152:155], a[104:107], v[84:87]\n" \
"ds_read_b64_tr_b16 a[140:141], v4 offset:49152\n" \
"v_mfma_f32_16x16x32_bf16 v[84:87], a[156:159], a[108:111], v[84:87]\n" \
"buffer_load_dwordx4 v10, s[52:55], s66 offen offset:3072 lds\n" \
"v_mfma_f32_16x16x32_bf16 v[80:83], a[156:159], a[100:103], v[80:83]\n" \
"ds_read_b64_tr_b16 a[142:143], v4 offset:49664\n" \
"v_mfma_f32_16x16x32_bf16 v[80:83], a[152:155], a[96:99], v[80:83]\n" \
"v_mfma_f32_16x16x32_bf16 v[76:79], a[152:155], a[88:91], v[76:79]\n" \
"s_add_u32 m0, s72, 0\n" \
"v_mfma_f32_16x16x32_bf16 v[76:79], a[156:159], a[92:95], v[76:79]\n" \
"buffer_load_dwordx4 v9, s[56:59], s67 offen offset:0 lds\n" \
"v_mfma_f32_16x16x32_bf16 v[72:75], a[156:159], a[84:87], v[72:75]\n" \
"v_mfma_f32_16x16x32_bf16 v[72:75], a[152:155], a[80:83], v[72:75]\n" \
"v_mfma_f32_16x16x32_bf16 v[68:71], a[152:155], a[72:75], v[68:71]\n" \
"v_mfma_f32_16x16x32_bf16 v[68:71], a[156:159], a[76:79], v[68:71]\n" \
"buffer_load_dwordx4 v9, s[60:63], s67 offen offset:1024 lds\n" \
"s_add_u32 s65, s65, s68\n" \
"s_add_u32 s66, s66, s68\n" \
"s_add_u32 s67, s67, s69\n" \
"v_mfma_f32_16x16x32_bf16 v[64:67], a[156:159], a[68:71], v[64:67]\n" \
"v_mfma_f32_16x16x32_bf16 v[64:67], a[152:155], a[64:67], v[64:67]\n" \
"s_waitcnt lgkmcnt(4)\n" \
"v_mfma_f32_16x16x32_bf16 v[96:99], a[128:131], a[64:67], v[96:99]\n" \
"ds_read_b64_tr_b16 a[144:145], v1 offset:34816\n" \
"s_add_u32 m0, s70, 0\n" \
"v_mfma_f32_16x16x32_bf16 v[96:99], a[132:135], a[68:71], v[96:99]\n" \
"buffer_load_dwordx4 v7, s[36:39], s64 offen offset:0 sc1 lds\n" \
"v_mfma_f32_16x16x32_bf16 v[100:103], a[132:135], a[76:79], v[100:103]\n" \
"ds_read_b64_tr_b16 a[146:147], v1 offset:35328\n" \
"v_mfma_f32_16x16x32_bf16 v[100:103], a[128:131], a[72:75], v[100:103]\n" \
"v_mfma_f32_16x16x32_bf16 v[104:107], a[128:131], a[80:83], v[104:107]\n" \
"ds_read_b64_tr_b16 a[148:149], v1 offset:51200\n" \
"v_mfma_f32_16x16x32_bf16 v[104:107], a[132:135], a[84:87], v[104:107]\n" \
"buffer_load_dwordx4 v7, s[36:39], s64 offen offset:1024 sc1 lds\n" \
"v_mfma_f32_16x16x32_bf16 v[108:111], a[132:135], a[92:95], v[108:111]\n" \
"ds_read_b64_tr_b16 a[150:151], v1 offset:51712\n" \
"v_mfma_f32_16x16x32_bf16 v[108:111], a[128:131], a[88:91], v[108:111]\n" \
"v_mfma_f32_16x16x32_bf16 v[112:115], a[128:131], a[96:99], v[112:115]\n" \
"v_mfma_f32_16x16x32_bf16 v[112:115], a[132:135], a[100:103], v[112:115]\n" \
"buffer_load_dwordx4 v7, s[36:39], s64 offen offset:2048 sc1 lds\n" \
"v_mfma_f32_16x16x32_bf16 v[116:119], a[132:135], a[108:111], v[116:119]\n" \
"v_mfma_f32_16x16x32_bf16 v[116:119], a[128:131], a[104:107], v[116:119]\n" \
"v_mfma_f32_16x16x32_bf16 v[120:123], a[128:131], a[112:115], v[120:123]\n" \
"v_mfma_f32_16x16x32_bf16 v[120:123], a[132:135], a[116:119], v[120:123]\n" \
"buffer_load_dwordx4 v7, s[36:39], s64 offen offset:3072 sc1 lds\n" \
"v_mfma_f32_16x16x32_bf16 v[124:127], a[132:135], a[124:127], v[124:127]\n" \
"v_mfma_f32_16x16x32_bf16 v[124:127], a[128:131], a[120:123], v[124:127]\n" \
"s_waitcnt lgkmcnt(4)\n" \
"v_mfma_f32_16x16x32_bf16 v[156:159], a[136:139], a[120:123], v[156:159]\n" \
"ds_read_b64_tr_b16 a[152:153], v2 offset:34816\n" \
"s_add_u32 m0, s70, 4096\n" \
"s_add_u32 s74, s64, 4096\n" \
"v_mfma_f32_16x16x32_bf16 v[156:159], a[140:143], a[124:127], v[156:159]\n" \
"buffer_load_dwordx4 v7, s[36:39], s74 offen offset:0 sc1 lds\n" \
"v_mfma_f32_16x16x32_bf16 v[152:155], a[140:143], a[116:119], v[152:155]\n" \
"ds_read_b64_tr_b16 a[154:155], v2 offset:35328\n" \
"v_mfma_f32_16x16x32_bf16 v[152:155], a[136:139], a[112:115], v[152:155]\n" \
"v_mfma_f32_16x16x32_bf16 v[148:151], a[136:139], a[104:107], v[148:151]\n" \
"ds_read_b64_tr_b16 a[156:157], v2 offset:51200\n" \
"v_mfma_f32_16x16x32_bf16 v[148:151], a[140:143], a[108:111], v[148:151]\n" \
"buffer_load_dwordx4 v7, s[36:39], s74 offen offset:1024 sc1 lds\n" \
"v_mfma_f32_16x16x32_bf16 v[144:147], a[140:143], a[100:103], v[144:147]\n" \
"ds_read_b64_tr_b16 a[158:159], v2 offset:51712\n" \
"v_mfma_f32_16x16x32_bf16 v[144:147], a[136:139], a[96:99], v[144:147]\n" \
"v_mfma_f32_16x16x32_bf16 v[140:143], a[136:139], a[88:91], v[140:143]\n" \
"v_mfma_f32_16x16x32_bf16 v[140:143], a[140:143], a[92:95], v[140:143]\n" \
"buffer_load_dwordx4 v7, s[36:39], s74 offen offset:2048 sc1 lds\n" \
"v_mfma_f32_16x16x32_bf16 v[136:139], a[140:143], a[84:87], v[136:139]\n" \
"v_mfma_f32_16x16x32_bf16 v[136:139], a[136:139], a[80:83], v[136:139]\n" \
"v_mfma_f32_16x16x32_bf16 v[132:135], a[136:139], a[72:75], v[132:135]\n" \
"v_mfma_f32_16x16x32_bf16 v[132:135], a[140:143], a[76:79], v[132:135]\n" \
"buffer_load_dwordx4 v7, s[36:39], s74 offen offset:3072 sc1 lds\n" \
"s_add_u32 s64, s64, 32768\n" \
"v_mfma_f32_16x16x32_bf16 v[128:131], a[140:143], a[68:71], v[128:131]\n" \
"v_mfma_f32_16x16x32_bf16 v[128:131], a[136:139], a[64:67], v[128:131]\n" \
"s_waitcnt lgkmcnt(4)\n" \
"v_mfma_f32_16x16x32_bf16 v[160:163], a[144:147], a[64:67], v[160:163]\n" \
"ds_read_b64_tr_b16 a[128:129], v3 offset:34816\n" \
"s_mov_b32 m0, s77\n" \
"s_sub_i32 s75, s75, 1\n" \
"v_mfma_f32_16x16x32_bf16 v[160:163], a[148:151], a[68:71], v[160:163]\n" \
"s_cmp_lt_u32 s75, s27\n" \
"s_cselect_b32 s76, 1, 0\n" \
"v_mfma_f32_16x16x32_bf16 v[164:167], a[148:151], a[76:79], v[164:167]\n" \
"ds_read_b64_tr_b16 a[130:131], v3 offset:35328\n" \
"global_load_lds_dword v[12:13], off\n" \
"v_mad_u64_u32 v[12:13], vcc, v11, s76, v[12:13]\n" \
"v_mfma_f32_16x16x32_bf16 v[164:167], a[144:147], a[72:75], v[164:167]\n" \
"v_mfma_f32_16x16x32_bf16 v[168:171], a[144:147], a[80:83], v[168:171]\n" \
"ds_read_b64_tr_b16 a[132:133], v3 offset:51200\n" \
"v_mfma_f32_16x16x32_bf16 v[168:171], a[148:151], a[84:87], v[168:171]\n" \
"v_mfma_f32_16x16x32_bf16 v[172:175], a[148:151], a[92:95], v[172:175]\n" \
"ds_read_b64_tr_b16 a[134:135], v3 offset:51712\n" \
"v_mfma_f32_16x16x32_bf16 v[172:175], a[144:147], a[88:91], v[172:175]\n" \
"v_mfma_f32_16x16x32_bf16 v[176:179], a[144:147], a[96:99], v[176:179]\n" \
"v_mfma_f32_16x16x32_bf16 v[176:179], a[148:151], a[100:103], v[176:179]\n" \
"v_mfma_f32_16x16x32_bf16 v[180:183], a[148:151], a[108:111], v[180:183]\n" \
"v_mfma_f32_16x16x32_bf16 v[180:183], a[144:147], a[104:107], v[180:183]\n" \
"v_mfma_f32_16x16x32_bf16 v[184:187], a[144:147], a[112:115], v[184:187]\n" \
"v_mfma_f32_16x16x32_bf16 v[184:187], a[148:151], a[116:119], v[184:187]\n" \
"v_mfma_f32_16x16x32_bf16 v[188:191], a[148:151], a[124:127], v[188:191]\n" \
"v_mfma_f32_16x16x32_bf16 v[188:191], a[144:147], a[120:123], v[188:191]\n" \
"s_waitcnt lgkmcnt(4)\n" \
"v_mfma_f32_16x16x32_bf16 a[188:191], a[152:155], a[120:123], a[188:191]\n" \
"ds_read_b64_tr_b16 a[136:137], v4 offset:34816\n" \
"v_mfma_f32_16x16x32_bf16 a[188:191], a[156:159], a[124:127], a[188:191]\n" \
"v_mfma_f32_16x16x32_bf16 a[184:187], a[156:159], a[116:119], a[184:187]\n" \
"ds_read_b64_tr_b16 a[138:139], v4 offset:35328\n" \
"v_mfma_f32_16x16x32_bf16 a[184:187], a[152:155], a[112:115], a[184:187]\n" \
"v_mfma_f32_16x16x32_bf16 a[180:183], a[152:155], a[104:107], a[180:183]\n" \
"ds_read_b64_tr_b16 a[140:141], v4 offset:51200\n" \
"v_mfma_f32_16x16x32_bf16 a[180:183], a[156:159], a[108:111], a[180:183]\n" \
"v_mfma_f32_16x16x32_bf16 a[176:179], a[156:159], a[100:103], a[176:179]\n" \
"ds_read_b64_tr_b16 a[142:143], v4 offset:51712\n" \
"v_mfma_f32_16x16x32_bf16 a[176:179], a[152:155], a[96:99], a[176:179]\n" \
"v_mfma_f32_16x16x32_bf16 a[172:175], a[152:155], a[88:91], a[172:175]\n" \
"v_mfma_f32_16x16x32_bf16 a[172:175], a[156:159], a[92:95], a[172:175]\n" \
"v_mfma_f32_16x16x32_bf16 a[168:171], a[156:159], a[84:87], a[168:171]\n" \
"v_mfma_f32_16x16x32_bf16 a[168:171], a[152:155], a[80:83], a[168:171]\n" \
"v_mfma_f32_16x16x32_bf16 a[164:167], a[152:155], a[72:75], a[164:167]\n" \
"v_mfma_f32_16x16x32_bf16 a[164:167], a[156:159], a[76:79], a[164:167]\n" \
"v_mfma_f32_16x16x32_bf16 a[160:163], a[156:159], a[68:71], a[160:163]\n" \
"v_mfma_f32_16x16x32_bf16 a[160:163], a[152:155], a[64:67], a[160:163]\n" \
"s_waitcnt lgkmcnt(4)\n" \
"v_mfma_f32_16x16x32_bf16 a[192:195], a[128:131], a[64:67], a[192:195]\n" \
"ds_read_b64_tr_b16 a[144:145], v5 offset:8192\n" \
"v_mfma_f32_16x16x32_bf16 a[192:195], a[132:135], a[68:71], a[192:195]\n" \
"v_mfma_f32_16x16x32_bf16 a[196:199], a[132:135], a[76:79], a[196:199]\n" \
"ds_read_b64_tr_b16 a[146:147], v5 offset:8448\n" \
"v_mfma_f32_16x16x32_bf16 a[196:199], a[128:131], a[72:75], a[196:199]\n" \
"v_mfma_f32_16x16x32_bf16 a[200:203], a[128:131], a[80:83], a[200:203]\n" \
"ds_read_b64_tr_b16 a[148:149], v5 offset:12288\n" \
"v_mfma_f32_16x16x32_bf16 a[200:203], a[132:135], a[84:87], a[200:203]\n" \
"v_mfma_f32_16x16x32_bf16 a[204:207], a[132:135], a[92:95], a[204:207]\n" \
"ds_read_b64_tr_b16 a[150:151], v5 offset:12544\n" \
"v_mfma_f32_16x16x32_bf16 a[204:207], a[128:131], a[88:91], a[204:207]\n" \
"v_mfma_f32_16x16x32_bf16 a[208:211], a[128:131], a[96:99], a[208:211]\n" \
"v_mfma_f32_16x16x32_bf16 a[208:211], a[132:135], a[100:103], a[208:211]\n" \
"v_mfma_f32_16x16x32_bf16 a[212:215], a[132:135], a[108:111], a[212:215]\n" \
"v_mfma_f32_16x16x32_bf16 a[212:215], a[128:131], a[104:107], a[212:215]\n" \
"v_mfma_f32_16x16x32_bf16 a[216:219], a[128:131], a[112:115], a[216:219]\n" \
"v_mfma_f32_16x16x32_bf16 a[216:219], a[132:135], a[116:119], a[216:219]\n" \
"v_mfma_f32_16x16x32_bf16 a[220:223], a[132:135], a[124:127], a[220:223]\n" \
"v_mfma_f32_16x16x32_bf16 a[220:223], a[128:131], a[120:123], a[220:223]\n" \
"s_waitcnt lgkmcnt(4)\n" \
"v_mfma_f32_16x16x32_bf16 a[252:255], a[136:139], a[120:123], a[252:255]\n" \
"ds_read_b64_tr_b16 a[152:153], v6 offset:8192\n" \
"v_mfma_f32_16x16x32_bf16 a[252:255], a[140:143], a[124:127], a[252:255]\n" \
"v_mfma_f32_16x16x32_bf16 a[248:251], a[140:143], a[116:119], a[248:251]\n" \
"ds_read_b64_tr_b16 a[154:155], v6 offset:8448\n" \
"v_mfma_f32_16x16x32_bf16 a[248:251], a[136:139], a[112:115], a[248:251]\n" \
"v_mfma_f32_16x16x32_bf16 a[244:247], a[136:139], a[104:107], a[244:247]\n" \
"ds_read_b64_tr_b16 a[156:157], v6 offset:12288\n" \
"v_mfma_f32_16x16x32_bf16 a[244:247], a[140:143], a[108:111], a[244:247]\n" \
"v_mfma_f32_16x16x32_bf16 a[240:243], a[140:143], a[100:103], a[240:243]\n" \
"ds_read_b64_tr_b16 a[158:159], v6 offset:12544\n" \
"v_mfma_f32_16x16x32_bf16 a[240:243], a[136:139], a[96:99], a[240:243]\n" \
"v_mfma_f32_16x16x32_bf16 a[236:239], a[136:139], a[88:91], a[236:239]\n" \
"v_mfma_f32_16x16x32_bf16 a[236:239], a[140:143], a[92:95], a[236:239]\n" \
"v_mfma_f32_16x16x32_bf16 a[232:235], a[140:143], a[84:87], a[232:235]\n" \
"v_mfma_f32_16x16x32_bf16 a[232:235], a[136:139], a[80:83], a[232:235]\n" \
"v_mfma_f32_16x16x32_bf16 a[228:231], a[136:139], a[72:75], a[228:231]\n" \
"v_mfma_f32_16x16x32_bf16 a[228:231], a[140:143], a[76:79], a[228:231]\n" \
"v_mfma_f32_16x16x32_bf16 a[224:227], a[140:143], a[68:71], a[224:227]\n" \
"v_mfma_f32_16x16x32_bf16 a[224:227], a[136:139], a[64:67], a[224:227]\n" \
"s_waitcnt vmcnt(1) lgkmcnt(0)\n" \
"s_barrier\n" \
"v_mfma_f32_16x16x32_bf16 v[192:195], a[144:147], a[64:67], v[192:195]\n" \
"ds_read_b64_tr_b16 a[128:129], v1 offset:0\n" \
"v_mfma_f32_16x16x32_bf16 v[192:195], a[148:151], a[68:71], v[192:195]\n" \
"ds_read_b64_tr_b16 a[130:131], v1 offset:512\n" \
"v_mfma_f32_16x16x32_bf16 v[196:199], a[148:151], a[76:79], v[196:199]\n" \
"ds_read_b64_tr_b16 a[132:133], v1 offset:16384\n" \
"v_mfma_f32_16x16x32_bf16 v[196:199], a[144:147], a[72:75], v[196:199]\n" \
"ds_read_b64_tr_b16 a[134:135], v1 offset:16896\n" \
"v_mfma_f32_16x16x32_bf16 v[200:203], a[144:147], a[80:83], v[200:203]\n" \
"ds_read_b128 a[0:3], v0 offset:0\n" \
"v_mfma_f32_16x16x32_bf16 v[200:203], a[148:151], a[84:87], v[200:203]\n" \
"ds_read_b128 a[4:7], v0 offset:1024\n" \
"v_mfma_f32_16x16x32_bf16 v[204:207], a[148:151], a[92:95], v[204:207]\n" \
"ds_read_b128 a[8:11], v0 offset:2048\n" \
"v_mfma_f32_16x16x32_bf16 v[204:207], a[144:147], a[88:91], v[204:207]\n" \
"ds_read_b128 a[12:15], v0 offset:3072\n" \
"v_mfma_f32_16x16x32_bf16 v[208:211], a[144:147], a[96:99], v[208:211]\n" \
"ds_read_b128 a[16:19], v0 offset:4096\n" \
"v_mfma_f32_16x16x32_bf16 v[208:211], a[148:151], a[100:103], v[208:211]\n" \
"ds_read_b128 a[20:23], v0 offset:5120\n" \
"v_mfma_f32_16x16x32_bf16 v[212:215], a[148:151], a[108:111], v[212:215]\n" \
"ds_read_b128 a[24:27], v0 offset:6144\n" \
"v_mfma_f32_16x16x32_bf16 v[212:215], a[144:147], a[104:107], v[212:215]\n" \
"ds_read_b128 a[28:31], v0 offset:7168\n" \
"v_mfma_f32_16x16x32_bf16 v[216:219], a[144:147], a[112:115], v[216:219]\n" \
"ds_read_b128 a[32:35], v0 offset:8192\n" \
"v_mfma_f32_16x16x32_bf16 v[216:219], a[148:151], a[116:119], v[216:219]\n" \
"ds_read_b128 a[36:39], v0 offset:9216\n" \
"v_mfma_f32_16x16x32_bf16 v[220:223], a[148:151], a[124:127], v[220:223]\n" \
"ds_read_b128 a[40:43], v0 offset:10240\n" \
"v_mfma_f32_16x16x32_bf16 v[220:223], a[144:147], a[120:123], v[220:223]\n" \
"ds_read_b128 a[44:47], v0 offset:11264\n" \
"v_mfma_f32_16x16x32_bf16 v[252:255], a[152:155], a[120:123], v[252:255]\n" \
"ds_read_b128 a[48:51], v0 offset:12288\n" \
"v_mfma_f32_16x16x32_bf16 v[252:255], a[156:159], a[124:127], v[252:255]\n" \
"ds_read_b128 a[52:55], v0 offset:13312\n" \
"v_mfma_f32_16x16x32_bf16 v[248:251], a[156:159], a[116:119], v[248:251]\n" \
"ds_read_b128 a[56:59], v0 offset:14336\n" \
"v_mfma_f32_16x16x32_bf16 v[248:251], a[152:155], a[112:115], v[248:251]\n" \
"ds_read_b128 a[60:63], v0 offset:15360\n" \
"v_mfma_f32_16x16x32_bf16 v[244:247], a[152:155], a[104:107], v[244:247]\n" \
"ds_read_b64_tr_b16 a[136:137], v2 offset:0\n" \
"v_mfma_f32_16x16x32_bf16 v[244:247], a[156:159], a[108:111], v[244:247]\n" \
"ds_read_b64_tr_b16 a[138:139], v2 offset:512\n" \
"v_mfma_f32_16x16x32_bf16 v[240:243], a[156:159], a[100:103], v[240:243]\n" \
"ds_read_b64_tr_b16 a[140:141], v2 offset:16384\n" \
"v_mfma_f32_16x16x32_bf16 v[240:243], a[152:155], a[96:99], v[240:243]\n" \
"ds_read_b64_tr_b16 a[142:143], v2 offset:16896\n" \
"v_mfma_f32_16x16x32_bf16 v[236:239], a[152:155], a[88:91], v[236:239]\n" \
"s_add_u32 m0, s71, 32768\n" \
"v_mfma_f32_16x16x32_bf16 v[236:239], a[156:159], a[92:95], v[236:239]\n" \
"buffer_load_dwordx4 v8, s[40:43], s65 offen offset:0 lds\n" \
"v_mfma_f32_16x16x32_bf16 v[232:235], a[156:159], a[84:87], v[232:235]\n" \
"v_mfma_f32_16x16x32_bf16 v[232:235], a[152:155], a[80:83], v[232:235]\n" \
"v_mfma_f32_16x16x32_bf16 v[228:231], a[152:155], a[72:75], v[228:231]\n" \
"v_mfma_f32_16x16x32_bf16 v[228:231], a[156:159], a[76:79], v[228:231]\n" \
"buffer_load_dwordx4 v10, s[40:43], s66 offen offset:1024 lds\n" \
"v_mfma_f32_16x16x32_bf16 v[224:227], a[156:159], a[68:71], v[224:227]\n" \
"v_mfma_f32_16x16x32_bf16 v[224:227], a[152:155], a[64:67], v[224:227]\n" \
"s_sub_u32 s73, s73, 1\n" \
"s_cmp_lg_u32 s73, 0\n" \
"s_cbranch_scc1 L_g1_%=\n" \
"s_waitcnt lgkmcnt(4)\n" \
"v_mfma_f32_16x16x32_bf16 v[32:35], a[128:131], a[0:3], v[32:35]\n" \
"ds_read_b64_tr_b16 a[144:145], v3 offset:0\n" \
"v_mfma_f32_16x16x32_bf16 v[32:35], a[132:135], a[4:7], v[32:35]\n" \
"buffer_load_dwordx4 v8, s[44:47], s65 offen offset:2048 lds\n" \
"v_mfma_f32_16x16x32_bf16 v[36:39], a[132:135], a[12:15], v[36:39]\n" \
"ds_read_b64_tr_b16 a[146:147], v3 offset:512\n" \
"v_mfma_f32_16x16x32_bf16 v[36:39], a[128:131], a[8:11], v[36:39]\n" \
"v_mfma_f32_16x16x32_bf16 v[40:43], a[128:131], a[16:19], v[40:43]\n" \
"ds_read_b64_tr_b16 a[148:149], v3 offset:16384\n" \
"v_mfma_f32_16x16x32_bf16 v[40:43], a[132:135], a[20:23], v[40:43]\n" \
"buffer_load_dwordx4 v10, s[44:47], s66 offen offset:3072 lds\n" \
"v_mfma_f32_16x16x32_bf16 v[44:47], a[132:135], a[28:31], v[44:47]\n" \
"ds_read_b64_tr_b16 a[150:151], v3 offset:16896\n" \
"v_mfma_f32_16x16x32_bf16 v[44:47], a[128:131], a[24:27], v[44:47]\n" \
"v_mfma_f32_16x16x32_bf16 v[48:51], a[128:131], a[32:35], v[48:51]\n" \
"s_add_u32 m0, s71, 36864\n" \
"v_mfma_f32_16x16x32_bf16 v[48:51], a[132:135], a[36:39], v[48:51]\n" \
"buffer_load_dwordx4 v8, s[48:51], s65 offen offset:0 lds\n" \
"v_mfma_f32_16x16x32_bf16 v[52:55], a[132:135], a[44:47], v[52:55]\n" \
"v_mfma_f32_16x16x32_bf16 v[52:55], a[128:131], a[40:43], v[52:55]\n" \
"v_mfma_f32_16x16x32_bf16 v[56:59], a[128:131], a[48:51], v[56:59]\n" \
"v_mfma_f32_16x16x32_bf16 v[56:59], a[132:135], a[52:55], v[56:59]\n" \
"buffer_load_dwordx4 v10, s[48:51], s66 offen offset:1024 lds\n" \
"v_mfma_f32_16x16x32_bf16 v[60:63], a[132:135], a[60:63], v[60:63]\n" \
"v_mfma_f32_16x16x32_bf16 v[60:63], a[128:131], a[56:59], v[60:63]\n" \
"s_waitcnt lgkmcnt(4)\n" \
"v_mfma_f32_16x16x32_bf16 v[92:95], a[136:139], a[56:59], v[92:95]\n" \
"ds_read_b64_tr_b16 a[152:153], v4 offset:0\n" \
"v_mfma_f32_16x16x32_bf16 v[92:95], a[140:143], a[60:63], v[92:95]\n" \
"buffer_load_dwordx4 v8, s[52:55], s65 offen offset:2048 lds\n" \
"v_mfma_f32_16x16x32_bf16 v[88:91], a[140:143], a[52:55], v[88:91]\n" \
"ds_read_b64_tr_b16 a[154:155], v4 offset:512\n" \
"v_mfma_f32_16x16x32_bf16 v[88:91], a[136:139], a[48:51], v[88:91]\n" \
"v_mfma_f32_16x16x32_bf16 v[84:87], a[136:139], a[40:43], v[84:87]\n" \
"ds_read_b64_tr_b16 a[156:157], v4 offset:16384\n" \
"v_mfma_f32_16x16x32_bf16 v[84:87], a[140:143], a[44:47], v[84:87]\n" \
"buffer_load_dwordx4 v10, s[52:55], s66 offen offset:3072 lds\n" \
"v_mfma_f32_16x16x32_bf16 v[80:83], a[140:143], a[36:39], v[80:83]\n" \
"ds_read_b64_tr_b16 a[158:159], v4 offset:16896\n" \
"v_mfma_f32_16x16x32_bf16 v[80:83], a[136:139], a[32:35], v[80:83]\n" \
"v_mfma_f32_16x16x32_bf16 v[76:79], a[136:139], a[24:27], v[76:79]\n" \
"s_add_u32 m0, s72, 8192\n" \
"v_mfma_f32_16x16x32_bf16 v[76:79], a[140:143], a[28:31], v[76:79]\n" \
"buffer_load_dwordx4 v9, s[56:59], s67 offen offset:0 lds\n" \
"v_mfma_f32_16x16x32_bf16 v[72:75], a[140:143], a[20:23], v[72:75]\n" \
"v_mfma_f32_16x16x32_bf16 v[72:75], a[136:139], a[16:19], v[72:75]\n" \
"v_mfma_f32_16x16x32_bf16 v[68:71], a[136:139], a[8:11], v[68:71]\n" \
"v_mfma_f32_16x16x32_bf16 v[68:71], a[140:143], a[12:15], v[68:71]\n" \
"buffer_load_dwordx4 v9, s[60:63], s67 offen offset:1024 lds\n" \
"s_add_u32 s65, s65, s68\n" \
"s_add_u32 s66, s66, s68\n" \
"s_add_u32 s67, s67, s69\n" \
"v_mfma_f32_16x16x32_bf16 v[64:67], a[140:143], a[4:7], v[64:67]\n" \
"v_mfma_f32_16x16x32_bf16 v[64:67], a[136:139], a[0:3], v[64:67]\n" \
"s_waitcnt lgkmcnt(4)\n" \
"v_mfma_f32_16x16x32_bf16 v[96:99], a[144:147], a[0:3], v[96:99]\n" \
"ds_read_b64_tr_b16 a[128:129], v1 offset:2048\n" \
"s_add_u32 m0, s70, 32768\n" \
"v_mfma_f32_16x16x32_bf16 v[96:99], a[148:151], a[4:7], v[96:99]\n" \
"buffer_load_dwordx4 v7, s[36:39], s64 offen offset:0 sc1 lds\n" \
"v_mfma_f32_16x16x32_bf16 v[100:103], a[148:151], a[12:15], v[100:103]\n" \
"ds_read_b64_tr_b16 a[130:131], v1 offset:2560\n" \
"v_mfma_f32_16x16x32_bf16 v[100:103], a[144:147], a[8:11], v[100:103]\n" \
"v_mfma_f32_16x16x32_bf16 v[104:107], a[144:147], a[16:19], v[104:107]\n" \
"ds_read_b64_tr_b16 a[132:133], v1 offset:18432\n" \
"v_mfma_f32_16x16x32_bf16 v[104:107], a[148:151], a[20:23], v[104:107]\n" \
"buffer_load_dwordx4 v7, s[36:39], s64 offen offset:1024 sc1 lds\n" \
"v_mfma_f32_16x16x32_bf16 v[108:111], a[148:151], a[28:31], v[108:111]\n" \
"ds_read_b64_tr_b16 a[134:135], v1 offset:18944\n" \
"v_mfma_f32_16x16x32_bf16 v[108:111], a[144:147], a[24:27], v[108:111]\n" \
"v_mfma_f32_16x16x32_bf16 v[112:115], a[144:147], a[32:35], v[112:115]\n" \
"v_mfma_f32_16x16x32_bf16 v[112:115], a[148:151], a[36:39], v[112:115]\n" \
"buffer_load_dwordx4 v7, s[36:39], s64 offen offset:2048 sc1 lds\n" \
"v_mfma_f32_16x16x32_bf16 v[116:119], a[148:151], a[44:47], v[116:119]\n" \
"v_mfma_f32_16x16x32_bf16 v[116:119], a[144:147], a[40:43], v[116:119]\n" \
"v_mfma_f32_16x16x32_bf16 v[120:123], a[144:147], a[48:51], v[120:123]\n" \
"v_mfma_f32_16x16x32_bf16 v[120:123], a[148:151], a[52:55], v[120:123]\n" \
"buffer_load_dwordx4 v7, s[36:39], s64 offen offset:3072 sc1 lds\n" \
"v_mfma_f32_16x16x32_bf16 v[124:127], a[148:151], a[60:63], v[124:127]\n" \
"v_mfma_f32_16x16x32_bf16 v[124:127], a[144:147], a[56:59], v[124:127]\n" \
"s_waitcnt lgkmcnt(4)\n" \
"v_mfma_f32_16x16x32_bf16 v[156:159], a[152:155], a[56:59], v[156:159]\n" \
"ds_read_b64_tr_b16 a[136:137], v2 offset:2048\n" \
"s_add_u32 m0, s70, 36864\n" \
"s_add_u32 s74, s64, 4096\n" \
"v_mfma_f32_16x16x32_bf16 v[156:159], a[156:159], a[60:63], v[156:159]\n" \
"buffer_load_dwordx4 v7, s[36:39], s74 offen offset:0 sc1 lds\n" \
"v_mfma_f32_16x16x32_bf16 v[152:155], a[156:159], a[52:55], v[152:155]\n" \
"ds_read_b64_tr_b16 a[138:139], v2 offset:2560\n" \
"v_mfma_f32_16x16x32_bf16 v[152:155], a[152:155], a[48:51], v[152:155]\n" \
"v_mfma_f32_16x16x32_bf16 v[148:151], a[152:155], a[40:43], v[148:151]\n" \
"ds_read_b64_tr_b16 a[140:141], v2 offset:18432\n" \
"v_mfma_f32_16x16x32_bf16 v[148:151], a[156:159], a[44:47], v[148:151]\n" \
"buffer_load_dwordx4 v7, s[36:39], s74 offen offset:1024 sc1 lds\n" \
"v_mfma_f32_16x16x32_bf16 v[144:147], a[156:159], a[36:39], v[144:147]\n" \
"ds_read_b64_tr_b16 a[142:143], v2 offset:18944\n" \
"v_mfma_f32_16x16x32_bf16 v[144:147], a[152:155], a[32:35], v[144:147]\n" \
"v_mfma_f32_16x16x32_bf16 v[140:143], a[152:155], a[24:27], v[140:143]\n" \
"v_mfma_f32_16x16x32_bf16 v[140:143], a[156:159], a[28:31], v[140:143]\n" \
"buffer_load_dwordx4 v7, s[36:39], s74 offen offset:2048 sc1 lds\n" \
"v_mfma_f32_16x16x32_bf16 v[136:139], a[156:159], a[20:23], v[136:139]\n" \
"v_mfma_f32_16x16x32_bf16 v[136:139], a[152:155], a[16:19], v[136:139]\n" \
"v_mfma_f32_16x16x32_bf16 v[132:135], a[152:155], a[8:11], v[132:135]\n" \
"v_mfma_f32_16x16x32_bf16 v[132:135], a[156:159], a[12:15], v[132:135]\n" \
"buffer_load_dwordx4 v7, s[36:39], s74 offen offset:3072 sc1 lds\n" \
"s_add_u32 s64, s64, 32768\n" \
"v_mfma_f32_16x16x32_bf16 v[128:131], a[156:159], a[4:7], v[128:131]\n" \
"v_mfma_f32_16x16x32_bf16 v[128:131], a[152:155], a[0:3], v[128:131]\n" \
"s_waitcnt lgkmcnt(4)\n" \
"v_mfma_f32_16x16x32_bf16 v[160:163], a[128:131], a[0:3], v[160:163]\n" \
"ds_read_b64_tr_b16 a[144:145], v3 offset:2048\n" \
"s_mov_b32 m0, s77\n" \
"s_sub_i32 s75, s75, 1\n" \
"v_mfma_f32_16x16x32_bf16 v[160:163], a[132:135], a[4:7], v[160:163]\n" \
"s_cmp_lt_u32 s75, s27\n" \
"s_cselect_b32 s76, 1, 0\n" \
"v_mfma_f32_16x16x32_bf16 v[164:167], a[132:135], a[12:15], v[164:167]\n" \
"ds_read_b64_tr_b16 a[146:147], v3 offset:2560\n" \
"global_load_lds_dword v[12:13], off\n" \
"v_mad_u64_u32 v[12:13], vcc, v11, s76, v[12:13]\n" \
"v_mfma_f32_16x16x32_bf16 v[164:167], a[128:131], a[8:11], v[164:167]\n" \
"v_mfma_f32_16x16x32_bf16 v[168:171], a[128:131], a[16:19], v[168:171]\n" \
"ds_read_b64_tr_b16 a[148:149], v3 offset:18432\n" \
"v_mfma_f32_16x16x32_bf16 v[168:171], a[132:135], a[20:23], v[168:171]\n" \
"v_mfma_f32_16x16x32_bf16 v[172:175], a[132:135], a[28:31], v[172:175]\n" \
"ds_read_b64_tr_b16 a[150:151], v3 offset:18944\n" \
"v_mfma_f32_16x16x32_bf16 v[172:175], a[128:131], a[24:27], v[172:175]\n" \
"v_mfma_f32_16x16x32_bf16 v[176:179], a[128:131], a[32:35], v[176:179]\n" \
"v_mfma_f32_16x16x32_bf16 v[176:179], a[132:135], a[36:39], v[176:179]\n" \
"v_mfma_f32_16x16x32_bf16 v[180:183], a[132:135], a[44:47], v[180:183]\n" \
"v_mfma_f32_16x16x32_bf16 v[180:183], a[128:131], a[40:43], v[180:183]\n" \
"v_mfma_f32_16x16x32_bf16 v[184:187], a[128:131], a[48:51], v[184:187]\n" \
"v_mfma_f32_16x16x32_bf16 v[184:187], a[132:135], a[52:55], v[184:187]\n" \
"v_mfma_f32_16x16x32_bf16 v[188:191], a[132:135], a[60:63], v[188:191]\n" \
"v_mfma_f32_16x16x32_bf16 v[188:191], a[128:131], a[56:59], v[188:191]\n" \
"s_waitcnt lgkmcnt(4)\n" \
"v_mfma_f32_16x16x32_bf16 a[188:191], a[136:139], a[56:59], a[188:191]\n" \
"ds_read_b64_tr_b16 a[152:153], v4 offset:2048\n" \
"v_mfma_f32_16x16x32_bf16 a[188:191], a[140:143], a[60:63], a[188:191]\n" \
"v_mfma_f32_16x16x32_bf16 a[184:187], a[140:143], a[52:55], a[184:187]\n" \
"ds_read_b64_tr_b16 a[154:155], v4 offset:2560\n" \
"v_mfma_f32_16x16x32_bf16 a[184:187], a[136:139], a[48:51], a[184:187]\n" \
"v_mfma_f32_16x16x32_bf16 a[180:183], a[136:139], a[40:43], a[180:183]\n" \
"ds_read_b64_tr_b16 a[156:157], v4 offset:18432\n" \
"v_mfma_f32_16x16x32_bf16 a[180:183], a[140:143], a[44:47], a[180:183]\n" \
"v_mfma_f32_16x16x32_bf16 a[176:179], a[140:143], a[36:39], a[176:179]\n" \
"ds_read_b64_tr_b16 a[158:159], v4 offset:18944\n" \
"v_mfma_f32_16x16x32_bf16 a[176:179], a[136:139], a[32:35], a[176:179]\n" \
"v_mfma_f32_16x16x32_bf16 a[172:175], a[136:139], a[24:27], a[172:175]\n" \
"v_mfma_f32_16x16x32_bf16 a[172:175], a[140:143], a[28:31], a[172:175]\n" \
"v_mfma_f32_16x16x32_bf16 a[168:171], a[140:143], a[20:23], a[168:171]\n" \
"v_mfma_f32_16x16x32_bf16 a[168:171], a[136:139], a[16:19], a[168:171]\n" \
"v_mfma_f32_16x16x32_bf16 a[164:167], a[136:139], a[8:11], a[164:167]\n" \
"v_mfma_f32_16x16x32_bf16 a[164:167], a[140:143], a[12:15], a[164:167]\n" \
"v_mfma_f32_16x16x32_bf16 a[160:163], a[140:143], a[4:7], a[160:163]\n" \
"v_mfma_f32_16x16x32_bf16 a[160:163], a[136:139], a[0:3], a[160:163]\n" \
"s_waitcnt lgkmcnt(4)\n" \
"v_mfma_f32_16x16x32_bf16 a[192:195], a[144:147], a[0:3], a[192:195]\n" \
"ds_read_b64_tr_b16 a[128:129], v5 offset:0\n" \
"v_mfma_f32_16x16x32_bf16 a[192:195], a[148:151], a[4:7], a[192:195]\n" \
"v_mfma_f32_16x16x32_bf16 a[196:199], a[148:151], a[12:15], a[196:199]\n" \
"ds_read_b64_tr_b16 a[130:131], v5 offset:256\n" \
"v_mfma_f32_16x16x32_bf16 a[196:199], a[144:147], a[8:11], a[196:199]\n" \
"v_mfma_f32_16x16x32_bf16 a[200:203], a[144:147], a[16:19], a[200:203]\n" \
"ds_read_b64_tr_b16 a[132:133], v5 offset:4096\n" \
"v_mfma_f32_16x16x32_bf16 a[200:203], a[148:151], a[20:23], a[200:203]\n" \
"v_mfma_f32_16x16x32_bf16 a[204:207], a[148:151], a[28:31], a[204:207]\n" \
"ds_read_b64_tr_b16 a[134:135], v5 offset:4352\n" \
"v_mfma_f32_16x16x32_bf16 a[204:207], a[144:147], a[24:27], a[204:207]\n" \
"v_mfma_f32_16x16x32_bf16 a[208:211], a[144:147], a[32:35], a[208:211]\n" \
"v_mfma_f32_16x16x32_bf16 a[208:211], a[148:151], a[36:39], a[208:211]\n" \
"v_mfma_f32_16x16x32_bf16 a[212:215], a[148:151], a[44:47], a[212:215]\n" \
"v_mfma_f32_16x16x32_bf16 a[212:215], a[144:147], a[40:43], a[212:215]\n" \
"v_mfma_f32_16x16x32_bf16 a[216:219], a[144:147], a[48:51], a[216:219]\n" \
"v_mfma_f32_16x16x32_bf16 a[216:219], a[148:151], a[52:55], a[216:219]\n" \
"v_mfma_f32_16x16x32_bf16 a[220:223], a[148:151], a[60:63], a[220:223]\n" \
"v_mfma_f32_16x16x32_bf16 a[220:223], a[144:147], a[56:59], a[220:223]\n" \
"s_waitcnt lgkmcnt(4)\n" \
"v_mfma_f32_16x16x32_bf16 a[252:255], a[152:155], a[56:59], a[252:255]\n" \
"ds_read_b64_tr_b16 a[136:137], v6 offset:0\n" \
"v_mfma_f32_16x16x32_bf16 a[252:255], a[156:159], a[60:63], a[252:255]\n" \
"v_mfma_f32_16x16x32_bf16 a[248:251], a[156:159], a[52:55], a[248:251]\n" \
"ds_read_b64_tr_b16 a[138:139], v6 offset:256\n" \
"v_mfma_f32_16x16x32_bf16 a[248:251], a[152:155], a[48:51], a[248:251]\n" \
"v_mfma_f32_16x16x32_bf16 a[244:247], a[152:155], a[40:43], a[244:247]\n" \
"ds_read_b64_tr_b16 a[140:141], v6 offset:4096\n" \
"v_mfma_f32_16x16x32_bf16 a[244:247], a[156:159], a[44:47], a[244:247]\n" \
"v_mfma_f32_16x16x32_bf16 a[240:243], a[156:159], a[36:39], a[240:243]\n" \
"ds_read_b64_tr_b16 a[142:143], v6 offset:4352\n" \
"v_mfma_f32_16x16x32_bf16 a[240:243], a[152:155], a[32:35], a[240:243]\n" \
"v_mfma_f32_16x16x32_bf16 a[236:239], a[152:155], a[24:27], a[236:239]\n" \
"v_mfma_f32_16x16x32_bf16 a[236:239], a[156:159], a[28:31], a[236:239]\n" \
"v_mfma_f32_16x16x32_bf16 a[232:235], a[156:159], a[20:23], a[232:235]\n" \
"v_mfma_f32_16x16x32_bf16 a[232:235], a[152:155], a[16:19], a[232:235]\n" \
"v_mfma_f32_16x16x32_bf16 a[228:231], a[152:155], a[8:11], a[228:231]\n" \
"v_mfma_f32_16x16x32_bf16 a[228:231], a[156:159], a[12:15], a[228:231]\n" \
"v_mfma_f32_16x16x32_bf16 a[224:227], a[156:159], a[4:7], a[224:227]\n" \
"v_mfma_f32_16x16x32_bf16 a[224:227], a[152:155], a[0:3], a[224:227]\n" \
"s_waitcnt vmcnt(0) lgkmcnt(0)\n" \
"s_barrier\n" \
"v_mfma_f32_16x16x32_bf16 v[192:195], a[128:131], a[0:3], v[192:195]\n" \
"ds_read_b64_tr_b16 a[144:145], v1 offset:32768\n" \
"v_mfma_f32_16x16x32_bf16 v[192:195], a[132:135], a[4:7], v[192:195]\n" \
"ds_read_b64_tr_b16 a[146:147], v1 offset:33280\n" \
"v_mfma_f32_16x16x32_bf16 v[196:199], a[132:135], a[12:15], v[196:199]\n" \
"ds_read_b64_tr_b16 a[148:149], v1 offset:49152\n" \
"v_mfma_f32_16x16x32_bf16 v[196:199], a[128:131], a[8:11], v[196:199]\n" \
"ds_read_b64_tr_b16 a[150:151], v1 offset:49664\n" \
"v_mfma_f32_16x16x32_bf16 v[200:203], a[128:131], a[16:19], v[200:203]\n" \
"ds_read_b128 a[64:67], v0 offset:32768\n" \
"v_mfma_f32_16x16x32_bf16 v[200:203], a[132:135], a[20:23], v[200:203]\n" \
"ds_read_b128 a[68:71], v0 offset:33792\n" \
"v_mfma_f32_16x16x32_bf16 v[204:207], a[132:135], a[28:31], v[204:207]\n" \
"ds_read_b128 a[72:75], v0 offset:34816\n" \
"v_mfma_f32_16x16x32_bf16 v[204:207], a[128:131], a[24:27], v[204:207]\n" \
"ds_read_b128 a[76:79], v0 offset:35840\n" \
"v_mfma_f32_16x16x32_bf16 v[208:211], a[128:131], a[32:35], v[208:211]\n" \
"ds_read_b128 a[80:83], v0 offset:36864\n" \
"v_mfma_f32_16x16x32_bf16 v[208:211], a[132:135], a[36:39], v[208:211]\n" \
"ds_read_b128 a[84:87], v0 offset:37888\n" \
"v_mfma_f32_16x16x32_bf16 v[212:215], a[132:135], a[44:47], v[212:215]\n" \
"ds_read_b128 a[88:91], v0 offset:38912\n" \
"v_mfma_f32_16x16x32_bf16 v[212:215], a[128:131], a[40:43], v[212:215]\n" \
"ds_read_b128 a[92:95], v0 offset:39936\n" \
"v_mfma_f32_16x16x32_bf16 v[216:219], a[128:131], a[48:51], v[216:219]\n" \
"ds_read_b128 a[96:99], v0 offset:40960\n" \
"v_mfma_f32_16x16x32_bf16 v[216:219], a[132:135], a[52:55], v[216:219]\n" \
"ds_read_b128 a[100:103], v0 offset:41984\n" \
"v_mfma_f32_16x16x32_bf16 v[220:223], a[132:135], a[60:63], v[220:223]\n" \
"ds_read_b128 a[104:107], v0 offset:43008\n" \
"v_mfma_f32_16x16x32_bf16 v[220:223], a[128:131], a[56:59], v[220:223]\n" \
"ds_read_b128 a[108:111], v0 offset:44032\n" \
"v_mfma_f32_16x16x32_bf16 v[252:255], a[136:139], a[56:59], v[252:255]\n" \
"ds_read_b128 a[112:115], v0 offset:45056\n" \
"v_mfma_f32_16x16x32_bf16 v[252:255], a[140:143], a[60:63], v[252:255]\n" \
"ds_read_b128 a[116:119], v0 offset:46080\n" \
"v_mfma_f32_16x16x32_bf16 v[248:251], a[140:143], a[52:55], v[248:251]\n" \
"ds_read_b128 a[120:123], v0 offset:47104\n" \
"v_mfma_f32_16x16x32_bf16 v[248:251], a[136:139], a[48:51], v[248:251]\n" \
"ds_read_b128 a[124:127], v0 offset:48128\n" \
"v_mfma_f32_16x16x32_bf16 v[244:247], a[136:139], a[40:43], v[244:247]\n" \
"ds_read_b64_tr_b16 a[152:153], v2 offset:32768\n" \
"v_mfma_f32_16x16x32_bf16 v[244:247], a[140:143], a[44:47], v[244:247]\n" \
"ds_read_b64_tr_b16 a[154:155], v2 offset:33280\n" \
"v_mfma_f32_16x16x32_bf16 v[240:243], a[140:143], a[36:39], v[240:243]\n" \
"ds_read_b64_tr_b16 a[156:157], v2 offset:49152\n" \
"v_mfma_f32_16x16x32_bf16 v[240:243], a[136:139], a[32:35], v[240:243]\n" \
"ds_read_b64_tr_b16 a[158:159], v2 offset:49664\n" \
"v_mfma_f32_16x16x32_bf16 v[236:239], a[136:139], a[24:27], v[236:239]\n" \
"v_mfma_f32_16x16x32_bf16 v[236:239], a[140:143], a[28:31], v[236:239]\n" \
"v_mfma_f32_16x16x32_bf16 v[232:235], a[140:143], a[20:23], v[232:235]\n" \
"v_mfma_f32_16x16x32_bf16 v[232:235], a[136:139], a[16:19], v[232:235]\n" \
"v_mfma_f32_16x16x32_bf16 v[228:231], a[136:139], a[8:11], v[228:231]\n" \
"v_mfma_f32_16x16x32_bf16 v[228:231], a[140:143], a[12:15], v[228:231]\n" \
"v_mfma_f32_16x16x32_bf16 v[224:227], a[140:143], a[4:7], v[224:227]\n" \
"v_mfma_f32_16x16x32_bf16 v[224:227], a[136:139], a[0:3], v[224:227]\n" \
"s_waitcnt lgkmcnt(4)\n" \
"v_mfma_f32_16x16x32_bf16 v[32:35], a[144:147], a[64:67], v[32:35]\n" \
"ds_read_b64_tr_b16 a[128:129], v3 offset:32768\n" \
"v_mfma_f32_16x16x32_bf16 v[32:35], a[148:151], a[68:71], v[32:35]\n" \
"v_mfma_f32_16x16x32_bf16 v[36:39], a[148:151], a[76:79], v[36:39]\n" \
"ds_read_b64_tr_b16 a[130:131], v3 offset:33280\n" \
"s_add_u32 m0, s79, 0\n" \
"s_mov_b32 s74, 525312\n" \
"v_mfma_f32_16x16x32_bf16 v[36:39], a[144:147], a[72:75], v[36:39]\n" \
"buffer_load_dwordx4 v15, s[88:91], s74 offen offset:0 lds\n" \
"v_mfma_f32_16x16x32_bf16 v[40:43], a[144:147], a[80:83], v[40:43]\n" \
"ds_read_b64_tr_b16 a[132:133], v3 offset:49152\n" \
"v_mfma_f32_16x16x32_bf16 v[40:43], a[148:151], a[84:87], v[40:43]\n" \
"v_mfma_f32_16x16x32_bf16 v[44:47], a[148:151], a[92:95], v[44:47]\n" \
"ds_read_b64_tr_b16 a[134:135], v3 offset:49664\n" \
"v_mfma_f32_16x16x32_bf16 v[44:47], a[144:147], a[88:91], v[44:47]\n" \
"v_mfma_f32_16x16x32_bf16 v[48:51], a[144:147], a[96:99], v[48:51]\n" \
"s_mov_b32 s74, 524416\n" \
"v_mfma_f32_16x16x32_bf16 v[48:51], a[148:151], a[100:103], v[48:51]\n" \
"buffer_load_dwordx4 v15, s[88:91], s74 offen offset:1024 lds\n" \
"v_mfma_f32_16x16x32_bf16 v[52:55], a[148:151], a[108:111], v[52:55]\n" \
"v_mfma_f32_16x16x32_bf16 v[52:55], a[144:147], a[104:107], v[52:55]\n" \
"v_mfma_f32_16x16x32_bf16 v[56:59], a[144:147], a[112:115], v[56:59]\n" \
"v_mfma_f32_16x16x32_bf16 v[56:59], a[148:151], a[116:119], v[56:59]\n" \
"v_mfma_f32_16x16x32_bf16 v[60:63], a[148:151], a[124:127], v[60:63]\n" \
"s_mov_b32 s74, 539648\n" \
"v_mfma_f32_16x16x32_bf16 v[60:63], a[144:147], a[120:123], v[60:63]\n" \
"buffer_load_dwordx4 v15, s[88:91], s74 offen offset:2048 lds\n" \
"s_waitcnt lgkmcnt(4)\n" \
"v_mfma_f32_16x16x32_bf16 v[92:95], a[152:155], a[120:123], v[92:95]\n" \
"ds_read_b64_tr_b16 a[136:137], v4 offset:32768\n" \
"v_mfma_f32_16x16x32_bf16 v[92:95], a[156:159], a[124:127], v[92:95]\n" \
"v_mfma_f32_16x16x32_bf16 v[88:91], a[156:159], a[116:119], v[88:91]\n" \
"ds_read_b64_tr_b16 a[138:139], v4 offset:33280\n" \
"v_mfma_f32_16x16x32_bf16 v[88:91], a[152:155], a[112:115], v[88:91]\n" \
"v_mfma_f32_16x16x32_bf16 v[84:87], a[152:155], a[104:107], v[84:87]\n" \
"ds_read_b64_tr_b16 a[140:141], v4 offset:49152\n" \
"s_mov_b32 s74, 538752\n" \
"v_mfma_f32_16x16x32_bf16 v[84:87], a[156:159], a[108:111], v[84:87]\n" \
"buffer_load_dwordx4 v15, s[88:91], s74 offen offset:3072 lds\n" \
"v_mfma_f32_16x16x32_bf16 v[80:83], a[156:159], a[100:103], v[80:83]\n" \
"ds_read_b64_tr_b16 a[142:143], v4 offset:49664\n" \
"v_mfma_f32_16x16x32_bf16 v[80:83], a[152:155], a[96:99], v[80:83]\n" \
"v_mfma_f32_16x16x32_bf16 v[76:79], a[152:155], a[88:91], v[76:79]\n" \
"v_mfma_f32_16x16x32_bf16 v[76:79], a[156:159], a[92:95], v[76:79]\n" \
"v_mfma_f32_16x16x32_bf16 v[72:75], a[156:159], a[84:87], v[72:75]\n" \
"s_add_u32 m0, s79, 16384\n" \
"s_mov_b32 s74, 1180672\n" \
"v_mfma_f32_16x16x32_bf16 v[72:75], a[152:155], a[80:83], v[72:75]\n" \
"buffer_load_dwordx4 v15, s[88:91], s74 offen offset:0 lds\n" \
"v_mfma_f32_16x16x32_bf16 v[68:71], a[152:155], a[72:75], v[68:71]\n" \
"v_mfma_f32_16x16x32_bf16 v[68:71], a[156:159], a[76:79], v[68:71]\n" \
"v_mfma_f32_16x16x32_bf16 v[64:67], a[156:159], a[68:71], v[64:67]\n" \
"v_mfma_f32_16x16x32_bf16 v[64:67], a[152:155], a[64:67], v[64:67]\n" \
"s_waitcnt lgkmcnt(4)\n" \
"v_mfma_f32_16x16x32_bf16 v[96:99], a[128:131], a[64:67], v[96:99]\n" \
"ds_read_b64_tr_b16 a[144:145], v1 offset:34816\n" \
"s_mov_b32 s74, 1179776\n" \
"v_mfma_f32_16x16x32_bf16 v[96:99], a[132:135], a[68:71], v[96:99]\n" \
"buffer_load_dwordx4 v15, s[88:91], s74 offen offset:1024 lds\n" \
"v_mfma_f32_16x16x32_bf16 v[100:103], a[132:135], a[76:79], v[100:103]\n" \
"ds_read_b64_tr_b16 a[146:147], v1 offset:35328\n" \
"v_mfma_f32_16x16x32_bf16 v[100:103], a[128:131], a[72:75], v[100:103]\n" \
"v_mfma_f32_16x16x32_bf16 v[104:107], a[128:131], a[80:83], v[104:107]\n" \
"ds_read_b64_tr_b16 a[148:149], v1 offset:51200\n" \
"v_mfma_f32_16x16x32_bf16 v[104:107], a[132:135], a[84:87], v[104:107]\n" \
"v_mfma_f32_16x16x32_bf16 v[108:111], a[132:135], a[92:95], v[108:111]\n" \
"ds_read_b64_tr_b16 a[150:151], v1 offset:51712\n" \
"s_mov_b32 s74, 1195008\n" \
"v_mfma_f32_16x16x32_bf16 v[108:111], a[128:131], a[88:91], v[108:111]\n" \
"buffer_load_dwordx4 v15, s[88:91], s74 offen offset:2048 lds\n" \
"v_mfma_f32_16x16x32_bf16 v[112:115], a[128:131], a[96:99], v[112:115]\n" \
"v_mfma_f32_16x16x32_bf16 v[112:115], a[132:135], a[100:103], v[112:115]\n" \
"v_mfma_f32_16x16x32_bf16 v[116:119], a[132:135], a[108:111], v[116:119]\n" \
"v_mfma_f32_16x16x32_bf16 v[116:119], a[128:131], a[104:107], v[116:119]\n" \
"v_mfma_f32_16x16x32_bf16 v[120:123], a[128:131], a[112:115], v[120:123]\n" \
"s_mov_b32 s74, 1194112\n" \
"v_mfma_f32_16x16x32_bf16 v[120:123], a[132:135], a[116:119], v[120:123]\n" \
"buffer_load_dwordx4 v15, s[88:91], s74 offen offset:3072 lds\n" \
"v_mfma_f32_16x16x32_bf16 v[124:127], a[132:135], a[124:127], v[124:127]\n" \
"v_mfma_f32_16x16x32_bf16 v[124:127], a[128:131], a[120:123], v[124:127]\n" \
"s_waitcnt lgkmcnt(4)\n" \
"v_mfma_f32_16x16x32_bf16 v[156:159], a[136:139], a[120:123], v[156:159]\n" \
"ds_read_b64_tr_b16 a[152:153], v2 offset:34816\n" \
"v_mfma_f32_16x16x32_bf16 v[156:159], a[140:143], a[124:127], v[156:159]\n" \
"v_mfma_f32_16x16x32_bf16 v[152:155], a[140:143], a[116:119], v[152:155]\n" \
"ds_read_b64_tr_b16 a[154:155], v2 offset:35328\n" \
"s_add_u32 m0, s79, 65536\n" \
"s_mov_b32 s74, 1836032\n" \
"v_mfma_f32_16x16x32_bf16 v[152:155], a[136:139], a[112:115], v[152:155]\n" \
"buffer_load_dwordx4 v15, s[88:91], s74 offen offset:0 lds\n" \
"v_mfma_f32_16x16x32_bf16 v[148:151], a[136:139], a[104:107], v[148:151]\n" \
"ds_read_b64_tr_b16 a[156:157], v2 offset:51200\n" \
"v_mfma_f32_16x16x32_bf16 v[148:151], a[140:143], a[108:111], v[148:151]\n" \
"v_mfma_f32_16x16x32_bf16 v[144:147], a[140:143], a[100:103], v[144:147]\n" \
"ds_read_b64_tr_b16 a[158:159], v2 offset:51712\n" \
"v_mfma_f32_16x16x32_bf16 v[144:147], a[136:139], a[96:99], v[144:147]\n" \
"v_mfma_f32_16x16x32_bf16 v[140:143], a[136:139], a[88:91], v[140:143]\n" \
"s_mov_b32 s74, 1835136\n" \
"v_mfma_f32_16x16x32_bf16 v[140:143], a[140:143], a[92:95], v[140:143]\n" \
"buffer_load_dwordx4 v15, s[88:91], s74 offen offset:1024 lds\n" \
"v_mfma_f32_16x16x32_bf16 v[136:139], a[140:143], a[84:87], v[136:139]\n" \
"v_mfma_f32_16x16x32_bf16 v[136:139], a[136:139], a[80:83], v[136:139]\n" \
"v_mfma_f32_16x16x32_bf16 v[132:135], a[136:139], a[72:75], v[132:135]\n" \
"v_mfma_f32_16x16x32_bf16 v[132:135], a[140:143], a[76:79], v[132:135]\n" \
"v_mfma_f32_16x16x32_bf16 v[128:131], a[140:143], a[68:71], v[128:131]\n" \
"s_mov_b32 s74, 1850368\n" \
"v_mfma_f32_16x16x32_bf16 v[128:131], a[136:139], a[64:67], v[128:131]\n" \
"buffer_load_dwordx4 v15, s[88:91], s74 offen offset:2048 lds\n" \
"s_waitcnt lgkmcnt(4)\n" \
"v_mfma_f32_16x16x32_bf16 v[160:163], a[144:147], a[64:67], v[160:163]\n" \
"ds_read_b64_tr_b16 a[128:129], v3 offset:34816\n" \
"v_mfma_f32_16x16x32_bf16 v[160:163], a[148:151], a[68:71], v[160:163]\n" \
"v_mfma_f32_16x16x32_bf16 v[164:167], a[148:151], a[76:79], v[164:167]\n" \
"ds_read_b64_tr_b16 a[130:131], v3 offset:35328\n" \
"v_mfma_f32_16x16x32_bf16 v[164:167], a[144:147], a[72:75], v[164:167]\n" \
"v_mfma_f32_16x16x32_bf16 v[168:171], a[144:147], a[80:83], v[168:171]\n" \
"ds_read_b64_tr_b16 a[132:133], v3 offset:51200\n" \
"s_mov_b32 s74, 1849472\n" \
"v_mfma_f32_16x16x32_bf16 v[168:171], a[148:151], a[84:87], v[168:171]\n" \
"buffer_load_dwordx4 v15, s[88:91], s74 offen offset:3072 lds\n" \
"v_mfma_f32_16x16x32_bf16 v[172:175], a[148:151], a[92:95], v[172:175]\n" \
"ds_read_b64_tr_b16 a[134:135], v3 offset:51712\n" \
"v_mfma_f32_16x16x32_bf16 v[172:175], a[144:147], a[88:91], v[172:175]\n" \
"v_mfma_f32_16x16x32_bf16 v[176:179], a[144:147], a[96:99], v[176:179]\n" \
"v_mfma_f32_16x16x32_bf16 v[176:179], a[148:151], a[100:103], v[176:179]\n" \
"v_mfma_f32_16x16x32_bf16 v[180:183], a[148:151], a[108:111], v[180:183]\n" \
"s_add_u32 m0, s79, 81920\n" \
"s_mov_b32 s74, 2491392\n" \
"v_mfma_f32_16x16x32_bf16 v[180:183], a[144:147], a[104:107], v[180:183]\n" \
"buffer_load_dwordx4 v15, s[88:91], s74 offen offset:0 lds\n" \
"v_mfma_f32_16x16x32_bf16 v[184:187], a[144:147], a[112:115], v[184:187]\n" \
"v_mfma_f32_16x16x32_bf16 v[184:187], a[148:151], a[116:119], v[184:187]\n" \
"v_mfma_f32_16x16x32_bf16 v[188:191], a[148:151], a[124:127], v[188:191]\n" \
"v_mfma_f32_16x16x32_bf16 v[188:191], a[144:147], a[120:123], v[188:191]\n" \
"s_waitcnt lgkmcnt(4)\n" \
"v_mfma_f32_16x16x32_bf16 a[188:191], a[152:155], a[120:123], a[188:191]\n" \
"ds_read_b64_tr_b16 a[136:137], v4 offset:34816\n" \
"s_mov_b32 s74, 2490496\n" \
"v_mfma_f32_16x16x32_bf16 a[188:191], a[156:159], a[124:127], a[188:191]\n" \
"buffer_load_dwordx4 v15, s[88:91], s74 offen offset:1024 lds\n" \
"v_mfma_f32_16x16x32_bf16 a[184:187], a[156:159], a[116:119], a[184:187]\n" \
"ds_read_b64_tr_b16 a[138:139], v4 offset:35328\n" \
"v_mfma_f32_16x16x32_bf16 a[184:187], a[152:155], a[112:115], a[184:187]\n" \
"v_mfma_f32_16x16x32_bf16 a[180:183], a[152:155], a[104:107], a[180:183]\n" \
"ds_read_b64_tr_b16 a[140:141], v4 offset:51200\n" \
"v_mfma_f32_16x16x32_bf16 a[180:183], a[156:159], a[108:111], a[180:183]\n" \
"v_mfma_f32_16x16x32_bf16 a[176:179], a[156:159], a[100:103], a[176:179]\n" \
"ds_read_b64_tr_b16 a[142:143], v4 offset:51712\n" \
"s_mov_b32 s74, 2505728\n" \
"v_mfma_f32_16x16x32_bf16 a[176:179], a[152:155], a[96:99], a[176:179]\n" \
"buffer_load_dwordx4 v15, s[88:91], s74 offen offset:2048 lds\n" \
"v_mfma_f32_16x16x32_bf16 a[172:175], a[152:155], a[88:91], a[172:175]\n" \
"v_mfma_f32_16x16x32_bf16 a[172:175], a[156:159], a[92:95], a[172:175]\n" \
"v_mfma_f32_16x16x32_bf16 a[168:171], a[156:159], a[84:87], a[168:171]\n" \
"v_mfma_f32_16x16x32_bf16 a[168:171], a[152:155], a[80:83], a[168:171]\n" \
"v_mfma_f32_16x16x32_bf16 a[164:167], a[152:155], a[72:75], a[164:167]\n" \
"s_mov_b32 s74, 2504832\n" \
"v_mfma_f32_16x16x32_bf16 a[164:167], a[156:159], a[76:79], a[164:167]\n" \
"buffer_load_dwordx4 v15, s[88:91], s74 offen offset:3072 lds\n" \
"v_mfma_f32_16x16x32_bf16 a[160:163], a[156:159], a[68:71], a[160:163]\n" \
"v_mfma_f32_16x16x32_bf16 a[160:163], a[152:155], a[64:67], a[160:163]\n" \
"s_waitcnt lgkmcnt(4)\n" \
"v_mfma_f32_16x16x32_bf16 a[192:195], a[128:131], a[64:67], a[192:195]\n" \
"ds_read_b64_tr_b16 a[144:145], v5 offset:8192\n" \
"v_mfma_f32_16x16x32_bf16 a[192:195], a[132:135], a[68:71], a[192:195]\n" \
"v_mfma_f32_16x16x32_bf16 a[196:199], a[132:135], a[76:79], a[196:199]\n" \
"ds_read_b64_tr_b16 a[146:147], v5 offset:8448\n" \
"s_add_u32 m0, s79, 147456\n" \
"s_mov_b32 s74, 1180672\n" \
"v_mfma_f32_16x16x32_bf16 a[196:199], a[128:131], a[72:75], a[196:199]\n" \
"buffer_load_dwordx4 v15, s[92:95], s74 offen offset:0 lds\n" \
"v_mfma_f32_16x16x32_bf16 a[200:203], a[128:131], a[80:83], a[200:203]\n" \
"ds_read_b64_tr_b16 a[148:149], v5 offset:12288\n" \
"v_mfma_f32_16x16x32_bf16 a[200:203], a[132:135], a[84:87], a[200:203]\n" \
"v_mfma_f32_16x16x32_bf16 a[204:207], a[132:135], a[92:95], a[204:207]\n" \
"ds_read_b64_tr_b16 a[150:151], v5 offset:12544\n" \
"v_mfma_f32_16x16x32_bf16 a[204:207], a[128:131], a[88:91], a[204:207]\n" \
"v_mfma_f32_16x16x32_bf16 a[208:211], a[128:131], a[96:99], a[208:211]\n" \
"s_mov_b32 s74, 1179776\n" \
"v_mfma_f32_16x16x32_bf16 a[208:211], a[132:135], a[100:103], a[208:211]\n" \
"buffer_load_dwordx4 v15, s[92:95], s74 offen offset:1024 lds\n" \
"v_mfma_f32_16x16x32_bf16 a[212:215], a[132:135], a[108:111], a[212:215]\n" \
"v_mfma_f32_16x16x32_bf16 a[212:215], a[128:131], a[104:107], a[212:215]\n" \
"v_mfma_f32_16x16x32_bf16 a[216:219], a[128:131], a[112:115], a[216:219]\n" \
"v_mfma_f32_16x16x32_bf16 a[216:219], a[132:135], a[116:119], a[216:219]\n" \
"v_mfma_f32_16x16x32_bf16 a[220:223], a[132:135], a[124:127], a[220:223]\n" \
"s_mov_b32 s74, 1195008\n" \
"v_mfma_f32_16x16x32_bf16 a[220:223], a[128:131], a[120:123], a[220:223]\n" \
"buffer_load_dwordx4 v15, s[92:95], s74 offen offset:2048 lds\n" \
"s_waitcnt lgkmcnt(4)\n" \
"v_mfma_f32_16x16x32_bf16 a[252:255], a[136:139], a[120:123], a[252:255]\n" \
"ds_read_b64_tr_b16 a[152:153], v6 offset:8192\n" \
"v_mfma_f32_16x16x32_bf16 a[252:255], a[140:143], a[124:127], a[252:255]\n" \
"v_mfma_f32_16x16x32_bf16 a[248:251], a[140:143], a[116:119], a[248:251]\n" \
"ds_read_b64_tr_b16 a[154:155], v6 offset:8448\n" \
"v_mfma_f32_16x16x32_bf16 a[248:251], a[136:139], a[112:115], a[248:251]\n" \
"v_mfma_f32_16x16x32_bf16 a[244:247], a[136:139], a[104:107], a[244:247]\n" \
"ds_read_b64_tr_b16 a[156:157], v6 offset:12288\n" \
"s_mov_b32 s74, 1194112\n" \
"v_mfma_f32_16x16x32_bf16 a[244:247], a[140:143], a[108:111], a[244:247]\n" \
"buffer_load_dwordx4 v15, s[92:95], s74 offen offset:3072 lds\n" \
"v_mfma_f32_16x16x32_bf16 a[240:243], a[140:143], a[100:103], a[240:243]\n" \
"ds_read_b64_tr_b16 a[158:159], v6 offset:12544\n" \
"v_mfma_f32_16x16x32_bf16 a[240:243], a[136:139], a[96:99], a[240:243]\n" \
"v_mfma_f32_16x16x32_bf16 a[236:239], a[136:139], a[88:91], a[236:239]\n" \
"v_mfma_f32_16x16x32_bf16 a[236:239], a[140:143], a[92:95], a[236:239]\n" \
"v_mfma_f32_16x16x32_bf16 a[232:235], a[140:143], a[84:87], a[232:235]\n" \
"v_mfma_f32_16x16x32_bf16 a[232:235], a[136:139], a[80:83], a[232:235]\n" \
"v_mfma_f32_16x16x32_bf16 a[228:231], a[136:139], a[72:75], a[228:231]\n" \
"v_mfma_f32_16x16x32_bf16 a[228:231], a[140:143], a[76:79], a[228:231]\n" \
"v_mfma_f32_16x16x32_bf16 a[224:227], a[140:143], a[68:71], a[224:227]\n" \
"v_mfma_f32_16x16x32_bf16 a[224:227], a[136:139], a[64:67], a[224:227]\n" \
"s_waitcnt lgkmcnt(0)\n" \
"s_barrier\n" \
"v_mfma_f32_16x16x32_bf16 v[192:195], a[144:147], a[64:67], v[192:195]\n" \
"s_add_u32 m0, s79, 98304\n" \
"s_mov_b32 s74, 132096\n" \
"v_mfma_f32_16x16x32_bf16 v[192:195], a[148:151], a[68:71], v[192:195]\n" \
"buffer_load_dwordx4 v15, s[92:95], s74 offen offset:0 lds\n" \
"v_mfma_f32_16x16x32_bf16 v[196:199], a[148:151], a[76:79], v[196:199]\n" \
"v_mfma_f32_16x16x32_bf16 v[196:199], a[144:147], a[72:75], v[196:199]\n" \
"v_mfma_f32_16x16x32_bf16 v[200:203], a[144:147], a[80:83], v[200:203]\n" \
"s_mov_b32 s74, 131200\n" \
"v_mfma_f32_16x16x32_bf16 v[200:203], a[148:151], a[84:87], v[200:203]\n" \
"buffer_load_dwordx4 v15, s[92:95], s74 offen offset:1024 lds\n" \
"v_mfma_f32_16x16x32_bf16 v[204:207], a[148:151], a[92:95], v[204:207]\n" \
"v_mfma_f32_16x16x32_bf16 v[204:207], a[144:147], a[88:91], v[204:207]\n" \
"v_mfma_f32_16x16x32_bf16 v[208:211], a[144:147], a[96:99], v[208:211]\n" \
"s_mov_b32 s74, 146432\n" \
"v_mfma_f32_16x16x32_bf16 v[208:211], a[148:151], a[100:103], v[208:211]\n" \
"buffer_load_dwordx4 v15, s[92:95], s74 offen offset:2048 lds\n" \
"v_mfma_f32_16x16x32_bf16 v[212:215], a[148:151], a[108:111], v[212:215]\n" \
"v_mfma_f32_16x16x32_bf16 v[212:215], a[144:147], a[104:107], v[212:215]\n" \
"v_mfma_f32_16x16x32_bf16 v[216:219], a[144:147], a[112:115], v[216:219]\n" \
"s_mov_b32 s74, 145536\n" \
"v_mfma_f32_16x16x32_bf16 v[216:219], a[148:151], a[116:119], v[216:219]\n" \
"buffer_load_dwordx4 v15, s[92:95], s74 offen offset:3072 lds\n" \
"v_mfma_f32_16x16x32_bf16 v[220:223], a[148:151], a[124:127], v[220:223]\n" \
"v_mfma_f32_16x16x32_bf16 v[220:223], a[144:147], a[120:123], v[220:223]\n" \
"v_mfma_f32_16x16x32_bf16 v[252:255], a[152:155], a[120:123], v[252:255]\n" \
"s_add_u32 m0, s79, 114688\n" \
"s_mov_b32 s74, 656384\n" \
"v_mfma_f32_16x16x32_bf16 v[252:255], a[156:159], a[124:127], v[252:255]\n" \
"buffer_load_dwordx4 v15, s[92:95], s74 offen offset:0 lds\n" \
"v_mfma_f32_16x16x32_bf16 v[248:251], a[156:159], a[116:119], v[248:251]\n" \
"v_mfma_f32_16x16x32_bf16 v[248:251], a[152:155], a[112:115], v[248:251]\n" \
"v_mfma_f32_16x16x32_bf16 v[244:247], a[152:155], a[104:107], v[244:247]\n" \
"s_mov_b32 s74, 655488\n" \
"v_mfma_f32_16x16x32_bf16 v[244:247], a[156:159], a[108:111], v[244:247]\n" \
"buffer_load_dwordx4 v15, s[92:95], s74 offen offset:1024 lds\n" \
"v_mfma_f32_16x16x32_bf16 v[240:243], a[156:159], a[100:103], v[240:243]\n" \
"v_mfma_f32_16x16x32_bf16 v[240:243], a[152:155], a[96:99], v[240:243]\n" \
"v_mfma_f32_16x16x32_bf16 v[236:239], a[152:155], a[88:91], v[236:239]\n" \
"s_mov_b32 s74, 670720\n" \
"v_mfma_f32_16x16x32_bf16 v[236:239], a[156:159], a[92:95], v[236:239]\n" \
"buffer_load_dwordx4 v15, s[92:95], s74 offen offset:2048 lds\n" \
"v_mfma_f32_16x16x32_bf16 v[232:235], a[156:159], a[84:87], v[232:235]\n" \
"v_mfma_f32_16x16x32_bf16 v[232:235], a[152:155], a[80:83], v[232:235]\n" \
"v_mfma_f32_16x16x32_bf16 v[228:231], a[152:155], a[72:75], v[228:231]\n" \
"s_mov_b32 s74, 669824\n" \
"v_mfma_f32_16x16x32_bf16 v[228:231], a[156:159], a[76:79], v[228:231]\n" \
"buffer_load_dwordx4 v15, s[92:95], s74 offen offset:3072 lds\n" \
"v_mfma_f32_16x16x32_bf16 v[224:227], a[156:159], a[68:71], v[224:227]\n" \
"v_mfma_f32_16x16x32_bf16 v[224:227], a[152:155], a[64:67], v[224:227]\n" \
"s_mov_b32 s44, 0xbfb8aa3b\n" \
"s_mov_b32 s45, 0xbfb8aa3b\n" \
"s_mov_b32 s46, 1.0\n" \
"s_mov_b32 s47, 1.0\n" \
"s_mov_b32 s48, 0x3fb8aa3b\n" \
"s_mov_b32 s49, 0x3fb8aa3b\n" \
"s_mov_b32 s50, 0x358637bd\n" \
"s_lshl_b32 s74, s14, 11\n" \
"s_add_u32 s74, s74, 384\n" \
"v_lshrrev_b32 v30, 1, v14\n" \
"v_and_b32 v30, 56, v30\n" \
"v_add_u32 v30, s74, v30\n" \
"s_nop 1\n" \
"L_epi_gu:\n" \
"s_cmp_eq_u32 s78, 2\n" \
"s_cbranch_scc1 L_nomix_%=\n" \
"v_pk_mul_f32 v[20:21], v[192:193], s[44:45]\n" \
"v_pk_mul_f32 v[22:23], v[194:195], s[44:45]\n" \
"v_exp_f32 v20, v20\n" \
"v_exp_f32 v21, v21\n" \
"v_exp_f32 v22, v22\n" \
"v_exp_f32 v23, v23\n" \
"v_pk_mul_f32 v[24:25], v[196:197], s[44:45]\n" \
"v_pk_mul_f32 v[26:27], v[198:199], s[44:45]\n" \
"v_exp_f32 v24, v24\n" \
"v_exp_f32 v25, v25\n" \
"v_exp_f32 v26, v26\n" \
"v_exp_f32 v27, v27\n" \
"v_pk_add_f32 v[20:21], v[20:21], s[46:47]\n" \
"v_pk_add_f32 v[22:23], v[22:23], s[46:47]\n" \
"v_pk_add_f32 v[24:25], v[24:25], s[46:47]\n" \
"v_pk_add_f32 v[26:27], v[26:27], s[46:47]\n" \
"v_rcp_f32 v20, v20\n" \
"v_pk_mul_f32 v[192:193], v[192:193], v[224:225]\n" \
"v_rcp_f32 v21, v21\n" \
"v_pk_mul_f32 v[194:195], v[194:195], v[226:227]\n" \
"v_rcp_f32 v22, v22\n" \
"v_rcp_f32 v23, v23\n" \
"v_rcp_f32 v24, v24\n" \
"v_pk_mul_f32 v[196:197], v[196:197], v[228:229]\n" \
"v_rcp_f32 v25, v25\n" \
"v_pk_mul_f32 v[198:199], v[198:199], v[230:231]\n" \
"v_rcp_f32 v26, v26\n" \
"v_rcp_f32 v27, v27\n" \
"v_pk_mul_f32 v[192:193], v[192:193], v[20:21]\n" \
"v_pk_mul_f32 v[196:197], v[196:197], v[24:25]\n" \
"v_pk_mul_f32 v[194:195], v[194:195], v[22:23]\n" \
"v_pk_mul_f32 v[198:199], v[198:199], v[26:27]\n" \
"v_cvt_pk_bf16_f32 v192, v192, v193\n" \
"v_cvt_pk_bf16_f32 v193, v194, v195\n" \
"v_cvt_pk_bf16_f32 v196, v196, v197\n" \
"v_cvt_pk_bf16_f32 v197, v198, v199\n" \
"v_pk_mul_f32 v[20:21], v[200:201], s[44:45]\n" \
"v_pk_mul_f32 v[22:23], v[202:203], s[44:45]\n" \
"v_exp_f32 v20, v20\n" \
"v_exp_f32 v21, v21\n" \
"v_exp_f32 v22, v22\n" \
"v_exp_f32 v23, v23\n" \
"v_pk_mul_f32 v[24:25], v[204:205], s[44:45]\n" \
"v_pk_mul_f32 v[26:27], v[206:207], s[44:45]\n" \
"v_exp_f32 v24, v24\n" \
"v_exp_f32 v25, v25\n" \
"v_exp_f32 v26, v26\n" \
"v_exp_f32 v27, v27\n" \
"v_pk_add_f32 v[20:21], v[20:21], s[46:47]\n" \
"v_pk_add_f32 v[22:23], v[22:23], s[46:47]\n" \
"v_pk_add_f32 v[24:25], v[24:25], s[46:47]\n" \
"v_pk_add_f32 v[26:27], v[26:27], s[46:47]\n" \
"v_rcp_f32 v20, v20\n" \
"v_pk_mul_f32 v[200:201], v[200:201], v[232:233]\n" \
"v_rcp_f32 v21, v21\n" \
"v_pk_mul_f32 v[202:203], v[202:203], v[234:235]\n" \
"v_rcp_f32 v22, v22\n" \
"v_rcp_f32 v23, v23\n" \
"v_rcp_f32 v24, v24\n" \
"v_pk_mul_f32 v[204:205], v[204:205], v[236:237]\n" \
"v_rcp_f32 v25, v25\n" \
"v_pk_mul_f32 v[206:207], v[206:207], v[238:239]\n" \
"v_rcp_f32 v26, v26\n" \
"v_rcp_f32 v27, v27\n" \
"v_pk_mul_f32 v[200:201], v[200:201], v[20:21]\n" \
"v_pk_mul_f32 v[204:205], v[204:205], v[24:25]\n" \
"v_pk_mul_f32 v[202:203], v[202:203], v[22:23]\n" \
"v_pk_mul_f32 v[206:207], v[206:207], v[26:27]\n" \
"v_cvt_pk_bf16_f32 v200, v200, v201\n" \
"v_cvt_pk_bf16_f32 v201, v202, v203\n" \
"v_cvt_pk_bf16_f32 v204, v204, v205\n" \
"v_cvt_pk_bf16_f32 v205, v206, v207\n" \
"v_pk_mul_f32 v[20:21], v[208:209], s[44:45]\n" \
"v_pk_mul_f32 v[22:23], v[210:211], s[44:45]\n" \
"v_exp_f32 v20, v20\n" \
"v_exp_f32 v21, v21\n" \
"v_exp_f32 v22, v22\n" \
"v_exp_f32 v23, v23\n" \
"v_pk_mul_f32 v[24:25], v[212:213], s[44:45]\n" \
"v_pk_mul_f32 v[26:27], v[214:215], s[44:45]\n" \
"v_exp_f32 v24, v24\n" \
"v_exp_f32 v25, v25\n" \
"v_exp_f32 v26, v26\n" \
"v_exp_f32 v27, v27\n" \
"v_pk_add_f32 v[20:21], v[20:21], s[46:47]\n" \
"v_pk_add_f32 v[22:23], v[22:23], s[46:47]\n" \
"v_pk_add_f32 v[24:25], v[24:25], s[46:47]\n" \
"v_pk_add_f32 v[26:27], v[26:27], s[46:47]\n" \
"v_rcp_f32 v20, v20\n" \
"v_pk_mul_f32 v[208:209], v[208:209], v[240:241]\n" \
"v_rcp_f32 v21, v21\n" \
"v_pk_mul_f32 v[210:211], v[210:211], v[242:243]\n" \
"v_rcp_f32 v22, v22\n" \
"v_rcp_f32 v23, v23\n" \
"v_rcp_f32 v24, v24\n" \
"v_pk_mul_f32 v[212:213], v[212:213], v[244:245]\n" \
"v_rcp_f32 v25, v25\n" \
"v_pk_mul_f32 v[214:215], v[214:215], v[246:247]\n" \
"v_rcp_f32 v26, v26\n" \
"v_rcp_f32 v27, v27\n" \
"v_pk_mul_f32 v[208:209], v[208:209], v[20:21]\n" \
"v_pk_mul_f32 v[212:213], v[212:213], v[24:25]\n" \
"v_pk_mul_f32 v[210:211], v[210:211], v[22:23]\n" \
"v_pk_mul_f32 v[214:215], v[214:215], v[26:27]\n" \
"v_cvt_pk_bf16_f32 v208, v208, v209\n" \
"v_cvt_pk_bf16_f32 v209, v210, v211\n" \
"v_cvt_pk_bf16_f32 v212, v212, v213\n" \
"v_cvt_pk_bf16_f32 v213, v214, v215\n" \
"v_pk_mul_f32 v[20:21], v[216:217], s[44:45]\n" \
"v_pk_mul_f32 v[22:23], v[218:219], s[44:45]\n" \
"v_exp_f32 v20, v20\n" \
"v_exp_f32 v21, v21\n" \
"v_exp_f32 v22, v22\n" \
"v_exp_f32 v23, v23\n" \
"v_pk_mul_f32 v[24:25], v[220:221], s[44:45]\n" \
"v_pk_mul_f32 v[26:27], v[222:223], s[44:45]\n" \
"v_exp_f32 v24, v24\n" \
"v_exp_f32 v25, v25\n" \
"v_exp_f32 v26, v26\n" \
"v_exp_f32 v27, v27\n" \
"v_pk_add_f32 v[20:21], v[20:21], s[46:47]\n" \
"v_pk_add_f32 v[22:23], v[22:23], s[46:47]\n" \
"v_pk_add_f32 v[24:25], v[24:25], s[46:47]\n" \
"v_pk_add_f32 v[26:27], v[26:27], s[46:47]\n" \
"v_rcp_f32 v20, v20\n" \
"v_pk_mul_f32 v[216:217], v[216:217], v[248:249]\n" \
"v_rcp_f32 v21, v21\n" \
"v_pk_mul_f32 v[218:219], v[218:219], v[250:251]\n" \
"v_rcp_f32 v22, v22\n" \
"v_rcp_f32 v23, v23\n" \
"v_rcp_f32 v24, v24\n" \
"v_pk_mul_f32 v[220:221], v[220:221], v[252:253]\n" \
"v_rcp_f32 v25, v25\n" \
"v_pk_mul_f32 v[222:223], v[222:223], v[254:255]\n" \
"v_rcp_f32 v26, v26\n" \
"v_rcp_f32 v27, v27\n" \
"v_pk_mul_f32 v[216:217], v[216:217], v[20:21]\n" \
"v_pk_mul_f32 v[220:221], v[220:221], v[24:25]\n" \
"v_pk_mul_f32 v[218:219], v[218:219], v[22:23]\n" \
"v_pk_mul_f32 v[222:223], v[222:223], v[26:27]\n" \
"v_cvt_pk_bf16_f32 v216, v216, v217\n" \
"v_cvt_pk_bf16_f32 v217, v218, v219\n" \
"v_cvt_pk_bf16_f32 v220, v220, v221\n" \
"v_cvt_pk_bf16_f32 v221, v222, v223\n" \
"s_cmp_lg_u32 s6, 0\n" \
"s_cbranch_scc1 L_mx1_%=\n" \
"ds_write_b64 v18, v[208:209] offset:0\n" \
"ds_write_b64 v18, v[212:213] offset:512\n" \
"ds_write_b64 v18, v[216:217] offset:1024\n" \
"ds_write_b64 v18, v[220:221] offset:1536\n" \
"s_branch L_mx2_%=\n" \
"L_mx1_%=:\n" \
"ds_write_b64 v18, v[192:193] offset:0\n" \
"ds_write_b64 v18, v[196:197] offset:512\n" \
"ds_write_b64 v18, v[200:201] offset:1024\n" \
"ds_write_b64 v18, v[204:205] offset:1536\n" \
"L_mx2_%=:\n" \
"v_xor_b32 v18, 0x800, v18\n" \
"L_nomix_%=:\n" \
"v_pk_mul_f32 v[20:21], v[32:33], s[44:45]\n" \
"v_pk_mul_f32 v[22:23], v[34:35], s[44:45]\n" \
"v_exp_f32 v20, v20\n" \
"v_exp_f32 v21, v21\n" \
"v_exp_f32 v22, v22\n" \
"v_exp_f32 v23, v23\n" \
"v_pk_mul_f32 v[24:25], v[64:65], s[44:45]\n" \
"v_pk_mul_f32 v[26:27], v[66:67], s[44:45]\n" \
"v_exp_f32 v24, v24\n" \
"v_accvgpr_read_b32 v4, a160\n" \
"v_exp_f32 v25, v25\n" \
"v_accvgpr_read_b32 v5, a161\n" \
"v_exp_f32 v26, v26\n" \
"v_accvgpr_read_b32 v6, a162\n" \
"v_exp_f32 v27, v27\n" \
"v_accvgpr_read_b32 v7, a163\n" \
"v_pk_add_f32 v[20:21], v[20:21], s[46:47]\n" \
"v_pk_add_f32 v[22:23], v[22:23], s[46:47]\n" \
"v_pk_add_f32 v[24:25], v[24:25], s[46:47]\n" \
"v_pk_add_f32 v[26:27], v[26:27], s[46:47]\n" \
"v_rcp_f32 v20, v20\n" \
"v_pk_mul_f32 v[32:33], v[32:33], v[160:161]\n" \
"v_rcp_f32 v21, v21\n" \
"v_pk_mul_f32 v[34:35], v[34:35], v[162:163]\n" \
"v_rcp_f32 v22, v22\n" \
"v_rcp_f32 v23, v23\n" \
"v_rcp_f32 v24, v24\n" \
"v_pk_mul_f32 v[64:65], v[64:65], v[4:5]\n" \
"v_rcp_f32 v25, v25\n" \
"v_pk_mul_f32 v[66:67], v[66:67], v[6:7]\n" \
"v_rcp_f32 v26, v26\n" \
"v_rcp_f32 v27, v27\n" \
"v_pk_mul_f32 v[32:33], v[32:33], v[20:21]\n" \
"v_pk_mul_f32 v[64:65], v[64:65], v[24:25]\n" \
"v_pk_mul_f32 v[34:35], v[34:35], v[22:23]\n" \
"v_pk_mul_f32 v[66:67], v[66:67], v[26:27]\n" \
"v_cvt_pk_bf16_f32 v32, v32, v33\n" \
"v_cvt_pk_bf16_f32 v33, v34, v35\n" \
"v_cvt_pk_bf16_f32 v34, v64, v65\n" \
"v_cvt_pk_bf16_f32 v35, v66, v67\n" \
"buffer_store_dwordx4 v[32:35], v14, s[80:83], s28 offen offset:0 sc1\n" \
"s_cmp_eq_u32 s78, 2\n" \
"s_cbranch_scc1 L_nomix0_%=\n" \
"s_waitcnt lgkmcnt(0)\n" \
"s_barrier\n" \
"s_cmp_lg_u32 s6, 0\n" \
"s_cbranch_scc1 L_mx3_%=\n" \
"ds_read_b64 v[194:195], v18 offset:0\n" \
"ds_read_b64 v[198:199], v18 offset:512\n" \
"ds_read_b64 v[202:203], v18 offset:1024\n" \
"ds_read_b64 v[206:207], v18 offset:1536\n" \
"s_branch L_mx4_%=\n" \
"L_mx3_%=:\n" \
"ds_read_b64 v[192:193], v18 offset:0\n" \
"v_mov_b32 v194, v208\n" \
"v_mov_b32 v195, v209\n" \
"ds_read_b64 v[196:197], v18 offset:512\n" \
"v_mov_b32 v198, v212\n" \
"v_mov_b32 v199, v213\n" \
"ds_read_b64 v[200:201], v18 offset:1024\n" \
"v_mov_b32 v202, v216\n" \
"v_mov_b32 v203, v217\n" \
"ds_read_b64 v[204:205], v18 offset:1536\n" \
"v_mov_b32 v206, v220\n" \
"v_mov_b32 v207, v221\n" \
"L_mx4_%=:\n" \
"L_nomix0_%=:\n" \
"v_pk_mul_f32 v[20:21], v[96:97], s[44:45]\n" \
"v_pk_mul_f32 v[22:23], v[98:99], s[44:45]\n" \
"v_exp_f32 v20, v20\n" \
"v_accvgpr_read_b32 v0, a192\n" \
"v_exp_f32 v21, v21\n" \
"v_accvgpr_read_b32 v1, a193\n" \
"v_exp_f32 v22, v22\n" \
"v_accvgpr_read_b32 v2, a194\n" \
"v_exp_f32 v23, v23\n" \
"v_accvgpr_read_b32 v3, a195\n" \
"v_pk_mul_f32 v[24:25], v[128:129], s[44:45]\n" \
"v_pk_mul_f32 v[26:27], v[130:131], s[44:45]\n" \
"v_exp_f32 v24, v24\n" \
"v_accvgpr_read_b32 v4, a224\n" \
"v_exp_f32 v25, v25\n" \
"v_accvgpr_read_b32 v5, a225\n" \
"v_exp_f32 v26, v26\n" \
"v_accvgpr_read_b32 v6, a226\n" \
"v_exp_f32 v27, v27\n" \
"v_accvgpr_read_b32 v7, a227\n" \
"v_pk_add_f32 v[20:21], v[20:21], s[46:47]\n" \
"v_pk_add_f32 v[22:23], v[22:23], s[46:47]\n" \
"v_pk_add_f32 v[24:25], v[24:25], s[46:47]\n" \
"v_pk_add_f32 v[26:27], v[26:27], s[46:47]\n" \
"v_rcp_f32 v20, v20\n" \
"v_pk_mul_f32 v[96:97], v[96:97], v[0:1]\n" \
"v_rcp_f32 v21, v21\n" \
"v_pk_mul_f32 v[98:99], v[98:99], v[2:3]\n" \
"v_rcp_f32 v22, v22\n" \
"v_rcp_f32 v23, v23\n" \
"v_rcp_f32 v24, v24\n" \
"v_pk_mul_f32 v[128:129], v[128:129], v[4:5]\n" \
"v_rcp_f32 v25, v25\n" \
"v_pk_mul_f32 v[130:131], v[130:131], v[6:7]\n" \
"v_rcp_f32 v26, v26\n" \
"v_rcp_f32 v27, v27\n" \
"v_pk_mul_f32 v[96:97], v[96:97], v[20:21]\n" \
"v_pk_mul_f32 v[128:129], v[128:129], v[24:25]\n" \
"v_pk_mul_f32 v[98:99], v[98:99], v[22:23]\n" \
"v_pk_mul_f32 v[130:131], v[130:131], v[26:27]\n" \
"v_cvt_pk_bf16_f32 v96, v96, v97\n" \
"v_cvt_pk_bf16_f32 v97, v98, v99\n" \
"v_cvt_pk_bf16_f32 v98, v128, v129\n" \
"v_cvt_pk_bf16_f32 v99, v130, v131\n" \
"buffer_store_dwordx4 v[96:99], v14, s[80:83], s28 offen offset:1024 sc1\n" \
"s_cmp_eq_u32 s78, 2\n" \
"s_cbranch_scc1 L_nomix1_%=\n" \
"s_waitcnt lgkmcnt(0)\n" \
"buffer_store_dwordx4 v[192:195], v14, s[80:83], s29 offen offset:0 sc1\n" \
"buffer_store_dwordx4 v[196:199], v14, s[80:83], s29 offen offset:2048 sc1\n" \
"s_add_u32 s74, s29, 4096\n" \
"buffer_store_dwordx4 v[200:203], v14, s[80:83], s74 offen offset:0 sc1\n" \
"s_add_u32 s74, s29, 4096\n" \
"buffer_store_dwordx4 v[204:207], v14, s[80:83], s74 offen offset:2048 sc1\n" \
"L_nomix1_%=:\n" \
"v_pk_mul_f32 v[20:21], v[36:37], s[44:45]\n" \
"v_pk_mul_f32 v[22:23], v[38:39], s[44:45]\n" \
"v_exp_f32 v20, v20\n" \
"v_exp_f32 v21, v21\n" \
"v_exp_f32 v22, v22\n" \
"v_exp_f32 v23, v23\n" \
"v_pk_mul_f32 v[24:25], v[68:69], s[44:45]\n" \
"v_pk_mul_f32 v[26:27], v[70:71], s[44:45]\n" \
"v_exp_f32 v24, v24\n" \
"v_accvgpr_read_b32 v4, a164\n" \
"v_exp_f32 v25, v25\n" \
"v_accvgpr_read_b32 v5, a165\n" \
"v_exp_f32 v26, v26\n" \
"v_accvgpr_read_b32 v6, a166\n" \
"v_exp_f32 v27, v27\n" \
"v_accvgpr_read_b32 v7, a167\n" \
"v_pk_add_f32 v[20:21], v[20:21], s[46:47]\n" \
"v_pk_add_f32 v[22:23], v[22:23], s[46:47]\n" \
"v_pk_add_f32 v[24:25], v[24:25], s[46:47]\n" \
"v_pk_add_f32 v[26:27], v[26:27], s[46:47]\n" \
"v_rcp_f32 v20, v20\n" \
"v_pk_mul_f32 v[36:37], v[36:37], v[164:165]\n" \
"v_rcp_f32 v21, v21\n" \
"v_pk_mul_f32 v[38:39], v[38:39], v[166:167]\n" \
"v_rcp_f32 v22, v22\n" \
"v_rcp_f32 v23, v23\n" \
"v_rcp_f32 v24, v24\n" \
"v_pk_mul_f32 v[68:69], v[68:69], v[4:5]\n" \
"v_rcp_f32 v25, v25\n" \
"v_pk_mul_f32 v[70:71], v[70:71], v[6:7]\n" \
"v_rcp_f32 v26, v26\n" \
"v_rcp_f32 v27, v27\n" \
"v_pk_mul_f32 v[36:37], v[36:37], v[20:21]\n" \
"v_pk_mul_f32 v[68:69], v[68:69], v[24:25]\n" \
"v_pk_mul_f32 v[38:39], v[38:39], v[22:23]\n" \
"v_pk_mul_f32 v[70:71], v[70:71], v[26:27]\n" \
"v_cvt_pk_bf16_f32 v36, v36, v37\n" \
"v_cvt_pk_bf16_f32 v37, v38, v39\n" \
"v_cvt_pk_bf16_f32 v38, v68, v69\n" \
"v_cvt_pk_bf16_f32 v39, v70, v71\n" \
"buffer_store_dwordx4 v[36:39], v14, s[80:83], s28 offen offset:2048 sc1\n" \
"v_pk_mul_f32 v[20:21], v[100:101], s[44:45]\n" \
"v_pk_mul_f32 v[22:23], v[102:103], s[44:45]\n" \
"v_exp_f32 v20, v20\n" \
"v_accvgpr_read_b32 v0, a196\n" \
"v_exp_f32 v21, v21\n" \
"v_accvgpr_read_b32 v1, a197\n" \
"v_exp_f32 v22, v22\n" \
"v_accvgpr_read_b32 v2, a198\n" \
"v_exp_f32 v23, v23\n" \
"v_accvgpr_read_b32 v3, a199\n" \
"v_pk_mul_f32 v[24:25], v[132:133], s[44:45]\n" \
"v_pk_mul_f32 v[26:27], v[134:135], s[44:45]\n" \
"v_exp_f32 v24, v24\n" \
"v_accvgpr_read_b32 v4, a228\n" \
"v_exp_f32 v25, v25\n" \
"v_accvgpr_read_b32 v5, a229\n" \
"v_exp_f32 v26, v26\n" \
"v_accvgpr_read_b32 v6, a230\n" \
"v_exp_f32 v27, v27\n" \
"v_accvgpr_read_b32 v7, a231\n" \
"v_pk_add_f32 v[20:21], v[20:21], s[46:47]\n" \
"v_pk_add_f32 v[22:23], v[22:23], s[46:47]\n" \
"v_pk_add_f32 v[24:25], v[24:25], s[46:47]\n" \
"v_pk_add_f32 v[26:27], v[26:27], s[46:47]\n" \
"v_rcp_f32 v20, v20\n" \
"v_pk_mul_f32 v[100:101], v[100:101], v[0:1]\n" \
"v_rcp_f32 v21, v21\n" \
"v_pk_mul_f32 v[102:103], v[102:103], v[2:3]\n" \
"v_rcp_f32 v22, v22\n" \
"v_rcp_f32 v23, v23\n" \
"v_rcp_f32 v24, v24\n" \
"v_pk_mul_f32 v[132:133], v[132:133], v[4:5]\n" \
"v_rcp_f32 v25, v25\n" \
"v_pk_mul_f32 v[134:135], v[134:135], v[6:7]\n" \
"v_rcp_f32 v26, v26\n" \
"v_rcp_f32 v27, v27\n" \
"v_pk_mul_f32 v[100:101], v[100:101], v[20:21]\n" \
"v_pk_mul_f32 v[132:133], v[132:133], v[24:25]\n" \
"v_pk_mul_f32 v[102:103], v[102:103], v[22:23]\n" \
"v_pk_mul_f32 v[134:135], v[134:135], v[26:27]\n" \
"v_cvt_pk_bf16_f32 v100, v100, v101\n" \
"v_cvt_pk_bf16_f32 v101, v102, v103\n" \
"v_cvt_pk_bf16_f32 v102, v132, v133\n" \
"v_cvt_pk_bf16_f32 v103, v134, v135\n" \
"buffer_store_dwordx4 v[100:103], v14, s[80:83], s28 offen offset:3072 sc1\n" \
"v_pk_mul_f32 v[20:21], v[40:41], s[44:45]\n" \
"v_pk_mul_f32 v[22:23], v[42:43], s[44:45]\n" \
"v_exp_f32 v20, v20\n" \
"v_exp_f32 v21, v21\n" \
"v_exp_f32 v22, v22\n" \
"v_exp_f32 v23, v23\n" \
"v_pk_mul_f32 v[24:25], v[72:73], s[44:45]\n" \
"v_pk_mul_f32 v[26:27], v[74:75], s[44:45]\n" \
"v_exp_f32 v24, v24\n" \
"v_accvgpr_read_b32 v4, a168\n" \
"v_exp_f32 v25, v25\n" \
"v_accvgpr_read_b32 v5, a169\n" \
"v_exp_f32 v26, v26\n" \
"v_accvgpr_read_b32 v6, a170\n" \
"v_exp_f32 v27, v27\n" \
"v_accvgpr_read_b32 v7, a171\n" \
"v_pk_add_f32 v[20:21], v[20:21], s[46:47]\n" \
"v_pk_add_f32 v[22:23], v[22:23], s[46:47]\n" \
"v_pk_add_f32 v[24:25], v[24:25], s[46:47]\n" \
"v_pk_add_f32 v[26:27], v[26:27], s[46:47]\n" \
"v_rcp_f32 v20, v20\n" \
"v_pk_mul_f32 v[40:41], v[40:41], v[168:169]\n" \
"v_rcp_f32 v21, v21\n" \
"v_pk_mul_f32 v[42:43], v[42:43], v[170:171]\n" \
"v_rcp_f32 v22, v22\n" \
"v_rcp_f32 v23, v23\n" \
"v_rcp_f32 v24, v24\n" \
"v_pk_mul_f32 v[72:73], v[72:73], v[4:5]\n" \
"v_rcp_f32 v25, v25\n" \
"v_pk_mul_f32 v[74:75], v[74:75], v[6:7]\n" \
"v_rcp_f32 v26, v26\n" \
"v_rcp_f32 v27, v27\n" \
"v_pk_mul_f32 v[40:41], v[40:41], v[20:21]\n" \
"v_pk_mul_f32 v[72:73], v[72:73], v[24:25]\n" \
"v_pk_mul_f32 v[42:43], v[42:43], v[22:23]\n" \
"v_pk_mul_f32 v[74:75], v[74:75], v[26:27]\n" \
"v_cvt_pk_bf16_f32 v40, v40, v41\n" \
"v_cvt_pk_bf16_f32 v41, v42, v43\n" \
"v_cvt_pk_bf16_f32 v42, v72, v73\n" \
"v_cvt_pk_bf16_f32 v43, v74, v75\n" \
"s_add_u32 s74, s28, 4096\n" \
"buffer_store_dwordx4 v[40:43], v14, s[80:83], s74 offen offset:0 sc1\n" \
"v_pk_mul_f32 v[20:21], v[104:105], s[44:45]\n" \
"v_pk_mul_f32 v[22:23], v[106:107], s[44:45]\n" \
"v_exp_f32 v20, v20\n" \
"v_accvgpr_read_b32 v0, a200\n" \
"v_exp_f32 v21, v21\n" \
"v_accvgpr_read_b32 v1, a201\n" \
"v_exp_f32 v22, v22\n" \
"v_accvgpr_read_b32 v2, a202\n" \
"v_exp_f32 v23, v23\n" \
"v_accvgpr_read_b32 v3, a203\n" \
"v_pk_mul_f32 v[24:25], v[136:137], s[44:45]\n" \
"v_pk_mul_f32 v[26:27], v[138:139], s[44:45]\n" \
"v_exp_f32 v24, v24\n" \
"v_accvgpr_read_b32 v4, a232\n" \
"v_exp_f32 v25, v25\n" \
"v_accvgpr_read_b32 v5, a233\n" \
"v_exp_f32 v26, v26\n" \
"v_accvgpr_read_b32 v6, a234\n" \
"v_exp_f32 v27, v27\n" \
"v_accvgpr_read_b32 v7, a235\n" \
"v_pk_add_f32 v[20:21], v[20:21], s[46:47]\n" \
"v_pk_add_f32 v[22:23], v[22:23], s[46:47]\n" \
"v_pk_add_f32 v[24:25], v[24:25], s[46:47]\n" \
"v_pk_add_f32 v[26:27], v[26:27], s[46:47]\n" \
"v_rcp_f32 v20, v20\n" \
"v_pk_mul_f32 v[104:105], v[104:105], v[0:1]\n" \
"v_rcp_f32 v21, v21\n" \
"v_pk_mul_f32 v[106:107], v[106:107], v[2:3]\n" \
"v_rcp_f32 v22, v22\n" \
"v_rcp_f32 v23, v23\n" \
"v_rcp_f32 v24, v24\n" \
"v_pk_mul_f32 v[136:137], v[136:137], v[4:5]\n" \
"v_rcp_f32 v25, v25\n" \
"v_pk_mul_f32 v[138:139], v[138:139], v[6:7]\n" \
"v_rcp_f32 v26, v26\n" \
"v_rcp_f32 v27, v27\n" \
"v_pk_mul_f32 v[104:105], v[104:105], v[20:21]\n" \
"v_pk_mul_f32 v[136:137], v[136:137], v[24:25]\n" \
"v_pk_mul_f32 v[106:107], v[106:107], v[22:23]\n" \
"v_pk_mul_f32 v[138:139], v[138:139], v[26:27]\n" \
"v_cvt_pk_bf16_f32 v104, v104, v105\n" \
"v_cvt_pk_bf16_f32 v105, v106, v107\n" \
"v_cvt_pk_bf16_f32 v106, v136, v137\n" \
"v_cvt_pk_bf16_f32 v107, v138, v139\n" \
"s_add_u32 s74, s28, 4096\n" \
"buffer_store_dwordx4 v[104:107], v14, s[80:83], s74 offen offset:1024 sc1\n" \
"v_pk_mul_f32 v[20:21], v[44:45], s[44:45]\n" \
"v_pk_mul_f32 v[22:23], v[46:47], s[44:45]\n" \
"v_exp_f32 v20, v20\n" \
"v_exp_f32 v21, v21\n" \
"v_exp_f32 v22, v22\n" \
"v_exp_f32 v23, v23\n" \
"v_pk_mul_f32 v[24:25], v[76:77], s[44:45]\n" \
"v_pk_mul_f32 v[26:27], v[78:79], s[44:45]\n" \
"v_exp_f32 v24, v24\n" \
"v_accvgpr_read_b32 v4, a172\n" \
"v_exp_f32 v25, v25\n" \
"v_accvgpr_read_b32 v5, a173\n" \
"v_exp_f32 v26, v26\n" \
"v_accvgpr_read_b32 v6, a174\n" \
"v_exp_f32 v27, v27\n" \
"v_accvgpr_read_b32 v7, a175\n" \
"v_pk_add_f32 v[20:21], v[20:21], s[46:47]\n" \
"v_pk_add_f32 v[22:23], v[22:23], s[46:47]\n" \
"v_pk_add_f32 v[24:25], v[24:25], s[46:47]\n" \
"v_pk_add_f32 v[26:27], v[26:27], s[46:47]\n" \
"v_rcp_f32 v20, v20\n" \
"v_pk_mul_f32 v[44:45], v[44:45], v[172:173]\n" \
"v_rcp_f32 v21, v21\n" \
"v_pk_mul_f32 v[46:47], v[46:47], v[174:175]\n" \
"v_rcp_f32 v22, v22\n" \
"v_rcp_f32 v23, v23\n" \
"v_rcp_f32 v24, v24\n" \
"v_pk_mul_f32 v[76:77], v[76:77], v[4:5]\n" \
"v_rcp_f32 v25, v25\n" \
"v_pk_mul_f32 v[78:79], v[78:79], v[6:7]\n" \
"v_rcp_f32 v26, v26\n" \
"v_rcp_f32 v27, v27\n" \
"v_pk_mul_f32 v[44:45], v[44:45], v[20:21]\n" \
"v_pk_mul_f32 v[76:77], v[76:77], v[24:25]\n" \
"v_pk_mul_f32 v[46:47], v[46:47], v[22:23]\n" \
"v_pk_mul_f32 v[78:79], v[78:79], v[26:27]\n" \
"v_cvt_pk_bf16_f32 v44, v44, v45\n" \
"v_cvt_pk_bf16_f32 v45, v46, v47\n" \
"v_cvt_pk_bf16_f32 v46, v76, v77\n" \
"v_cvt_pk_bf16_f32 v47, v78, v79\n" \
"s_add_u32 s74, s28, 4096\n" \
"buffer_store_dwordx4 v[44:47], v14, s[80:83], s74 offen offset:2048 sc1\n" \
"s_cmp_eq_u32 s78, 2\n" \
"s_cbranch_scc1 L_noearly_%=\n" \
"s_waitcnt vmcnt(5)\n" \
"s_barrier\n" \
"buffer_store_dwordx2 v[28:29], v16, s[96:99], 0 offen offset:128 sc1\n" \
"L_noearly_%=:\n" \
"v_pk_mul_f32 v[20:21], v[108:109], s[44:45]\n" \
"v_pk_mul_f32 v[22:23], v[110:111], s[44:45]\n" \
"v_exp_f32 v20, v20\n" \
"v_accvgpr_read_b32 v0, a204\n" \
"v_exp_f32 v21, v21\n" \
"v_accvgpr_read_b32 v1, a205\n" \
"v_exp_f32 v22, v22\n" \
"v_accvgpr_read_b32 v2, a206\n" \
"v_exp_f32 v23, v23\n" \
"v_accvgpr_read_b32 v3, a207\n" \
"v_pk_mul_f32 v[24:25], v[140:141], s[44:45]\n" \
"v_pk_mul_f32 v[26:27], v[142:143], s[44:45]\n" \
"v_exp_f32 v24, v24\n" \
"v_accvgpr_read_b32 v4, a236\n" \
"v_exp_f32 v25, v25\n" \
"v_accvgpr_read_b32 v5, a237\n" \
"v_exp_f32 v26, v26\n" \
"v_accvgpr_read_b32 v6, a238\n" \
"v_exp_f32 v27, v27\n" \
"v_accvgpr_read_b32 v7, a239\n" \
"v_pk_add_f32 v[20:21], v[20:21], s[46:47]\n" \
"v_pk_add_f32 v[22:23], v[22:23], s[46:47]\n" \
"v_pk_add_f32 v[24:25], v[24:25], s[46:47]\n" \
"v_pk_add_f32 v[26:27], v[26:27], s[46:47]\n" \
"v_rcp_f32 v20, v20\n" \
"v_pk_mul_f32 v[108:109], v[108:109], v[0:1]\n" \
"v_rcp_f32 v21, v21\n" \
"v_pk_mul_f32 v[110:111], v[110:111], v[2:3]\n" \
"v_rcp_f32 v22, v22\n" \
"v_rcp_f32 v23, v23\n" \
"v_rcp_f32 v24, v24\n" \
"v_pk_mul_f32 v[140:141], v[140:141], v[4:5]\n" \
"v_rcp_f32 v25, v25\n" \
"v_pk_mul_f32 v[142:143], v[142:143], v[6:7]\n" \
"v_rcp_f32 v26, v26\n" \
"v_rcp_f32 v27, v27\n" \
"v_pk_mul_f32 v[108:109], v[108:109], v[20:21]\n" \
"v_pk_mul_f32 v[140:141], v[140:141], v[24:25]\n" \
"v_pk_mul_f32 v[110:111], v[110:111], v[22:23]\n" \
"v_pk_mul_f32 v[142:143], v[142:143], v[26:27]\n" \
"v_cvt_pk_bf16_f32 v108, v108, v109\n" \
"v_cvt_pk_bf16_f32 v109, v110, v111\n" \
"v_cvt_pk_bf16_f32 v110, v140, v141\n" \
"v_cvt_pk_bf16_f32 v111, v142, v143\n" \
"s_add_u32 s74, s28, 4096\n" \
"buffer_store_dwordx4 v[108:111], v14, s[80:83], s74 offen offset:3072 sc1\n" \
"v_pk_mul_f32 v[20:21], v[48:49], s[44:45]\n" \
"v_pk_mul_f32 v[22:23], v[50:51], s[44:45]\n" \
"v_exp_f32 v20, v20\n" \
"v_exp_f32 v21, v21\n" \
"v_exp_f32 v22, v22\n" \
"v_exp_f32 v23, v23\n" \
"v_pk_mul_f32 v[24:25], v[80:81], s[44:45]\n" \
"v_pk_mul_f32 v[26:27], v[82:83], s[44:45]\n" \
"v_exp_f32 v24, v24\n" \
"v_accvgpr_read_b32 v4, a176\n" \
"v_exp_f32 v25, v25\n" \
"v_accvgpr_read_b32 v5, a177\n" \
"v_exp_f32 v26, v26\n" \
"v_accvgpr_read_b32 v6, a178\n" \
"v_exp_f32 v27, v27\n" \
"v_accvgpr_read_b32 v7, a179\n" \
"v_pk_add_f32 v[20:21], v[20:21], s[46:47]\n" \
"v_pk_add_f32 v[22:23], v[22:23], s[46:47]\n" \
"v_pk_add_f32 v[24:25], v[24:25], s[46:47]\n" \
"v_pk_add_f32 v[26:27], v[26:27], s[46:47]\n" \
"v_rcp_f32 v20, v20\n" \
"v_pk_mul_f32 v[48:49], v[48:49], v[176:177]\n" \
"v_rcp_f32 v21, v21\n" \
"v_pk_mul_f32 v[50:51], v[50:51], v[178:179]\n" \
"v_rcp_f32 v22, v22\n" \
"v_rcp_f32 v23, v23\n" \
"v_rcp_f32 v24, v24\n" \
"v_pk_mul_f32 v[80:81], v[80:81], v[4:5]\n" \
"v_rcp_f32 v25, v25\n" \
"v_pk_mul_f32 v[82:83], v[82:83], v[6:7]\n" \
"v_rcp_f32 v26, v26\n" \
"v_rcp_f32 v27, v27\n" \
"v_pk_mul_f32 v[48:49], v[48:49], v[20:21]\n" \
"v_pk_mul_f32 v[80:81], v[80:81], v[24:25]\n" \
"v_pk_mul_f32 v[50:51], v[50:51], v[22:23]\n" \
"v_pk_mul_f32 v[82:83], v[82:83], v[26:27]\n" \
"v_cvt_pk_bf16_f32 v48, v48, v49\n" \
"v_cvt_pk_bf16_f32 v49, v50, v51\n" \
"v_cvt_pk_bf16_f32 v50, v80, v81\n" \
"v_cvt_pk_bf16_f32 v51, v82, v83\n" \
"s_add_u32 s74, s28, 8192\n" \
"buffer_store_dwordx4 v[48:51], v14, s[80:83], s74 offen offset:0 sc1\n" \
"v_pk_mul_f32 v[20:21], v[112:113], s[44:45]\n" \
"v_pk_mul_f32 v[22:23], v[114:115], s[44:45]\n" \
"v_exp_f32 v20, v20\n" \
"v_accvgpr_read_b32 v0, a208\n" \
"v_exp_f32 v21, v21\n" \
"v_accvgpr_read_b32 v1, a209\n" \
"v_exp_f32 v22, v22\n" \
"v_accvgpr_read_b32 v2, a210\n" \
"v_exp_f32 v23, v23\n" \
"v_accvgpr_read_b32 v3, a211\n" \
"v_pk_mul_f32 v[24:25], v[144:145], s[44:45]\n" \
"v_pk_mul_f32 v[26:27], v[146:147], s[44:45]\n" \
"v_exp_f32 v24, v24\n" \
"v_accvgpr_read_b32 v4, a240\n" \
"v_exp_f32 v25, v25\n" \
"v_accvgpr_read_b32 v5, a241\n" \
"v_exp_f32 v26, v26\n" \
"v_accvgpr_read_b32 v6, a242\n" \
"v_exp_f32 v27, v27\n" \
"v_accvgpr_read_b32 v7, a243\n" \
"v_pk_add_f32 v[20:21], v[20:21], s[46:47]\n" \
"v_pk_add_f32 v[22:23], v[22:23], s[46:47]\n" \
"v_pk_add_f32 v[24:25], v[24:25], s[46:47]\n" \
"v_pk_add_f32 v[26:27], v[26:27], s[46:47]\n" \
"v_rcp_f32 v20, v20\n" \
"v_pk_mul_f32 v[112:113], v[112:113], v[0:1]\n" \
"v_rcp_f32 v21, v21\n" \
"v_pk_mul_f32 v[114:115], v[114:115], v[2:3]\n" \
"v_rcp_f32 v22, v22\n" \
"v_rcp_f32 v23, v23\n" \
"v_rcp_f32 v24, v24\n" \
"v_pk_mul_f32 v[144:145], v[144:145], v[4:5]\n" \
"v_rcp_f32 v25, v25\n" \
"v_pk_mul_f32 v[146:147], v[146:147], v[6:7]\n" \
"v_rcp_f32 v26, v26\n" \
"v_rcp_f32 v27, v27\n" \
"v_pk_mul_f32 v[112:113], v[112:113], v[20:21]\n" \
"v_pk_mul_f32 v[144:145], v[144:145], v[24:25]\n" \
"v_pk_mul_f32 v[114:115], v[114:115], v[22:23]\n" \
"v_pk_mul_f32 v[146:147], v[146:147], v[26:27]\n" \
"v_cvt_pk_bf16_f32 v112, v112, v113\n" \
"v_cvt_pk_bf16_f32 v113, v114, v115\n" \
"v_cvt_pk_bf16_f32 v114, v144, v145\n" \
"v_cvt_pk_bf16_f32 v115, v146, v147\n" \
"s_add_u32 s74, s28, 8192\n" \
"buffer_store_dwordx4 v[112:115], v14, s[80:83], s74 offen offset:1024 sc1\n" \
"v_pk_mul_f32 v[20:21], v[52:53], s[44:45]\n" \
"v_pk_mul_f32 v[22:23], v[54:55], s[44:45]\n" \
"v_exp_f32 v20, v20\n" \
"v_exp_f32 v21, v21\n" \
"v_exp_f32 v22, v22\n" \
"v_exp_f32 v23, v23\n" \
"v_pk_mul_f32 v[24:25], v[84:85], s[44:45]\n" \
"v_pk_mul_f32 v[26:27], v[86:87], s[44:45]\n" \
"v_exp_f32 v24, v24\n" \
"v_accvgpr_read_b32 v4, a180\n" \
"v_exp_f32 v25, v25\n" \
"v_accvgpr_read_b32 v5, a181\n" \
"v_exp_f32 v26, v26\n" \
"v_accvgpr_read_b32 v6, a182\n" \
"v_exp_f32 v27, v27\n" \
"v_accvgpr_read_b32 v7, a183\n" \
"v_pk_add_f32 v[20:21], v[20:21], s[46:47]\n" \
"v_pk_add_f32 v[22:23], v[22:23], s[46:47]\n" \
"v_pk_add_f32 v[24:25], v[24:25], s[46:47]\n" \
"v_pk_add_f32 v[26:27], v[26:27], s[46:47]\n" \
"v_rcp_f32 v20, v20\n" \
"v_pk_mul_f32 v[52:53], v[52:53], v[180:181]\n" \
"v_rcp_f32 v21, v21\n" \
"v_pk_mul_f32 v[54:55], v[54:55], v[182:183]\n" \
"v_rcp_f32 v22, v22\n" \
"v_rcp_f32 v23, v23\n" \
"v_rcp_f32 v24, v24\n" \
"v_pk_mul_f32 v[84:85], v[84:85], v[4:5]\n" \
"v_rcp_f32 v25, v25\n" \
"v_pk_mul_f32 v[86:87], v[86:87], v[6:7]\n" \
"v_rcp_f32 v26, v26\n" \
"v_rcp_f32 v27, v27\n" \
"v_pk_mul_f32 v[52:53], v[52:53], v[20:21]\n" \
"v_pk_mul_f32 v[84:85], v[84:85], v[24:25]\n" \
"v_pk_mul_f32 v[54:55], v[54:55], v[22:23]\n" \
"v_pk_mul_f32 v[86:87], v[86:87], v[26:27]\n" \
"v_cvt_pk_bf16_f32 v52, v52, v53\n" \
"v_cvt_pk_bf16_f32 v53, v54, v55\n" \
"v_cvt_pk_bf16_f32 v54, v84, v85\n" \
"v_cvt_pk_bf16_f32 v55, v86, v87\n" \
"s_add_u32 s74, s28, 8192\n" \
"buffer_store_dwordx4 v[52:55], v14, s[80:83], s74 offen offset:2048 sc1\n" \
"v_pk_mul_f32 v[20:21], v[116:117], s[44:45]\n" \
"v_pk_mul_f32 v[22:23], v[118:119], s[44:45]\n" \
"v_exp_f32 v20, v20\n" \
"v_accvgpr_read_b32 v0, a212\n" \
"v_exp_f32 v21, v21\n" \
"v_accvgpr_read_b32 v1, a213\n" \
"v_exp_f32 v22, v22\n" \
"v_accvgpr_read_b32 v2, a214\n" \
"v_exp_f32 v23, v23\n" \
"v_accvgpr_read_b32 v3, a215\n" \
"v_pk_mul_f32 v[24:25], v[148:149], s[44:45]\n" \
"v_pk_mul_f32 v[26:27], v[150:151], s[44:45]\n" \
"v_exp_f32 v24, v24\n" \
"v_accvgpr_read_b32 v4, a244\n" \
"v_exp_f32 v25, v25\n" \
"v_accvgpr_read_b32 v5, a245\n" \
"v_exp_f32 v26, v26\n" \
"v_accvgpr_read_b32 v6, a246\n" \
"v_exp_f32 v27, v27\n" \
"v_accvgpr_read_b32 v7, a247\n" \
"v_pk_add_f32 v[20:21], v[20:21], s[46:47]\n" \
"v_pk_add_f32 v[22:23], v[22:23], s[46:47]\n" \
"v_pk_add_f32 v[24:25], v[24:25], s[46:47]\n" \
"v_pk_add_f32 v[26:27], v[26:27], s[46:47]\n" \
"v_rcp_f32 v20, v20\n" \
"v_pk_mul_f32 v[116:117], v[116:117], v[0:1]\n" \
"v_rcp_f32 v21, v21\n" \
"v_pk_mul_f32 v[118:119], v[118:119], v[2:3]\n" \
"v_rcp_f32 v22, v22\n" \
"v_rcp_f32 v23, v23\n" \
"v_rcp_f32 v24, v24\n" \
"v_pk_mul_f32 v[148:149], v[148:149], v[4:5]\n" \
"v_rcp_f32 v25, v25\n" \
"v_pk_mul_f32 v[150:151], v[150:151], v[6:7]\n" \
"v_rcp_f32 v26, v26\n" \
"v_rcp_f32 v27, v27\n" \
"v_pk_mul_f32 v[116:117], v[116:117], v[20:21]\n" \
"v_pk_mul_f32 v[148:149], v[148:149], v[24:25]\n" \
"v_pk_mul_f32 v[118:119], v[118:119], v[22:23]\n" \
"v_pk_mul_f32 v[150:151], v[150:151], v[26:27]\n" \
"v_cvt_pk_bf16_f32 v116, v116, v117\n" \
"v_cvt_pk_bf16_f32 v117, v118, v119\n" \
"v_cvt_pk_bf16_f32 v118, v148, v149\n" \
"v_cvt_pk_bf16_f32 v119, v150, v151\n" \
"s_add_u32 s74, s28, 8192\n" \
"buffer_store_dwordx4 v[116:119], v14, s[80:83], s74 offen offset:3072 sc1\n" \
"v_pk_mul_f32 v[20:21], v[56:57], s[44:45]\n" \
"v_pk_mul_f32 v[22:23], v[58:59], s[44:45]\n" \
"v_exp_f32 v20, v20\n" \
"v_exp_f32 v21, v21\n" \
"v_exp_f32 v22, v22\n" \
"v_exp_f32 v23, v23\n" \
"v_pk_mul_f32 v[24:25], v[88:89], s[44:45]\n" \
"v_pk_mul_f32 v[26:27], v[90:91], s[44:45]\n" \
"v_exp_f32 v24, v24\n" \
"v_accvgpr_read_b32 v4, a184\n" \
"v_exp_f32 v25, v25\n" \
"v_accvgpr_read_b32 v5, a185\n" \
"v_exp_f32 v26, v26\n" \
"v_accvgpr_read_b32 v6, a186\n" \
"v_exp_f32 v27, v27\n" \
"v_accvgpr_read_b32 v7, a187\n" \
"v_pk_add_f32 v[20:21], v[20:21], s[46:47]\n" \
"v_pk_add_f32 v[22:23], v[22:23], s[46:47]\n" \
"v_pk_add_f32 v[24:25], v[24:25], s[46:47]\n" \
"v_pk_add_f32 v[26:27], v[26:27], s[46:47]\n" \
"v_rcp_f32 v20, v20\n" \
"v_pk_mul_f32 v[56:57], v[56:57], v[184:185]\n" \
"v_rcp_f32 v21, v21\n" \
"v_pk_mul_f32 v[58:59], v[58:59], v[186:187]\n" \
"v_rcp_f32 v22, v22\n" \
"v_rcp_f32 v23, v23\n" \
"v_rcp_f32 v24, v24\n" \
"v_pk_mul_f32 v[88:89], v[88:89], v[4:5]\n" \
"v_rcp_f32 v25, v25\n" \
"v_pk_mul_f32 v[90:91], v[90:91], v[6:7]\n" \
"v_rcp_f32 v26, v26\n" \
"v_rcp_f32 v27, v27\n" \
"v_pk_mul_f32 v[56:57], v[56:57], v[20:21]\n" \
"v_pk_mul_f32 v[88:89], v[88:89], v[24:25]\n" \
"v_pk_mul_f32 v[58:59], v[58:59], v[22:23]\n" \
"v_pk_mul_f32 v[90:91], v[90:91], v[26:27]\n" \
"v_cvt_pk_bf16_f32 v56, v56, v57\n" \
"v_cvt_pk_bf16_f32 v57, v58, v59\n" \
"v_cvt_pk_bf16_f32 v58, v88, v89\n" \
"v_cvt_pk_bf16_f32 v59, v90, v91\n" \
"s_add_u32 s74, s28, 12288\n" \
"buffer_store_dwordx4 v[56:59], v14, s[80:83], s74 offen offset:0 sc1\n" \
"s_cmp_eq_u32 s78, 2\n" \
"s_cbranch_scc1 L_noep_%=\n" \
"s_cmp_lg_u32 s15, 0\n" \
"s_cbranch_scc1 L_ep2249_%=\n" \
"buffer_load_dwordx2 v[12:13], v30, s[96:99], 0 offen sc1\n" \
"L_ep2249_%=:\n" \
"L_noep_%=:\n" \
"v_pk_mul_f32 v[20:21], v[120:121], s[44:45]\n" \
"v_pk_mul_f32 v[22:23], v[122:123], s[44:45]\n" \
"v_exp_f32 v20, v20\n" \
"v_accvgpr_read_b32 v0, a216\n" \
"v_exp_f32 v21, v21\n" \
"v_accvgpr_read_b32 v1, a217\n" \
"v_exp_f32 v22, v22\n" \
"v_accvgpr_read_b32 v2, a218\n" \
"v_exp_f32 v23, v23\n" \
"v_accvgpr_read_b32 v3, a219\n" \
"v_pk_mul_f32 v[24:25], v[152:153], s[44:45]\n" \
"v_pk_mul_f32 v[26:27], v[154:155], s[44:45]\n" \
"v_exp_f32 v24, v24\n" \
"v_accvgpr_read_b32 v4, a248\n" \
"v_exp_f32 v25, v25\n" \
"v_accvgpr_read_b32 v5, a249\n" \
"v_exp_f32 v26, v26\n" \
"v_accvgpr_read_b32 v6, a250\n" \
"v_exp_f32 v27, v27\n" \
"v_accvgpr_read_b32 v7, a251\n" \
"v_pk_add_f32 v[20:21], v[20:21], s[46:47]\n" \
"v_pk_add_f32 v[22:23], v[22:23], s[46:47]\n" \
"v_pk_add_f32 v[24:25], v[24:25], s[46:47]\n" \
"v_pk_add_f32 v[26:27], v[26:27], s[46:47]\n" \
"v_rcp_f32 v20, v20\n" \
"v_pk_mul_f32 v[120:121], v[120:121], v[0:1]\n" \
"v_rcp_f32 v21, v21\n" \
"v_pk_mul_f32 v[122:123], v[122:123], v[2:3]\n" \
"v_rcp_f32 v22, v22\n" \
"v_rcp_f32 v23, v23\n" \
"v_rcp_f32 v24, v24\n" \
"v_pk_mul_f32 v[152:153], v[152:153], v[4:5]\n" \
"v_rcp_f32 v25, v25\n" \
"v_pk_mul_f32 v[154:155], v[154:155], v[6:7]\n" \
"v_rcp_f32 v26, v26\n" \
"v_rcp_f32 v27, v27\n" \
"v_pk_mul_f32 v[120:121], v[120:121], v[20:21]\n" \
"v_pk_mul_f32 v[152:153], v[152:153], v[24:25]\n" \
"v_pk_mul_f32 v[122:123], v[122:123], v[22:23]\n" \
"v_pk_mul_f32 v[154:155], v[154:155], v[26:27]\n" \
"v_cvt_pk_bf16_f32 v120, v120, v121\n" \
"v_cvt_pk_bf16_f32 v121, v122, v123\n" \
"v_cvt_pk_bf16_f32 v122, v152, v153\n" \
"v_cvt_pk_bf16_f32 v123, v154, v155\n" \
"s_add_u32 s74, s28, 12288\n" \
"buffer_store_dwordx4 v[120:123], v14, s[80:83], s74 offen offset:1024 sc1\n" \
"v_pk_mul_f32 v[20:21], v[60:61], s[44:45]\n" \
"v_pk_mul_f32 v[22:23], v[62:63], s[44:45]\n" \
"v_exp_f32 v20, v20\n" \
"v_exp_f32 v21, v21\n" \
"v_exp_f32 v22, v22\n" \
"v_exp_f32 v23, v23\n" \
"v_pk_mul_f32 v[24:25], v[92:93], s[44:45]\n" \
"v_pk_mul_f32 v[26:27], v[94:95], s[44:45]\n" \
"v_exp_f32 v24, v24\n" \
"v_accvgpr_read_b32 v4, a188\n" \
"v_exp_f32 v25, v25\n" \
"v_accvgpr_read_b32 v5, a189\n" \
"v_exp_f32 v26, v26\n" \
"v_accvgpr_read_b32 v6, a190\n" \
"v_exp_f32 v27, v27\n" \
"v_accvgpr_read_b32 v7, a191\n" \
"v_pk_add_f32 v[20:21], v[20:21], s[46:47]\n" \
"v_pk_add_f32 v[22:23], v[22:23], s[46:47]\n" \
"v_pk_add_f32 v[24:25], v[24:25], s[46:47]\n" \
"v_pk_add_f32 v[26:27], v[26:27], s[46:47]\n" \
"v_rcp_f32 v20, v20\n" \
"v_pk_mul_f32 v[60:61], v[60:61], v[188:189]\n" \
"v_rcp_f32 v21, v21\n" \
"v_pk_mul_f32 v[62:63], v[62:63], v[190:191]\n" \
"v_rcp_f32 v22, v22\n" \
"v_rcp_f32 v23, v23\n" \
"v_rcp_f32 v24, v24\n" \
"v_pk_mul_f32 v[92:93], v[92:93], v[4:5]\n" \
"v_rcp_f32 v25, v25\n" \
"v_pk_mul_f32 v[94:95], v[94:95], v[6:7]\n" \
"v_rcp_f32 v26, v26\n" \
"v_rcp_f32 v27, v27\n" \
"v_pk_mul_f32 v[60:61], v[60:61], v[20:21]\n" \
"v_pk_mul_f32 v[92:93], v[92:93], v[24:25]\n" \
"v_pk_mul_f32 v[62:63], v[62:63], v[22:23]\n" \
"v_pk_mul_f32 v[94:95], v[94:95], v[26:27]\n" \
"v_cvt_pk_bf16_f32 v60, v60, v61\n" \
"v_cvt_pk_bf16_f32 v61, v62, v63\n" \
"v_cvt_pk_bf16_f32 v62, v92, v93\n" \
"v_cvt_pk_bf16_f32 v63, v94, v95\n" \
"s_add_u32 s74, s28, 12288\n" \
"buffer_store_dwordx4 v[60:63], v14, s[80:83], s74 offen offset:2048 sc1\n" \
"v_pk_mul_f32 v[20:21], v[124:125], s[44:45]\n" \
"v_pk_mul_f32 v[22:23], v[126:127], s[44:45]\n" \
"v_exp_f32 v20, v20\n" \
"v_accvgpr_read_b32 v0, a220\n" \
"v_exp_f32 v21, v21\n" \
"v_accvgpr_read_b32 v1, a221\n" \
"v_exp_f32 v22, v22\n" \
"v_accvgpr_read_b32 v2, a222\n" \
"v_exp_f32 v23, v23\n" \
"v_accvgpr_read_b32 v3, a223\n" \
"v_pk_mul_f32 v[24:25], v[156:157], s[44:45]\n" \
"v_pk_mul_f32 v[26:27], v[158:159], s[44:45]\n" \
"v_exp_f32 v24, v24\n" \
"v_accvgpr_read_b32 v4, a252\n" \
"v_exp_f32 v25, v25\n" \
"v_accvgpr_read_b32 v5, a253\n" \
"v_exp_f32 v26, v26\n" \
"v_accvgpr_read_b32 v6, a254\n" \
"v_exp_f32 v27, v27\n" \
"v_accvgpr_read_b32 v7, a255\n" \
"v_pk_add_f32 v[20:21], v[20:21], s[46:47]\n" \
"v_pk_add_f32 v[22:23], v[22:23], s[46:47]\n" \
"v_pk_add_f32 v[24:25], v[24:25], s[46:47]\n" \
"v_pk_add_f32 v[26:27], v[26:27], s[46:47]\n" \
"v_rcp_f32 v20, v20\n" \
"v_pk_mul_f32 v[124:125], v[124:125], v[0:1]\n" \
"v_rcp_f32 v21, v21\n" \
"v_pk_mul_f32 v[126:127], v[126:127], v[2:3]\n" \
"v_rcp_f32 v22, v22\n" \
"v_rcp_f32 v23, v23\n" \
"v_rcp_f32 v24, v24\n" \
"v_pk_mul_f32 v[156:157], v[156:157], v[4:5]\n" \
"v_rcp_f32 v25, v25\n" \
"v_pk_mul_f32 v[158:159], v[158:159], v[6:7]\n" \
"v_rcp_f32 v26, v26\n" \
"v_rcp_f32 v27, v27\n" \
"v_pk_mul_f32 v[124:125], v[124:125], v[20:21]\n" \
"v_pk_mul_f32 v[156:157], v[156:157], v[24:25]\n" \
"v_pk_mul_f32 v[126:127], v[126:127], v[22:23]\n" \
"v_pk_mul_f32 v[158:159], v[158:159], v[26:27]\n" \
"v_cvt_pk_bf16_f32 v124, v124, v125\n" \
"v_cvt_pk_bf16_f32 v125, v126, v127\n" \
"v_cvt_pk_bf16_f32 v126, v156, v157\n" \
"v_cvt_pk_bf16_f32 v127, v158, v159\n" \
"s_add_u32 s74, s28, 12288\n" \
"buffer_store_dwordx4 v[124:127], v14, s[80:83], s74 offen offset:3072 sc1\n" \
"s_cmp_lg_u32 s78, 2\n" \
"s_cbranch_scc1 L_noq_%=\n" \
"v_pk_mul_f32 v[20:21], v[192:193], s[48:49]\n" \
"v_pk_mul_f32 v[22:23], v[194:195], s[48:49]\n" \
"v_pk_mul_f32 v[24:25], v[224:225], s[48:49]\n" \
"v_pk_mul_f32 v[26:27], v[226:227], s[48:49]\n" \
"v_exp_f32_e64 v20, v20 clamp\n" \
"v_max_f32 v192, s50, v192\n" \
"v_exp_f32_e64 v21, v21 clamp\n" \
"v_max_f32 v193, s50, v193\n" \
"v_exp_f32_e64 v22, v22 clamp\n" \
"v_max_f32 v194, s50, v194\n" \
"v_exp_f32_e64 v23, v23 clamp\n" \
"v_max_f32 v195, s50, v195\n" \
"v_exp_f32_e64 v24, v24 clamp\n" \
"v_max_f32 v224, s50, v224\n" \
"v_exp_f32_e64 v25, v25 clamp\n" \
"v_max_f32 v225, s50, v225\n" \
"v_exp_f32_e64 v26, v26 clamp\n" \
"v_max_f32 v226, s50, v226\n" \
"v_exp_f32_e64 v27, v27 clamp\n" \
"v_max_f32 v227, s50, v227\n" \
"v_pk_add_f32 v[192:193], v[192:193], v[20:21]\n" \
"v_pk_add_f32 v[194:195], v[194:195], v[22:23]\n" \
"v_pk_add_f32 v[224:225], v[224:225], v[24:25]\n" \
"v_pk_add_f32 v[226:227], v[226:227], v[26:27]\n" \
"v_cvt_pk_bf16_f32 v192, v192, v193\n" \
"v_cvt_pk_bf16_f32 v193, v194, v195\n" \
"v_cvt_pk_bf16_f32 v194, v224, v225\n" \
"v_cvt_pk_bf16_f32 v195, v226, v227\n" \
"buffer_store_dwordx4 v[192:195], v14, s[84:87], s29 offen offset:0 sc1\n" \
"v_pk_mul_f32 v[20:21], v[196:197], s[48:49]\n" \
"v_pk_mul_f32 v[22:23], v[198:199], s[48:49]\n" \
"v_pk_mul_f32 v[24:25], v[228:229], s[48:49]\n" \
"v_pk_mul_f32 v[26:27], v[230:231], s[48:49]\n" \
"v_exp_f32_e64 v20, v20 clamp\n" \
"v_max_f32 v196, s50, v196\n" \
"v_exp_f32_e64 v21, v21 clamp\n" \
"v_max_f32 v197, s50, v197\n" \
"v_exp_f32_e64 v22, v22 clamp\n" \
"v_max_f32 v198, s50, v198\n" \
"v_exp_f32_e64 v23, v23 clamp\n" \
"v_max_f32 v199, s50, v199\n" \
"v_exp_f32_e64 v24, v24 clamp\n" \
"v_max_f32 v228, s50, v228\n" \
"v_exp_f32_e64 v25, v25 clamp\n" \
"v_max_f32 v229, s50, v229\n" \
"v_exp_f32_e64 v26, v26 clamp\n" \
"v_max_f32 v230, s50, v230\n" \
"v_exp_f32_e64 v27, v27 clamp\n" \
"v_max_f32 v231, s50, v231\n" \
"v_pk_add_f32 v[196:197], v[196:197], v[20:21]\n" \
"v_pk_add_f32 v[198:199], v[198:199], v[22:23]\n" \
"v_pk_add_f32 v[228:229], v[228:229], v[24:25]\n" \
"v_pk_add_f32 v[230:231], v[230:231], v[26:27]\n" \
"v_cvt_pk_bf16_f32 v196, v196, v197\n" \
"v_cvt_pk_bf16_f32 v197, v198, v199\n" \
"v_cvt_pk_bf16_f32 v198, v228, v229\n" \
"v_cvt_pk_bf16_f32 v199, v230, v231\n" \
"buffer_store_dwordx4 v[196:199], v14, s[84:87], s29 offen offset:2048 sc1\n" \
"v_pk_mul_f32 v[20:21], v[200:201], s[48:49]\n" \
"v_pk_mul_f32 v[22:23], v[202:203], s[48:49]\n" \
"v_pk_mul_f32 v[24:25], v[232:233], s[48:49]\n" \
"v_pk_mul_f32 v[26:27], v[234:235], s[48:49]\n" \
"v_exp_f32_e64 v20, v20 clamp\n" \
"v_max_f32 v200, s50, v200\n" \
"v_exp_f32_e64 v21, v21 clamp\n" \
"v_max_f32 v201, s50, v201\n" \
"v_exp_f32_e64 v22, v22 clamp\n" \
"v_max_f32 v202, s50, v202\n" \
"v_exp_f32_e64 v23, v23 clamp\n" \
"v_max_f32 v203, s50, v203\n" \
"v_exp_f32_e64 v24, v24 clamp\n" \
"v_max_f32 v232, s50, v232\n" \
"v_exp_f32_e64 v25, v25 clamp\n" \
"v_max_f32 v233, s50, v233\n" \
"v_exp_f32_e64 v26, v26 clamp\n" \
"v_max_f32 v234, s50, v234\n" \
"v_exp_f32_e64 v27, v27 clamp\n" \
"v_max_f32 v235, s50, v235\n" \
"v_pk_add_f32 v[200:201], v[200:201], v[20:21]\n" \
"v_pk_add_f32 v[202:203], v[202:203], v[22:23]\n" \
"v_pk_add_f32 v[232:233], v[232:233], v[24:25]\n" \
"v_pk_add_f32 v[234:235], v[234:235], v[26:27]\n" \
"v_cvt_pk_bf16_f32 v200, v200, v201\n" \
"v_cvt_pk_bf16_f32 v201, v202, v203\n" \
"v_cvt_pk_bf16_f32 v202, v232, v233\n" \
"v_cvt_pk_bf16_f32 v203, v234, v235\n" \
"s_add_u32 s74, s29, 4096\n" \
"buffer_store_dwordx4 v[200:203], v14, s[84:87], s74 offen offset:0 sc1\n" \
"v_pk_mul_f32 v[20:21], v[204:205], s[48:49]\n" \
"v_pk_mul_f32 v[22:23], v[206:207], s[48:49]\n" \
"v_pk_mul_f32 v[24:25], v[236:237], s[48:49]\n" \
"v_pk_mul_f32 v[26:27], v[238:239], s[48:49]\n" \
"v_exp_f32_e64 v20, v20 clamp\n" \
"v_max_f32 v204, s50, v204\n" \
"v_exp_f32_e64 v21, v21 clamp\n" \
"v_max_f32 v205, s50, v205\n" \
"v_exp_f32_e64 v22, v22 clamp\n" \
"v_max_f32 v206, s50, v206\n" \
"v_exp_f32_e64 v23, v23 clamp\n" \
"v_max_f32 v207, s50, v207\n" \
"v_exp_f32_e64 v24, v24 clamp\n" \
"v_max_f32 v236, s50, v236\n" \
"v_exp_f32_e64 v25, v25 clamp\n" \
"v_max_f32 v237, s50, v237\n" \
"v_exp_f32_e64 v26, v26 clamp\n" \
"v_max_f32 v238, s50, v238\n" \
"v_exp_f32_e64 v27, v27 clamp\n" \
"v_max_f32 v239, s50, v239\n" \
"v_pk_add_f32 v[204:205], v[204:205], v[20:21]\n" \
"v_pk_add_f32 v[206:207], v[206:207], v[22:23]\n" \
"v_pk_add_f32 v[236:237], v[236:237], v[24:25]\n" \
"v_pk_add_f32 v[238:239], v[238:239], v[26:27]\n" \
"v_cvt_pk_bf16_f32 v204, v204, v205\n" \
"v_cvt_pk_bf16_f32 v205, v206, v207\n" \
"v_cvt_pk_bf16_f32 v206, v236, v237\n" \
"v_cvt_pk_bf16_f32 v207, v238, v239\n" \
"s_add_u32 s74, s29, 4096\n" \
"buffer_store_dwordx4 v[204:207], v14, s[84:87], s74 offen offset:2048 sc1\n" \
"v_pk_mul_f32 v[20:21], v[208:209], s[48:49]\n" \
"s_cmp_lg_u32 s15, 0\n" \
"s_cbranch_scc1 L_ep2509_%=\n" \
"buffer_load_dwordx2 v[12:13], v30, s[96:99], 0 offen sc1\n" \
"L_ep2509_%=:\n" \
"v_pk_mul_f32 v[22:23], v[210:211], s[48:49]\n" \
"v_pk_mul_f32 v[24:25], v[240:241], s[48:49]\n" \
"v_pk_mul_f32 v[26:27], v[242:243], s[48:49]\n" \
"v_exp_f32_e64 v20, v20 clamp\n" \
"v_max_f32 v208, s50, v208\n" \
"v_exp_f32_e64 v21, v21 clamp\n" \
"v_max_f32 v209, s50, v209\n" \
"v_exp_f32_e64 v22, v22 clamp\n" \
"v_max_f32 v210, s50, v210\n" \
"v_exp_f32_e64 v23, v23 clamp\n" \
"v_max_f32 v211, s50, v211\n" \
"v_exp_f32_e64 v24, v24 clamp\n" \
"v_max_f32 v240, s50, v240\n" \
"v_exp_f32_e64 v25, v25 clamp\n" \
"v_max_f32 v241, s50, v241\n" \
"v_exp_f32_e64 v26, v26 clamp\n" \
"v_max_f32 v242, s50, v242\n" \
"v_exp_f32_e64 v27, v27 clamp\n" \
"v_max_f32 v243, s50, v243\n" \
"v_pk_add_f32 v[208:209], v[208:209], v[20:21]\n" \
"v_pk_add_f32 v[210:211], v[210:211], v[22:23]\n" \
"v_pk_add_f32 v[240:241], v[240:241], v[24:25]\n" \
"v_pk_add_f32 v[242:243], v[242:243], v[26:27]\n" \
"v_cvt_pk_bf16_f32 v208, v208, v209\n" \
"v_cvt_pk_bf16_f32 v209, v210, v211\n" \
"v_cvt_pk_bf16_f32 v210, v240, v241\n" \
"v_cvt_pk_bf16_f32 v211, v242, v243\n" \
"s_add_u32 s74, s29, 8192\n" \
"buffer_store_dwordx4 v[208:211], v14, s[84:87], s74 offen offset:0 sc1\n" \
"v_pk_mul_f32 v[20:21], v[212:213], s[48:49]\n" \
"v_pk_mul_f32 v[22:23], v[214:215], s[48:49]\n" \
"v_pk_mul_f32 v[24:25], v[244:245], s[48:49]\n" \
"v_pk_mul_f32 v[26:27], v[246:247], s[48:49]\n" \
"v_exp_f32_e64 v20, v20 clamp\n" \
"v_max_f32 v212, s50, v212\n" \
"v_exp_f32_e64 v21, v21 clamp\n" \
"v_max_f32 v213, s50, v213\n" \
"v_exp_f32_e64 v22, v22 clamp\n" \
"v_max_f32 v214, s50, v214\n" \
"v_exp_f32_e64 v23, v23 clamp\n" \
"v_max_f32 v215, s50, v215\n" \
"v_exp_f32_e64 v24, v24 clamp\n" \
"v_max_f32 v244, s50, v244\n" \
"v_exp_f32_e64 v25, v25 clamp\n" \
"v_max_f32 v245, s50, v245\n" \
"v_exp_f32_e64 v26, v26 clamp\n" \
"v_max_f32 v246, s50, v246\n" \
"v_exp_f32_e64 v27, v27 clamp\n" \
"v_max_f32 v247, s50, v247\n" \
"v_pk_add_f32 v[212:213], v[212:213], v[20:21]\n" \
"v_pk_add_f32 v[214:215], v[214:215], v[22:23]\n" \
"v_pk_add_f32 v[244:245], v[244:245], v[24:25]\n" \
"v_pk_add_f32 v[246:247], v[246:247], v[26:27]\n" \
"v_cvt_pk_bf16_f32 v212, v212, v213\n" \
"v_cvt_pk_bf16_f32 v213, v214, v215\n" \
"v_cvt_pk_bf16_f32 v214, v244, v245\n" \
"v_cvt_pk_bf16_f32 v215, v246, v247\n" \
"s_add_u32 s74, s29, 8192\n" \
"buffer_store_dwordx4 v[212:215], v14, s[84:87], s74 offen offset:2048 sc1\n" \
"v_pk_mul_f32 v[20:21], v[216:217], s[48:49]\n" \
"v_pk_mul_f32 v[22:23], v[218:219], s[48:49]\n" \
"v_pk_mul_f32 v[24:25], v[248:249], s[48:49]\n" \
"v_pk_mul_f32 v[26:27], v[250:251], s[48:49]\n" \
"v_exp_f32_e64 v20, v20 clamp\n" \
"v_max_f32 v216, s50, v216\n" \
"v_exp_f32_e64 v21, v21 clamp\n" \
"v_max_f32 v217, s50, v217\n" \
"v_exp_f32_e64 v22, v22 clamp\n" \
"v_max_f32 v218, s50, v218\n" \
"v_exp_f32_e64 v23, v23 clamp\n" \
"v_max_f32 v219, s50, v219\n" \
"v_exp_f32_e64 v24, v24 clamp\n" \
"v_max_f32 v248, s50, v248\n" \
"v_exp_f32_e64 v25, v25 clamp\n" \
"v_max_f32 v249, s50, v249\n" \
"v_exp_f32_e64 v26, v26 clamp\n" \
"v_max_f32 v250, s50, v250\n" \
"v_exp_f32_e64 v27, v27 clamp\n" \
"v_max_f32 v251, s50, v251\n" \
"v_pk_add_f32 v[216:217], v[216:217], v[20:21]\n" \
"v_pk_add_f32 v[218:219], v[218:219], v[22:23]\n" \
"v_pk_add_f32 v[248:249], v[248:249], v[24:25]\n" \
"v_pk_add_f32 v[250:251], v[250:251], v[26:27]\n" \
"v_cvt_pk_bf16_f32 v216, v216, v217\n" \
"v_cvt_pk_bf16_f32 v217, v218, v219\n" \
"v_cvt_pk_bf16_f32 v218, v248, v249\n" \
"v_cvt_pk_bf16_f32 v219, v250, v251\n" \
"s_add_u32 s74, s29, 12288\n" \
"buffer_store_dwordx4 v[216:219], v14, s[84:87], s74 offen offset:0 sc1\n" \
"v_pk_mul_f32 v[20:21], v[220:221], s[48:49]\n" \
"v_pk_mul_f32 v[22:23], v[222:223], s[48:49]\n" \
"v_pk_mul_f32 v[24:25], v[252:253], s[48:49]\n" \
"v_pk_mul_f32 v[26:27], v[254:255], s[48:49]\n" \
"v_exp_f32_e64 v20, v20 clamp\n" \
"v_max_f32 v220, s50, v220\n" \
"v_exp_f32_e64 v21, v21 clamp\n" \
"v_max_f32 v221, s50, v221\n" \
"v_exp_f32_e64 v22, v22 clamp\n" \
"v_max_f32 v222, s50, v222\n" \
"v_exp_f32_e64 v23, v23 clamp\n" \
"v_max_f32 v223, s50, v223\n" \
"v_exp_f32_e64 v24, v24 clamp\n" \
"v_max_f32 v252, s50, v252\n" \
"v_exp_f32_e64 v25, v25 clamp\n" \
"v_max_f32 v253, s50, v253\n" \
"v_exp_f32_e64 v26, v26 clamp\n" \
"v_max_f32 v254, s50, v254\n" \
"v_exp_f32_e64 v27, v27 clamp\n" \
"v_max_f32 v255, s50, v255\n" \
"v_pk_add_f32 v[220:221], v[220:221], v[20:21]\n" \
"v_pk_add_f32 v[222:223], v[222:223], v[22:23]\n" \
"v_pk_add_f32 v[252:253], v[252:253], v[24:25]\n" \
"v_pk_add_f32 v[254:255], v[254:255], v[26:27]\n" \
"v_cvt_pk_bf16_f32 v220, v220, v221\n" \
"v_cvt_pk_bf16_f32 v221, v222, v223\n" \
"v_cvt_pk_bf16_f32 v222, v252, v253\n" \
"v_cvt_pk_bf16_f32 v223, v254, v255\n" \
"s_add_u32 s74, s29, 12288\n" \
"buffer_store_dwordx4 v[220:223], v14, s[84:87], s74 offen offset:2048 sc1\n" \
"s_waitcnt vmcnt(4)\n" \
"s_branch L_end_%=\n" \
"L_noq_%=:\n" \
"s_waitcnt vmcnt(3)\n" \
"s_branch L_end_%=\n" \
"L_kv_%=:\n" \
"L_g1k_%=:\n" \
"s_waitcnt lgkmcnt(4)\n" \
"v_mfma_f32_16x16x32_bf16 v[32:35], a[0:3], a[128:131], v[32:35]\n" \
"ds_read_b64_tr_b16 a[144:145], v3 offset:0\n" \
"v_mfma_f32_16x16x32_bf16 v[32:35], a[4:7], a[132:135], v[32:35]\n" \
"buffer_load_dwordx4 v8, s[44:47], s65 offen offset:2048 lds\n" \
"v_mfma_f32_16x16x32_bf16 v[36:39], a[12:15], a[132:135], v[36:39]\n" \
"ds_read_b64_tr_b16 a[146:147], v3 offset:512\n" \
"v_mfma_f32_16x16x32_bf16 v[36:39], a[8:11], a[128:131], v[36:39]\n" \
"v_mfma_f32_16x16x32_bf16 v[40:43], a[16:19], a[128:131], v[40:43]\n" \
"ds_read_b64_tr_b16 a[148:149], v3 offset:16384\n" \
"v_mfma_f32_16x16x32_bf16 v[40:43], a[20:23], a[132:135], v[40:43]\n" \
"buffer_load_dwordx4 v10, s[44:47], s66 offen offset:3072 lds\n" \
"v_mfma_f32_16x16x32_bf16 v[44:47], a[28:31], a[132:135], v[44:47]\n" \
"ds_read_b64_tr_b16 a[150:151], v3 offset:16896\n" \
"v_mfma_f32_16x16x32_bf16 v[44:47], a[24:27], a[128:131], v[44:47]\n" \
"v_mfma_f32_16x16x32_bf16 v[48:51], a[32:35], a[128:131], v[48:51]\n" \
"s_add_u32 m0, s71, 36864\n" \
"v_mfma_f32_16x16x32_bf16 v[48:51], a[36:39], a[132:135], v[48:51]\n" \
"buffer_load_dwordx4 v8, s[48:51], s65 offen offset:0 lds\n" \
"v_mfma_f32_16x16x32_bf16 v[52:55], a[44:47], a[132:135], v[52:55]\n" \
"v_mfma_f32_16x16x32_bf16 v[52:55], a[40:43], a[128:131], v[52:55]\n" \
"v_mfma_f32_16x16x32_bf16 v[56:59], a[48:51], a[128:131], v[56:59]\n" \
"v_mfma_f32_16x16x32_bf16 v[56:59], a[52:55], a[132:135], v[56:59]\n" \
"buffer_load_dwordx4 v10, s[48:51], s66 offen offset:1024 lds\n" \
"v_mfma_f32_16x16x32_bf16 v[60:63], a[60:63], a[132:135], v[60:63]\n" \
"v_mfma_f32_16x16x32_bf16 v[60:63], a[56:59], a[128:131], v[60:63]\n" \
"s_waitcnt lgkmcnt(4)\n" \
"v_mfma_f32_16x16x32_bf16 v[92:95], a[56:59], a[136:139], v[92:95]\n" \
"ds_read_b64_tr_b16 a[152:153], v4 offset:0\n" \
"v_mfma_f32_16x16x32_bf16 v[92:95], a[60:63], a[140:143], v[92:95]\n" \
"buffer_load_dwordx4 v8, s[52:55], s65 offen offset:2048 lds\n" \
"v_mfma_f32_16x16x32_bf16 v[88:91], a[52:55], a[140:143], v[88:91]\n" \
"ds_read_b64_tr_b16 a[154:155], v4 offset:512\n" \
"v_mfma_f32_16x16x32_bf16 v[88:91], a[48:51], a[136:139], v[88:91]\n" \
"v_mfma_f32_16x16x32_bf16 v[84:87], a[40:43], a[136:139], v[84:87]\n" \
"ds_read_b64_tr_b16 a[156:157], v4 offset:16384\n" \
"v_mfma_f32_16x16x32_bf16 v[84:87], a[44:47], a[140:143], v[84:87]\n" \
"buffer_load_dwordx4 v10, s[52:55], s66 offen offset:3072 lds\n" \
"v_mfma_f32_16x16x32_bf16 v[80:83], a[36:39], a[140:143], v[80:83]\n" \
"ds_read_b64_tr_b16 a[158:159], v4 offset:16896\n" \
"v_mfma_f32_16x16x32_bf16 v[80:83], a[32:35], a[136:139], v[80:83]\n" \
"v_mfma_f32_16x16x32_bf16 v[76:79], a[24:27], a[136:139], v[76:79]\n" \
"s_add_u32 m0, s72, 8192\n" \
"v_mfma_f32_16x16x32_bf16 v[76:79], a[28:31], a[140:143], v[76:79]\n" \
"buffer_load_dwordx4 v9, s[56:59], s67 offen offset:0 lds\n" \
"v_mfma_f32_16x16x32_bf16 v[72:75], a[20:23], a[140:143], v[72:75]\n" \
"v_mfma_f32_16x16x32_bf16 v[72:75], a[16:19], a[136:139], v[72:75]\n" \
"v_mfma_f32_16x16x32_bf16 v[68:71], a[8:11], a[136:139], v[68:71]\n" \
"v_mfma_f32_16x16x32_bf16 v[68:71], a[12:15], a[140:143], v[68:71]\n" \
"buffer_load_dwordx4 v9, s[60:63], s67 offen offset:1024 lds\n" \
"s_add_u32 s65, s65, s68\n" \
"s_add_u32 s66, s66, s68\n" \
"s_add_u32 s67, s67, s69\n" \
"v_mfma_f32_16x16x32_bf16 v[64:67], a[4:7], a[140:143], v[64:67]\n" \
"v_mfma_f32_16x16x32_bf16 v[64:67], a[0:3], a[136:139], v[64:67]\n" \
"s_waitcnt lgkmcnt(4)\n" \
"v_mfma_f32_16x16x32_bf16 v[96:99], a[0:3], a[144:147], v[96:99]\n" \
"ds_read_b64_tr_b16 a[128:129], v1 offset:2048\n" \
"s_add_u32 m0, s70, 32768\n" \
"v_mfma_f32_16x16x32_bf16 v[96:99], a[4:7], a[148:151], v[96:99]\n" \
"buffer_load_dwordx4 v7, s[36:39], s64 offen offset:0 sc1 lds\n" \
"v_mfma_f32_16x16x32_bf16 v[100:103], a[12:15], a[148:151], v[100:103]\n" \
"ds_read_b64_tr_b16 a[130:131], v1 offset:2560\n" \
"v_mfma_f32_16x16x32_bf16 v[100:103], a[8:11], a[144:147], v[100:103]\n" \
"v_mfma_f32_16x16x32_bf16 v[104:107], a[16:19], a[144:147], v[104:107]\n" \
"ds_read_b64_tr_b16 a[132:133], v1 offset:18432\n" \
"v_mfma_f32_16x16x32_bf16 v[104:107], a[20:23], a[148:151], v[104:107]\n" \
"buffer_load_dwordx4 v7, s[36:39], s64 offen offset:1024 sc1 lds\n" \
"v_mfma_f32_16x16x32_bf16 v[108:111], a[28:31], a[148:151], v[108:111]\n" \
"ds_read_b64_tr_b16 a[134:135], v1 offset:18944\n" \
"v_mfma_f32_16x16x32_bf16 v[108:111], a[24:27], a[144:147], v[108:111]\n" \
"v_mfma_f32_16x16x32_bf16 v[112:115], a[32:35], a[144:147], v[112:115]\n" \
"v_mfma_f32_16x16x32_bf16 v[112:115], a[36:39], a[148:151], v[112:115]\n" \
"buffer_load_dwordx4 v7, s[36:39], s64 offen offset:2048 sc1 lds\n" \
"v_mfma_f32_16x16x32_bf16 v[116:119], a[44:47], a[148:151], v[116:119]\n" \
"v_mfma_f32_16x16x32_bf16 v[116:119], a[40:43], a[144:147], v[116:119]\n" \
"v_mfma_f32_16x16x32_bf16 v[120:123], a[48:51], a[144:147], v[120:123]\n" \
"v_mfma_f32_16x16x32_bf16 v[120:123], a[52:55], a[148:151], v[120:123]\n" \
"buffer_load_dwordx4 v7, s[36:39], s64 offen offset:3072 sc1 lds\n" \
"v_mfma_f32_16x16x32_bf16 v[124:127], a[60:63], a[148:151], v[124:127]\n" \
"v_mfma_f32_16x16x32_bf16 v[124:127], a[56:59], a[144:147], v[124:127]\n" \
"s_waitcnt lgkmcnt(4)\n" \
"v_mfma_f32_16x16x32_bf16 v[156:159], a[56:59], a[152:155], v[156:159]\n" \
"ds_read_b64_tr_b16 a[136:137], v2 offset:2048\n" \
"s_add_u32 m0, s70, 36864\n" \
"s_add_u32 s74, s64, 4096\n" \
"v_mfma_f32_16x16x32_bf16 v[156:159], a[60:63], a[156:159], v[156:159]\n" \
"buffer_load_dwordx4 v7, s[36:39], s74 offen offset:0 sc1 lds\n" \
"v_mfma_f32_16x16x32_bf16 v[152:155], a[52:55], a[156:159], v[152:155]\n" \
"ds_read_b64_tr_b16 a[138:139], v2 offset:2560\n" \
"v_mfma_f32_16x16x32_bf16 v[152:155], a[48:51], a[152:155], v[152:155]\n" \
"v_mfma_f32_16x16x32_bf16 v[148:151], a[40:43], a[152:155], v[148:151]\n" \
"ds_read_b64_tr_b16 a[140:141], v2 offset:18432\n" \
"v_mfma_f32_16x16x32_bf16 v[148:151], a[44:47], a[156:159], v[148:151]\n" \
"buffer_load_dwordx4 v7, s[36:39], s74 offen offset:1024 sc1 lds\n" \
"v_mfma_f32_16x16x32_bf16 v[144:147], a[36:39], a[156:159], v[144:147]\n" \
"ds_read_b64_tr_b16 a[142:143], v2 offset:18944\n" \
"v_mfma_f32_16x16x32_bf16 v[144:147], a[32:35], a[152:155], v[144:147]\n" \
"v_mfma_f32_16x16x32_bf16 v[140:143], a[24:27], a[152:155], v[140:143]\n" \
"v_mfma_f32_16x16x32_bf16 v[140:143], a[28:31], a[156:159], v[140:143]\n" \
"buffer_load_dwordx4 v7, s[36:39], s74 offen offset:2048 sc1 lds\n" \
"v_mfma_f32_16x16x32_bf16 v[136:139], a[20:23], a[156:159], v[136:139]\n" \
"v_mfma_f32_16x16x32_bf16 v[136:139], a[16:19], a[152:155], v[136:139]\n" \
"v_mfma_f32_16x16x32_bf16 v[132:135], a[8:11], a[152:155], v[132:135]\n" \
"v_mfma_f32_16x16x32_bf16 v[132:135], a[12:15], a[156:159], v[132:135]\n" \
"buffer_load_dwordx4 v7, s[36:39], s74 offen offset:3072 sc1 lds\n" \
"s_add_u32 s64, s64, 32768\n" \
"v_mfma_f32_16x16x32_bf16 v[128:131], a[4:7], a[156:159], v[128:131]\n" \
"v_mfma_f32_16x16x32_bf16 v[128:131], a[0:3], a[152:155], v[128:131]\n" \
"s_waitcnt lgkmcnt(4)\n" \
"v_mfma_f32_16x16x32_bf16 v[160:163], a[0:3], a[128:131], v[160:163]\n" \
"ds_read_b64_tr_b16 a[144:145], v3 offset:2048\n" \
"s_mov_b32 m0, s77\n" \
"s_sub_i32 s75, s75, 1\n" \
"v_mfma_f32_16x16x32_bf16 v[160:163], a[4:7], a[132:135], v[160:163]\n" \
"s_cmp_lt_u32 s75, s27\n" \
"s_cselect_b32 s76, 1, 0\n" \
"v_mfma_f32_16x16x32_bf16 v[164:167], a[12:15], a[132:135], v[164:167]\n" \
"ds_read_b64_tr_b16 a[146:147], v3 offset:2560\n" \
"global_load_lds_dword v[12:13], off\n" \
"v_mad_u64_u32 v[12:13], vcc, v11, s76, v[12:13]\n" \
"v_mfma_f32_16x16x32_bf16 v[164:167], a[8:11], a[128:131], v[164:167]\n" \
"v_mfma_f32_16x16x32_bf16 v[168:171], a[16:19], a[128:131], v[168:171]\n" \
"ds_read_b64_tr_b16 a[148:149], v3 offset:18432\n" \
"v_mfma_f32_16x16x32_bf16 v[168:171], a[20:23], a[132:135], v[168:171]\n" \
"v_mfma_f32_16x16x32_bf16 v[172:175], a[28:31], a[132:135], v[172:175]\n" \
"ds_read_b64_tr_b16 a[150:151], v3 offset:18944\n" \
"v_mfma_f32_16x16x32_bf16 v[172:175], a[24:27], a[128:131], v[172:175]\n" \
"v_mfma_f32_16x16x32_bf16 v[176:179], a[32:35], a[128:131], v[176:179]\n" \
"v_mfma_f32_16x16x32_bf16 v[176:179], a[36:39], a[132:135], v[176:179]\n" \
"v_mfma_f32_16x16x32_bf16 v[180:183], a[44:47], a[132:135], v[180:183]\n" \
"v_mfma_f32_16x16x32_bf16 v[180:183], a[40:43], a[128:131], v[180:183]\n" \
"v_mfma_f32_16x16x32_bf16 v[184:187], a[48:51], a[128:131], v[184:187]\n" \
"v_mfma_f32_16x16x32_bf16 v[184:187], a[52:55], a[132:135], v[184:187]\n" \
"v_mfma_f32_16x16x32_bf16 v[188:191], a[60:63], a[132:135], v[188:191]\n" \
"v_mfma_f32_16x16x32_bf16 v[188:191], a[56:59], a[128:131], v[188:191]\n" \
"s_waitcnt lgkmcnt(4)\n" \
"v_mfma_f32_16x16x32_bf16 a[188:191], a[56:59], a[136:139], a[188:191]\n" \
"ds_read_b64_tr_b16 a[152:153], v4 offset:2048\n" \
"v_mfma_f32_16x16x32_bf16 a[188:191], a[60:63], a[140:143], a[188:191]\n" \
"v_mfma_f32_16x16x32_bf16 a[184:187], a[52:55], a[140:143], a[184:187]\n" \
"ds_read_b64_tr_b16 a[154:155], v4 offset:2560\n" \
"v_mfma_f32_16x16x32_bf16 a[184:187], a[48:51], a[136:139], a[184:187]\n" \
"v_mfma_f32_16x16x32_bf16 a[180:183], a[40:43], a[136:139], a[180:183]\n" \
"ds_read_b64_tr_b16 a[156:157], v4 offset:18432\n" \
"v_mfma_f32_16x16x32_bf16 a[180:183], a[44:47], a[140:143], a[180:183]\n" \
"v_mfma_f32_16x16x32_bf16 a[176:179], a[36:39], a[140:143], a[176:179]\n" \
"ds_read_b64_tr_b16 a[158:159], v4 offset:18944\n" \
"v_mfma_f32_16x16x32_bf16 a[176:179], a[32:35], a[136:139], a[176:179]\n" \
"v_mfma_f32_16x16x32_bf16 a[172:175], a[24:27], a[136:139], a[172:175]\n" \
"v_mfma_f32_16x16x32_bf16 a[172:175], a[28:31], a[140:143], a[172:175]\n" \
"v_mfma_f32_16x16x32_bf16 a[168:171], a[20:23], a[140:143], a[168:171]\n" \
"v_mfma_f32_16x16x32_bf16 a[168:171], a[16:19], a[136:139], a[168:171]\n" \
"v_mfma_f32_16x16x32_bf16 a[164:167], a[8:11], a[136:139], a[164:167]\n" \
"v_mfma_f32_16x16x32_bf16 a[164:167], a[12:15], a[140:143], a[164:167]\n" \
"v_mfma_f32_16x16x32_bf16 a[160:163], a[4:7], a[140:143], a[160:163]\n" \
"v_mfma_f32_16x16x32_bf16 a[160:163], a[0:3], a[136:139], a[160:163]\n" \
"s_waitcnt lgkmcnt(4)\n" \
"v_mfma_f32_16x16x32_bf16 a[192:195], a[0:3], a[144:147], a[192:195]\n" \
"ds_read_b64_tr_b16 a[128:129], v5 offset:0\n" \
"v_mfma_f32_16x16x32_bf16 a[192:195], a[4:7], a[148:151], a[192:195]\n" \
"v_mfma_f32_16x16x32_bf16 a[196:199], a[12:15], a[148:151], a[196:199]\n" \
"ds_read_b64_tr_b16 a[130:131], v5 offset:256\n" \
"v_mfma_f32_16x16x32_bf16 a[196:199], a[8:11], a[144:147], a[196:199]\n" \
"v_mfma_f32_16x16x32_bf16 a[200:203], a[16:19], a[144:147], a[200:203]\n" \
"ds_read_b64_tr_b16 a[132:133], v5 offset:4096\n" \
"v_mfma_f32_16x16x32_bf16 a[200:203], a[20:23], a[148:151], a[200:203]\n" \
"v_mfma_f32_16x16x32_bf16 a[204:207], a[28:31], a[148:151], a[204:207]\n" \
"ds_read_b64_tr_b16 a[134:135], v5 offset:4352\n" \
"v_mfma_f32_16x16x32_bf16 a[204:207], a[24:27], a[144:147], a[204:207]\n" \
"v_mfma_f32_16x16x32_bf16 a[208:211], a[32:35], a[144:147], a[208:211]\n" \
"v_mfma_f32_16x16x32_bf16 a[208:211], a[36:39], a[148:151], a[208:211]\n" \
"v_mfma_f32_16x16x32_bf16 a[212:215], a[44:47], a[148:151], a[212:215]\n" \
"v_mfma_f32_16x16x32_bf16 a[212:215], a[40:43], a[144:147], a[212:215]\n" \
"v_mfma_f32_16x16x32_bf16 a[216:219], a[48:51], a[144:147], a[216:219]\n" \
"v_mfma_f32_16x16x32_bf16 a[216:219], a[52:55], a[148:151], a[216:219]\n" \
"v_mfma_f32_16x16x32_bf16 a[220:223], a[60:63], a[148:151], a[220:223]\n" \
"v_mfma_f32_16x16x32_bf16 a[220:223], a[56:59], a[144:147], a[220:223]\n" \
"s_waitcnt lgkmcnt(4)\n" \
"v_mfma_f32_16x16x32_bf16 a[252:255], a[56:59], a[152:155], a[252:255]\n" \
"ds_read_b64_tr_b16 a[136:137], v6 offset:0\n" \
"v_mfma_f32_16x16x32_bf16 a[252:255], a[60:63], a[156:159], a[252:255]\n" \
"v_mfma_f32_16x16x32_bf16 a[248:251], a[52:55], a[156:159], a[248:251]\n" \
"ds_read_b64_tr_b16 a[138:139], v6 offset:256\n" \
"v_mfma_f32_16x16x32_bf16 a[248:251], a[48:51], a[152:155], a[248:251]\n" \
"v_mfma_f32_16x16x32_bf16 a[244:247], a[40:43], a[152:155], a[244:247]\n" \
"ds_read_b64_tr_b16 a[140:141], v6 offset:4096\n" \
"v_mfma_f32_16x16x32_bf16 a[244:247], a[44:47], a[156:159], a[244:247]\n" \
"v_mfma_f32_16x16x32_bf16 a[240:243], a[36:39], a[156:159], a[240:243]\n" \
"ds_read_b64_tr_b16 a[142:143], v6 offset:4352\n" \
"v_mfma_f32_16x16x32_bf16 a[240:243], a[32:35], a[152:155], a[240:243]\n" \
"v_mfma_f32_16x16x32_bf16 a[236:239], a[24:27], a[152:155], a[236:239]\n" \
"v_mfma_f32_16x16x32_bf16 a[236:239], a[28:31], a[156:159], a[236:239]\n" \
"v_mfma_f32_16x16x32_bf16 a[232:235], a[20:23], a[156:159], a[232:235]\n" \
"v_mfma_f32_16x16x32_bf16 a[232:235], a[16:19], a[152:155], a[232:235]\n" \
"v_mfma_f32_16x16x32_bf16 a[228:231], a[8:11], a[152:155], a[228:231]\n" \
"v_mfma_f32_16x16x32_bf16 a[228:231], a[12:15], a[156:159], a[228:231]\n" \
"v_mfma_f32_16x16x32_bf16 a[224:227], a[4:7], a[156:159], a[224:227]\n" \
"v_mfma_f32_16x16x32_bf16 a[224:227], a[0:3], a[152:155], a[224:227]\n" \
"s_waitcnt vmcnt(1) lgkmcnt(0)\n" \
"s_barrier\n" \
"v_mfma_f32_16x16x32_bf16 v[192:195], a[128:131], a[0:3], v[192:195]\n" \
"ds_read_b64_tr_b16 a[144:145], v1 offset:32768\n" \
"v_mfma_f32_16x16x32_bf16 v[192:195], a[132:135], a[4:7], v[192:195]\n" \
"ds_read_b64_tr_b16 a[146:147], v1 offset:33280\n" \
"v_mfma_f32_16x16x32_bf16 v[196:199], a[132:135], a[12:15], v[196:199]\n" \
"ds_read_b64_tr_b16 a[148:149], v1 offset:49152\n" \
"v_mfma_f32_16x16x32_bf16 v[196:199], a[128:131], a[8:11], v[196:199]\n" \
"ds_read_b64_tr_b16 a[150:151], v1 offset:49664\n" \
"v_mfma_f32_16x16x32_bf16 v[200:203], a[128:131], a[16:19], v[200:203]\n" \
"ds_read_b128 a[64:67], v0 offset:32768\n" \
"v_mfma_f32_16x16x32_bf16 v[200:203], a[132:135], a[20:23], v[200:203]\n" \
"ds_read_b128 a[68:71], v0 offset:33792\n" \
"v_mfma_f32_16x16x32_bf16 v[204:207], a[132:135], a[28:31], v[204:207]\n" \
"ds_read_b128 a[72:75], v0 offset:34816\n" \
"v_mfma_f32_16x16x32_bf16 v[204:207], a[128:131], a[24:27], v[204:207]\n" \
"ds_read_b128 a[76:79], v0 offset:35840\n" \
"v_mfma_f32_16x16x32_bf16 v[208:211], a[128:131], a[32:35], v[208:211]\n" \
"ds_read_b128 a[80:83], v0 offset:36864\n" \
"v_mfma_f32_16x16x32_bf16 v[208:211], a[132:135], a[36:39], v[208:211]\n" \
"ds_read_b128 a[84:87], v0 offset:37888\n" \
"v_mfma_f32_16x16x32_bf16 v[212:215], a[132:135], a[44:47], v[212:215]\n" \
"ds_read_b128 a[88:91], v0 offset:38912\n" \
"v_mfma_f32_16x16x32_bf16 v[212:215], a[128:131], a[40:43], v[212:215]\n" \
"ds_read_b128 a[92:95], v0 offset:39936\n" \
"v_mfma_f32_16x16x32_bf16 v[216:219], a[128:131], a[48:51], v[216:219]\n" \
"ds_read_b128 a[96:99], v0 offset:40960\n" \
"v_mfma_f32_16x16x32_bf16 v[216:219], a[132:135], a[52:55], v[216:219]\n" \
"ds_read_b128 a[100:103], v0 offset:41984\n" \
"v_mfma_f32_16x16x32_bf16 v[220:223], a[132:135], a[60:63], v[220:223]\n" \
"ds_read_b128 a[104:107], v0 offset:43008\n" \
"v_mfma_f32_16x16x32_bf16 v[220:223], a[128:131], a[56:59], v[220:223]\n" \
"ds_read_b128 a[108:111], v0 offset:44032\n" \
"v_mfma_f32_16x16x32_bf16 v[252:255], a[136:139], a[56:59], v[252:255]\n" \
"ds_read_b128 a[112:115], v0 offset:45056\n" \
"v_mfma_f32_16x16x32_bf16 v[252:255], a[140:143], a[60:63], v[252:255]\n" \
"ds_read_b128 a[116:119], v0 offset:46080\n" \
"v_mfma_f32_16x16x32_bf16 v[248:251], a[140:143], a[52:55], v[248:251]\n" \
"ds_read_b128 a[120:123], v0 offset:47104\n" \
"v_mfma_f32_16x16x32_bf16 v[248:251], a[136:139], a[48:51], v[248:251]\n" \
"ds_read_b128 a[124:127], v0 offset:48128\n" \
"v_mfma_f32_16x16x32_bf16 v[244:247], a[136:139], a[40:43], v[244:247]\n" \
"ds_read_b64_tr_b16 a[152:153], v2 offset:32768\n" \
"v_mfma_f32_16x16x32_bf16 v[244:247], a[140:143], a[44:47], v[244:247]\n" \
"ds_read_b64_tr_b16 a[154:155], v2 offset:33280\n" \
"v_mfma_f32_16x16x32_bf16 v[240:243], a[140:143], a[36:39], v[240:243]\n" \
"ds_read_b64_tr_b16 a[156:157], v2 offset:49152\n" \
"v_mfma_f32_16x16x32_bf16 v[240:243], a[136:139], a[32:35], v[240:243]\n" \
"ds_read_b64_tr_b16 a[158:159], v2 offset:49664\n" \
"v_mfma_f32_16x16x32_bf16 v[236:239], a[136:139], a[24:27], v[236:239]\n" \
"s_add_u32 m0, s71, 0\n" \
"v_mfma_f32_16x16x32_bf16 v[236:239], a[140:143], a[28:31], v[236:239]\n" \
"buffer_load_dwordx4 v8, s[40:43], s65 offen offset:0 lds\n" \
"v_mfma_f32_16x16x32_bf16 v[232:235], a[140:143], a[20:23], v[232:235]\n" \
"v_mfma_f32_16x16x32_bf16 v[232:235], a[136:139], a[16:19], v[232:235]\n" \
"v_mfma_f32_16x16x32_bf16 v[228:231], a[136:139], a[8:11], v[228:231]\n" \
"v_mfma_f32_16x16x32_bf16 v[228:231], a[140:143], a[12:15], v[228:231]\n" \
"buffer_load_dwordx4 v10, s[40:43], s66 offen offset:1024 lds\n" \
"v_mfma_f32_16x16x32_bf16 v[224:227], a[140:143], a[4:7], v[224:227]\n" \
"v_mfma_f32_16x16x32_bf16 v[224:227], a[136:139], a[0:3], v[224:227]\n" \
"s_waitcnt lgkmcnt(4)\n" \
"v_mfma_f32_16x16x32_bf16 v[32:35], a[64:67], a[144:147], v[32:35]\n" \
"ds_read_b64_tr_b16 a[128:129], v3 offset:32768\n" \
"v_mfma_f32_16x16x32_bf16 v[32:35], a[68:71], a[148:151], v[32:35]\n" \
"buffer_load_dwordx4 v8, s[44:47], s65 offen offset:2048 lds\n" \
"v_mfma_f32_16x16x32_bf16 v[36:39], a[76:79], a[148:151], v[36:39]\n" \
"ds_read_b64_tr_b16 a[130:131], v3 offset:33280\n" \
"v_mfma_f32_16x16x32_bf16 v[36:39], a[72:75], a[144:147], v[36:39]\n" \
"v_mfma_f32_16x16x32_bf16 v[40:43], a[80:83], a[144:147], v[40:43]\n" \
"ds_read_b64_tr_b16 a[132:133], v3 offset:49152\n" \
"v_mfma_f32_16x16x32_bf16 v[40:43], a[84:87], a[148:151], v[40:43]\n" \
"buffer_load_dwordx4 v10, s[44:47], s66 offen offset:3072 lds\n" \
"v_mfma_f32_16x16x32_bf16 v[44:47], a[92:95], a[148:151], v[44:47]\n" \
"ds_read_b64_tr_b16 a[134:135], v3 offset:49664\n" \
"v_mfma_f32_16x16x32_bf16 v[44:47], a[88:91], a[144:147], v[44:47]\n" \
"v_mfma_f32_16x16x32_bf16 v[48:51], a[96:99], a[144:147], v[48:51]\n" \
"s_add_u32 m0, s71, 4096\n" \
"v_mfma_f32_16x16x32_bf16 v[48:51], a[100:103], a[148:151], v[48:51]\n" \
"buffer_load_dwordx4 v8, s[48:51], s65 offen offset:0 lds\n" \
"v_mfma_f32_16x16x32_bf16 v[52:55], a[108:111], a[148:151], v[52:55]\n" \
"v_mfma_f32_16x16x32_bf16 v[52:55], a[104:107], a[144:147], v[52:55]\n" \
"v_mfma_f32_16x16x32_bf16 v[56:59], a[112:115], a[144:147], v[56:59]\n" \
"v_mfma_f32_16x16x32_bf16 v[56:59], a[116:119], a[148:151], v[56:59]\n" \
"buffer_load_dwordx4 v10, s[48:51], s66 offen offset:1024 lds\n" \
"v_mfma_f32_16x16x32_bf16 v[60:63], a[124:127], a[148:151], v[60:63]\n" \
"v_mfma_f32_16x16x32_bf16 v[60:63], a[120:123], a[144:147], v[60:63]\n" \
"s_waitcnt lgkmcnt(4)\n" \
"v_mfma_f32_16x16x32_bf16 v[92:95], a[120:123], a[152:155], v[92:95]\n" \
"ds_read_b64_tr_b16 a[136:137], v4 offset:32768\n" \
"v_mfma_f32_16x16x32_bf16 v[92:95], a[124:127], a[156:159], v[92:95]\n" \
"buffer_load_dwordx4 v8, s[52:55], s65 offen offset:2048 lds\n" \
"v_mfma_f32_16x16x32_bf16 v[88:91], a[116:119], a[156:159], v[88:91]\n" \
"ds_read_b64_tr_b16 a[138:139], v4 offset:33280\n" \
"v_mfma_f32_16x16x32_bf16 v[88:91], a[112:115], a[152:155], v[88:91]\n" \
"v_mfma_f32_16x16x32_bf16 v[84:87], a[104:107], a[152:155], v[84:87]\n" \
"ds_read_b64_tr_b16 a[140:141], v4 offset:49152\n" \
"v_mfma_f32_16x16x32_bf16 v[84:87], a[108:111], a[156:159], v[84:87]\n" \
"buffer_load_dwordx4 v10, s[52:55], s66 offen offset:3072 lds\n" \
"v_mfma_f32_16x16x32_bf16 v[80:83], a[100:103], a[156:159], v[80:83]\n" \
"ds_read_b64_tr_b16 a[142:143], v4 offset:49664\n" \
"v_mfma_f32_16x16x32_bf16 v[80:83], a[96:99], a[152:155], v[80:83]\n" \
"v_mfma_f32_16x16x32_bf16 v[76:79], a[88:91], a[152:155], v[76:79]\n" \
"s_add_u32 m0, s72, 0\n" \
"v_mfma_f32_16x16x32_bf16 v[76:79], a[92:95], a[156:159], v[76:79]\n" \
"buffer_load_dwordx4 v9, s[56:59], s67 offen offset:0 lds\n" \
"v_mfma_f32_16x16x32_bf16 v[72:75], a[84:87], a[156:159], v[72:75]\n" \
"v_mfma_f32_16x16x32_bf16 v[72:75], a[80:83], a[152:155], v[72:75]\n" \
"v_mfma_f32_16x16x32_bf16 v[68:71], a[72:75], a[152:155], v[68:71]\n" \
"v_mfma_f32_16x16x32_bf16 v[68:71], a[76:79], a[156:159], v[68:71]\n" \
"buffer_load_dwordx4 v9, s[60:63], s67 offen offset:1024 lds\n" \
"s_add_u32 s65, s65, s68\n" \
"s_add_u32 s66, s66, s68\n" \
"s_add_u32 s67, s67, s69\n" \
"v_mfma_f32_16x16x32_bf16 v[64:67], a[68:71], a[156:159], v[64:67]\n" \
"v_mfma_f32_16x16x32_bf16 v[64:67], a[64:67], a[152:155], v[64:67]\n" \
"s_waitcnt lgkmcnt(4)\n" \
"v_mfma_f32_16x16x32_bf16 v[96:99], a[64:67], a[128:131], v[96:99]\n" \
"ds_read_b64_tr_b16 a[144:145], v1 offset:34816\n" \
"s_add_u32 m0, s70, 0\n" \
"v_mfma_f32_16x16x32_bf16 v[96:99], a[68:71], a[132:135], v[96:99]\n" \
"buffer_load_dwordx4 v7, s[36:39], s64 offen offset:0 sc1 lds\n" \
"v_mfma_f32_16x16x32_bf16 v[100:103], a[76:79], a[132:135], v[100:103]\n" \
"ds_read_b64_tr_b16 a[146:147], v1 offset:35328\n" \
"v_mfma_f32_16x16x32_bf16 v[100:103], a[72:75], a[128:131], v[100:103]\n" \
"v_mfma_f32_16x16x32_bf16 v[104:107], a[80:83], a[128:131], v[104:107]\n" \
"ds_read_b64_tr_b16 a[148:149], v1 offset:51200\n" \
"v_mfma_f32_16x16x32_bf16 v[104:107], a[84:87], a[132:135], v[104:107]\n" \
"buffer_load_dwordx4 v7, s[36:39], s64 offen offset:1024 sc1 lds\n" \
"v_mfma_f32_16x16x32_bf16 v[108:111], a[92:95], a[132:135], v[108:111]\n" \
"ds_read_b64_tr_b16 a[150:151], v1 offset:51712\n" \
"v_mfma_f32_16x16x32_bf16 v[108:111], a[88:91], a[128:131], v[108:111]\n" \
"v_mfma_f32_16x16x32_bf16 v[112:115], a[96:99], a[128:131], v[112:115]\n" \
"v_mfma_f32_16x16x32_bf16 v[112:115], a[100:103], a[132:135], v[112:115]\n" \
"buffer_load_dwordx4 v7, s[36:39], s64 offen offset:2048 sc1 lds\n" \
"v_mfma_f32_16x16x32_bf16 v[116:119], a[108:111], a[132:135], v[116:119]\n" \
"v_mfma_f32_16x16x32_bf16 v[116:119], a[104:107], a[128:131], v[116:119]\n" \
"v_mfma_f32_16x16x32_bf16 v[120:123], a[112:115], a[128:131], v[120:123]\n" \
"v_mfma_f32_16x16x32_bf16 v[120:123], a[116:119], a[132:135], v[120:123]\n" \
"buffer_load_dwordx4 v7, s[36:39], s64 offen offset:3072 sc1 lds\n" \
"v_mfma_f32_16x16x32_bf16 v[124:127], a[124:127], a[132:135], v[124:127]\n" \
"v_mfma_f32_16x16x32_bf16 v[124:127], a[120:123], a[128:131], v[124:127]\n" \
"s_waitcnt lgkmcnt(4)\n" \
"v_mfma_f32_16x16x32_bf16 v[156:159], a[120:123], a[136:139], v[156:159]\n" \
"ds_read_b64_tr_b16 a[152:153], v2 offset:34816\n" \
"s_add_u32 m0, s70, 4096\n" \
"s_add_u32 s74, s64, 4096\n" \
"v_mfma_f32_16x16x32_bf16 v[156:159], a[124:127], a[140:143], v[156:159]\n" \
"buffer_load_dwordx4 v7, s[36:39], s74 offen offset:0 sc1 lds\n" \
"v_mfma_f32_16x16x32_bf16 v[152:155], a[116:119], a[140:143], v[152:155]\n" \
"ds_read_b64_tr_b16 a[154:155], v2 offset:35328\n" \
"v_mfma_f32_16x16x32_bf16 v[152:155], a[112:115], a[136:139], v[152:155]\n" \
"v_mfma_f32_16x16x32_bf16 v[148:151], a[104:107], a[136:139], v[148:151]\n" \
"ds_read_b64_tr_b16 a[156:157], v2 offset:51200\n" \
"v_mfma_f32_16x16x32_bf16 v[148:151], a[108:111], a[140:143], v[148:151]\n" \
"buffer_load_dwordx4 v7, s[36:39], s74 offen offset:1024 sc1 lds\n" \
"v_mfma_f32_16x16x32_bf16 v[144:147], a[100:103], a[140:143], v[144:147]\n" \
"ds_read_b64_tr_b16 a[158:159], v2 offset:51712\n" \
"v_mfma_f32_16x16x32_bf16 v[144:147], a[96:99], a[136:139], v[144:147]\n" \
"v_mfma_f32_16x16x32_bf16 v[140:143], a[88:91], a[136:139], v[140:143]\n" \
"v_mfma_f32_16x16x32_bf16 v[140:143], a[92:95], a[140:143], v[140:143]\n" \
"buffer_load_dwordx4 v7, s[36:39], s74 offen offset:2048 sc1 lds\n" \
"v_mfma_f32_16x16x32_bf16 v[136:139], a[84:87], a[140:143], v[136:139]\n" \
"v_mfma_f32_16x16x32_bf16 v[136:139], a[80:83], a[136:139], v[136:139]\n" \
"v_mfma_f32_16x16x32_bf16 v[132:135], a[72:75], a[136:139], v[132:135]\n" \
"v_mfma_f32_16x16x32_bf16 v[132:135], a[76:79], a[140:143], v[132:135]\n" \
"buffer_load_dwordx4 v7, s[36:39], s74 offen offset:3072 sc1 lds\n" \
"s_add_u32 s64, s64, 32768\n" \
"v_mfma_f32_16x16x32_bf16 v[128:131], a[68:71], a[140:143], v[128:131]\n" \
"v_mfma_f32_16x16x32_bf16 v[128:131], a[64:67], a[136:139], v[128:131]\n" \
"s_waitcnt lgkmcnt(4)\n" \
"v_mfma_f32_16x16x32_bf16 v[160:163], a[64:67], a[144:147], v[160:163]\n" \
"ds_read_b64_tr_b16 a[128:129], v3 offset:34816\n" \
"s_mov_b32 m0, s77\n" \
"s_sub_i32 s75, s75, 1\n" \
"v_mfma_f32_16x16x32_bf16 v[160:163], a[68:71], a[148:151], v[160:163]\n" \
"s_cmp_lt_u32 s75, s27\n" \
"s_cselect_b32 s76, 1, 0\n" \
"v_mfma_f32_16x16x32_bf16 v[164:167], a[76:79], a[148:151], v[164:167]\n" \
"ds_read_b64_tr_b16 a[130:131], v3 offset:35328\n" \
"global_load_lds_dword v[12:13], off\n" \
"v_mad_u64_u32 v[12:13], vcc, v11, s76, v[12:13]\n" \
"v_mfma_f32_16x16x32_bf16 v[164:167], a[72:75], a[144:147], v[164:167]\n" \
"v_mfma_f32_16x16x32_bf16 v[168:171], a[80:83], a[144:147], v[168:171]\n" \
"ds_read_b64_tr_b16 a[132:133], v3 offset:51200\n" \
"v_mfma_f32_16x16x32_bf16 v[168:171], a[84:87], a[148:151], v[168:171]\n" \
"v_mfma_f32_16x16x32_bf16 v[172:175], a[92:95], a[148:151], v[172:175]\n" \
"ds_read_b64_tr_b16 a[134:135], v3 offset:51712\n" \
"v_mfma_f32_16x16x32_bf16 v[172:175], a[88:91], a[144:147], v[172:175]\n" \
"v_mfma_f32_16x16x32_bf16 v[176:179], a[96:99], a[144:147], v[176:179]\n" \
"v_mfma_f32_16x16x32_bf16 v[176:179], a[100:103], a[148:151], v[176:179]\n" \
"v_mfma_f32_16x16x32_bf16 v[180:183], a[108:111], a[148:151], v[180:183]\n" \
"v_mfma_f32_16x16x32_bf16 v[180:183], a[104:107], a[144:147], v[180:183]\n" \
"v_mfma_f32_16x16x32_bf16 v[184:187], a[112:115], a[144:147], v[184:187]\n" \
"v_mfma_f32_16x16x32_bf16 v[184:187], a[116:119], a[148:151], v[184:187]\n" \
"v_mfma_f32_16x16x32_bf16 v[188:191], a[124:127], a[148:151], v[188:191]\n" \
"v_mfma_f32_16x16x32_bf16 v[188:191], a[120:123], a[144:147], v[188:191]\n" \
"s_waitcnt lgkmcnt(4)\n" \
"v_mfma_f32_16x16x32_bf16 a[188:191], a[120:123], a[152:155], a[188:191]\n" \
"ds_read_b64_tr_b16 a[136:137], v4 offset:34816\n" \
"v_mfma_f32_16x16x32_bf16 a[188:191], a[124:127], a[156:159], a[188:191]\n" \
"v_mfma_f32_16x16x32_bf16 a[184:187], a[116:119], a[156:159], a[184:187]\n" \
"ds_read_b64_tr_b16 a[138:139], v4 offset:35328\n" \
"v_mfma_f32_16x16x32_bf16 a[184:187], a[112:115], a[152:155], a[184:187]\n" \
"v_mfma_f32_16x16x32_bf16 a[180:183], a[104:107], a[152:155], a[180:183]\n" \
"ds_read_b64_tr_b16 a[140:141], v4 offset:51200\n" \
"v_mfma_f32_16x16x32_bf16 a[180:183], a[108:111], a[156:159], a[180:183]\n" \
"v_mfma_f32_16x16x32_bf16 a[176:179], a[100:103], a[156:159], a[176:179]\n" \
"ds_read_b64_tr_b16 a[142:143], v4 offset:51712\n" \
"v_mfma_f32_16x16x32_bf16 a[176:179], a[96:99], a[152:155], a[176:179]\n" \
"v_mfma_f32_16x16x32_bf16 a[172:175], a[88:91], a[152:155], a[172:175]\n" \
"v_mfma_f32_16x16x32_bf16 a[172:175], a[92:95], a[156:159], a[172:175]\n" \
"v_mfma_f32_16x16x32_bf16 a[168:171], a[84:87], a[156:159], a[168:171]\n" \
"v_mfma_f32_16x16x32_bf16 a[168:171], a[80:83], a[152:155], a[168:171]\n" \
"v_mfma_f32_16x16x32_bf16 a[164:167], a[72:75], a[152:155], a[164:167]\n" \
"v_mfma_f32_16x16x32_bf16 a[164:167], a[76:79], a[156:159], a[164:167]\n" \
"v_mfma_f32_16x16x32_bf16 a[160:163], a[68:71], a[156:159], a[160:163]\n" \
"v_mfma_f32_16x16x32_bf16 a[160:163], a[64:67], a[152:155], a[160:163]\n" \
"s_waitcnt lgkmcnt(4)\n" \
"v_mfma_f32_16x16x32_bf16 a[192:195], a[64:67], a[128:131], a[192:195]\n" \
"ds_read_b64_tr_b16 a[144:145], v5 offset:8192\n" \
"v_mfma_f32_16x16x32_bf16 a[192:195], a[68:71], a[132:135], a[192:195]\n" \
"v_mfma_f32_16x16x32_bf16 a[196:199], a[76:79], a[132:135], a[196:199]\n" \
"ds_read_b64_tr_b16 a[146:147], v5 offset:8448\n" \
"v_mfma_f32_16x16x32_bf16 a[196:199], a[72:75], a[128:131], a[196:199]\n" \
"v_mfma_f32_16x16x32_bf16 a[200:203], a[80:83], a[128:131], a[200:203]\n" \
"ds_read_b64_tr_b16 a[148:149], v5 offset:12288\n" \
"v_mfma_f32_16x16x32_bf16 a[200:203], a[84:87], a[132:135], a[200:203]\n" \
"v_mfma_f32_16x16x32_bf16 a[204:207], a[92:95], a[132:135], a[204:207]\n" \
"ds_read_b64_tr_b16 a[150:151], v5 offset:12544\n" \
"v_mfma_f32_16x16x32_bf16 a[204:207], a[88:91], a[128:131], a[204:207]\n" \
"v_mfma_f32_16x16x32_bf16 a[208:211], a[96:99], a[128:131], a[208:211]\n" \
"v_mfma_f32_16x16x32_bf16 a[208:211], a[100:103], a[132:135], a[208:211]\n" \
"v_mfma_f32_16x16x32_bf16 a[212:215], a[108:111], a[132:135], a[212:215]\n" \
"v_mfma_f32_16x16x32_bf16 a[212:215], a[104:107], a[128:131], a[212:215]\n" \
"v_mfma_f32_16x16x32_bf16 a[216:219], a[112:115], a[128:131], a[216:219]\n" \
"v_mfma_f32_16x16x32_bf16 a[216:219], a[116:119], a[132:135], a[216:219]\n" \
"v_mfma_f32_16x16x32_bf16 a[220:223], a[124:127], a[132:135], a[220:223]\n" \
"v_mfma_f32_16x16x32_bf16 a[220:223], a[120:123], a[128:131], a[220:223]\n" \
"s_waitcnt lgkmcnt(4)\n" \
"v_mfma_f32_16x16x32_bf16 a[252:255], a[120:123], a[136:139], a[252:255]\n" \
"ds_read_b64_tr_b16 a[152:153], v6 offset:8192\n" \
"v_mfma_f32_16x16x32_bf16 a[252:255], a[124:127], a[140:143], a[252:255]\n" \
"v_mfma_f32_16x16x32_bf16 a[248:251], a[116:119], a[140:143], a[248:251]\n" \
"ds_read_b64_tr_b16 a[154:155], v6 offset:8448\n" \
"v_mfma_f32_16x16x32_bf16 a[248:251], a[112:115], a[136:139], a[248:251]\n" \
"v_mfma_f32_16x16x32_bf16 a[244:247], a[104:107], a[136:139], a[244:247]\n" \
"ds_read_b64_tr_b16 a[156:157], v6 offset:12288\n" \
"v_mfma_f32_16x16x32_bf16 a[244:247], a[108:111], a[140:143], a[244:247]\n" \
"v_mfma_f32_16x16x32_bf16 a[240:243], a[100:103], a[140:143], a[240:243]\n" \
"ds_read_b64_tr_b16 a[158:159], v6 offset:12544\n" \
"v_mfma_f32_16x16x32_bf16 a[240:243], a[96:99], a[136:139], a[240:243]\n" \
"v_mfma_f32_16x16x32_bf16 a[236:239], a[88:91], a[136:139], a[236:239]\n" \
"v_mfma_f32_16x16x32_bf16 a[236:239], a[92:95], a[140:143], a[236:239]\n" \
"v_mfma_f32_16x16x32_bf16 a[232:235], a[84:87], a[140:143], a[232:235]\n" \
"v_mfma_f32_16x16x32_bf16 a[232:235], a[80:83], a[136:139], a[232:235]\n" \
"v_mfma_f32_16x16x32_bf16 a[228:231], a[72:75], a[136:139], a[228:231]\n" \
"v_mfma_f32_16x16x32_bf16 a[228:231], a[76:79], a[140:143], a[228:231]\n" \
"v_mfma_f32_16x16x32_bf16 a[224:227], a[68:71], a[140:143], a[224:227]\n" \
"v_mfma_f32_16x16x32_bf16 a[224:227], a[64:67], a[136:139], a[224:227]\n" \
"s_waitcnt vmcnt(1) lgkmcnt(0)\n" \
"s_barrier\n" \
"v_mfma_f32_16x16x32_bf16 v[192:195], a[144:147], a[64:67], v[192:195]\n" \
"ds_read_b64_tr_b16 a[128:129], v1 offset:0\n" \
"v_mfma_f32_16x16x32_bf16 v[192:195], a[148:151], a[68:71], v[192:195]\n" \
"ds_read_b64_tr_b16 a[130:131], v1 offset:512\n" \
"v_mfma_f32_16x16x32_bf16 v[196:199], a[148:151], a[76:79], v[196:199]\n" \
"ds_read_b64_tr_b16 a[132:133], v1 offset:16384\n" \
"v_mfma_f32_16x16x32_bf16 v[196:199], a[144:147], a[72:75], v[196:199]\n" \
"ds_read_b64_tr_b16 a[134:135], v1 offset:16896\n" \
"v_mfma_f32_16x16x32_bf16 v[200:203], a[144:147], a[80:83], v[200:203]\n" \
"ds_read_b128 a[0:3], v0 offset:0\n" \
"v_mfma_f32_16x16x32_bf16 v[200:203], a[148:151], a[84:87], v[200:203]\n" \
"ds_read_b128 a[4:7], v0 offset:1024\n" \
"v_mfma_f32_16x16x32_bf16 v[204:207], a[148:151], a[92:95], v[204:207]\n" \
"ds_read_b128 a[8:11], v0 offset:2048\n" \
"v_mfma_f32_16x16x32_bf16 v[204:207], a[144:147], a[88:91], v[204:207]\n" \
"ds_read_b128 a[12:15], v0 offset:3072\n" \
"v_mfma_f32_16x16x32_bf16 v[208:211], a[144:147], a[96:99], v[208:211]\n" \
"ds_read_b128 a[16:19], v0 offset:4096\n" \
"v_mfma_f32_16x16x32_bf16 v[208:211], a[148:151], a[100:103], v[208:211]\n" \
"ds_read_b128 a[20:23], v0 offset:5120\n" \
"v_mfma_f32_16x16x32_bf16 v[212:215], a[148:151], a[108:111], v[212:215]\n" \
"ds_read_b128 a[24:27], v0 offset:6144\n" \
"v_mfma_f32_16x16x32_bf16 v[212:215], a[144:147], a[104:107], v[212:215]\n" \
"ds_read_b128 a[28:31], v0 offset:7168\n" \
"v_mfma_f32_16x16x32_bf16 v[216:219], a[144:147], a[112:115], v[216:219]\n" \
"ds_read_b128 a[32:35], v0 offset:8192\n" \
"v_mfma_f32_16x16x32_bf16 v[216:219], a[148:151], a[116:119], v[216:219]\n" \
"ds_read_b128 a[36:39], v0 offset:9216\n" \
"v_mfma_f32_16x16x32_bf16 v[220:223], a[148:151], a[124:127], v[220:223]\n" \
"ds_read_b128 a[40:43], v0 offset:10240\n" \
"v_mfma_f32_16x16x32_bf16 v[220:223], a[144:147], a[120:123], v[220:223]\n" \
"ds_read_b128 a[44:47], v0 offset:11264\n" \
"v_mfma_f32_16x16x32_bf16 v[252:255], a[152:155], a[120:123], v[252:255]\n" \
"ds_read_b128 a[48:51], v0 offset:12288\n" \
"v_mfma_f32_16x16x32_bf16 v[252:255], a[156:159], a[124:127], v[252:255]\n" \
"ds_read_b128 a[52:55], v0 offset:13312\n" \
"v_mfma_f32_16x16x32_bf16 v[248:251], a[156:159], a[116:119], v[248:251]\n" \
"ds_read_b128 a[56:59], v0 offset:14336\n" \
"v_mfma_f32_16x16x32_bf16 v[248:251], a[152:155], a[112:115], v[248:251]\n" \
"ds_read_b128 a[60:63], v0 offset:15360\n" \
"v_mfma_f32_16x16x32_bf16 v[244:247], a[152:155], a[104:107], v[244:247]\n" \
"ds_read_b64_tr_b16 a[136:137], v2 offset:0\n" \
"v_mfma_f32_16x16x32_bf16 v[244:247], a[156:159], a[108:111], v[244:247]\n" \
"ds_read_b64_tr_b16 a[138:139], v2 offset:512\n" \
"v_mfma_f32_16x16x32_bf16 v[240:243], a[156:159], a[100:103], v[240:243]\n" \
"ds_read_b64_tr_b16 a[140:141], v2 offset:16384\n" \
"v_mfma_f32_16x16x32_bf16 v[240:243], a[152:155], a[96:99], v[240:243]\n" \
"ds_read_b64_tr_b16 a[142:143], v2 offset:16896\n" \
"v_mfma_f32_16x16x32_bf16 v[236:239], a[152:155], a[88:91], v[236:239]\n" \
"s_add_u32 m0, s71, 32768\n" \
"v_mfma_f32_16x16x32_bf16 v[236:239], a[156:159], a[92:95], v[236:239]\n" \
"buffer_load_dwordx4 v8, s[40:43], s65 offen offset:0 lds\n" \
"v_mfma_f32_16x16x32_bf16 v[232:235], a[156:159], a[84:87], v[232:235]\n" \
"v_mfma_f32_16x16x32_bf16 v[232:235], a[152:155], a[80:83], v[232:235]\n" \
"v_mfma_f32_16x16x32_bf16 v[228:231], a[152:155], a[72:75], v[228:231]\n" \
"v_mfma_f32_16x16x32_bf16 v[228:231], a[156:159], a[76:79], v[228:231]\n" \
"buffer_load_dwordx4 v10, s[40:43], s66 offen offset:1024 lds\n" \
"v_mfma_f32_16x16x32_bf16 v[224:227], a[156:159], a[68:71], v[224:227]\n" \
"v_mfma_f32_16x16x32_bf16 v[224:227], a[152:155], a[64:67], v[224:227]\n" \
"s_sub_u32 s73, s73, 1\n" \
"s_cmp_lg_u32 s73, 0\n" \
"s_cbranch_scc1 L_g1k_%=\n" \
"s_waitcnt lgkmcnt(4)\n" \
"v_mfma_f32_16x16x32_bf16 v[32:35], a[0:3], a[128:131], v[32:35]\n" \
"ds_read_b64_tr_b16 a[144:145], v3 offset:0\n" \
"v_mfma_f32_16x16x32_bf16 v[32:35], a[4:7], a[132:135], v[32:35]\n" \
"buffer_load_dwordx4 v8, s[44:47], s65 offen offset:2048 lds\n" \
"v_mfma_f32_16x16x32_bf16 v[36:39], a[12:15], a[132:135], v[36:39]\n" \
"ds_read_b64_tr_b16 a[146:147], v3 offset:512\n" \
"v_mfma_f32_16x16x32_bf16 v[36:39], a[8:11], a[128:131], v[36:39]\n" \
"v_mfma_f32_16x16x32_bf16 v[40:43], a[16:19], a[128:131], v[40:43]\n" \
"ds_read_b64_tr_b16 a[148:149], v3 offset:16384\n" \
"v_mfma_f32_16x16x32_bf16 v[40:43], a[20:23], a[132:135], v[40:43]\n" \
"buffer_load_dwordx4 v10, s[44:47], s66 offen offset:3072 lds\n" \
"v_mfma_f32_16x16x32_bf16 v[44:47], a[28:31], a[132:135], v[44:47]\n" \
"ds_read_b64_tr_b16 a[150:151], v3 offset:16896\n" \
"v_mfma_f32_16x16x32_bf16 v[44:47], a[24:27], a[128:131], v[44:47]\n" \
"v_mfma_f32_16x16x32_bf16 v[48:51], a[32:35], a[128:131], v[48:51]\n" \
"s_add_u32 m0, s71, 36864\n" \
"v_mfma_f32_16x16x32_bf16 v[48:51], a[36:39], a[132:135], v[48:51]\n" \
"buffer_load_dwordx4 v8, s[48:51], s65 offen offset:0 lds\n" \
"v_mfma_f32_16x16x32_bf16 v[52:55], a[44:47], a[132:135], v[52:55]\n" \
"v_mfma_f32_16x16x32_bf16 v[52:55], a[40:43], a[128:131], v[52:55]\n" \
"v_mfma_f32_16x16x32_bf16 v[56:59], a[48:51], a[128:131], v[56:59]\n" \
"v_mfma_f32_16x16x32_bf16 v[56:59], a[52:55], a[132:135], v[56:59]\n" \
"buffer_load_dwordx4 v10, s[48:51], s66 offen offset:1024 lds\n" \
"v_mfma_f32_16x16x32_bf16 v[60:63], a[60:63], a[132:135], v[60:63]\n" \
"v_mfma_f32_16x16x32_bf16 v[60:63], a[56:59], a[128:131], v[60:63]\n" \
"s_waitcnt lgkmcnt(4)\n" \
"v_mfma_f32_16x16x32_bf16 v[92:95], a[56:59], a[136:139], v[92:95]\n" \
"ds_read_b64_tr_b16 a[152:153], v4 offset:0\n" \
"v_mfma_f32_16x16x32_bf16 v[92:95], a[60:63], a[140:143], v[92:95]\n" \
"buffer_load_dwordx4 v8, s[52:55], s65 offen offset:2048 lds\n" \
"v_mfma_f32_16x16x32_bf16 v[88:91], a[52:55], a[140:143], v[88:91]\n" \
"ds_read_b64_tr_b16 a[154:155], v4 offset:512\n" \
"v_mfma_f32_16x16x32_bf16 v[88:91], a[48:51], a[136:139], v[88:91]\n" \
"v_mfma_f32_16x16x32_bf16 v[84:87], a[40:43], a[136:139], v[84:87]\n" \
"ds_read_b64_tr_b16 a[156:157], v4 offset:16384\n" \
"v_mfma_f32_16x16x32_bf16 v[84:87], a[44:47], a[140:143], v[84:87]\n" \
"buffer_load_dwordx4 v10, s[52:55], s66 offen offset:3072 lds\n" \
"v_mfma_f32_16x16x32_bf16 v[80:83], a[36:39], a[140:143], v[80:83]\n" \
"ds_read_b64_tr_b16 a[158:159], v4 offset:16896\n" \
"v_mfma_f32_16x16x32_bf16 v[80:83], a[32:35], a[136:139], v[80:83]\n" \
"v_mfma_f32_16x16x32_bf16 v[76:79], a[24:27], a[136:139], v[76:79]\n" \
"s_add_u32 m0, s72, 8192\n" \
"v_mfma_f32_16x16x32_bf16 v[76:79], a[28:31], a[140:143], v[76:79]\n" \
"buffer_load_dwordx4 v9, s[56:59], s67 offen offset:0 lds\n" \
"v_mfma_f32_16x16x32_bf16 v[72:75], a[20:23], a[140:143], v[72:75]\n" \
"v_mfma_f32_16x16x32_bf16 v[72:75], a[16:19], a[136:139], v[72:75]\n" \
"v_mfma_f32_16x16x32_bf16 v[68:71], a[8:11], a[136:139], v[68:71]\n" \
"v_mfma_f32_16x16x32_bf16 v[68:71], a[12:15], a[140:143], v[68:71]\n" \
"buffer_load_dwordx4 v9, s[60:63], s67 offen offset:1024 lds\n" \
"s_add_u32 s65, s65, s68\n" \
"s_add_u32 s66, s66, s68\n" \
"s_add_u32 s67, s67, s69\n" \
"v_mfma_f32_16x16x32_bf16 v[64:67], a[4:7], a[140:143], v[64:67]\n" \
"v_mfma_f32_16x16x32_bf16 v[64:67], a[0:3], a[136:139], v[64:67]\n" \
"s_waitcnt lgkmcnt(4)\n" \
"v_mfma_f32_16x16x32_bf16 v[96:99], a[0:3], a[144:147], v[96:99]\n" \
"ds_read_b64_tr_b16 a[128:129], v1 offset:2048\n" \
"s_add_u32 m0, s70, 32768\n" \
"v_mfma_f32_16x16x32_bf16 v[96:99], a[4:7], a[148:151], v[96:99]\n" \
"buffer_load_dwordx4 v7, s[36:39], s64 offen offset:0 sc1 lds\n" \
"v_mfma_f32_16x16x32_bf16 v[100:103], a[12:15], a[148:151], v[100:103]\n" \
"ds_read_b64_tr_b16 a[130:131], v1 offset:2560\n" \
"v_mfma_f32_16x16x32_bf16 v[100:103], a[8:11], a[144:147], v[100:103]\n" \
"v_mfma_f32_16x16x32_bf16 v[104:107], a[16:19], a[144:147], v[104:107]\n" \
"ds_read_b64_tr_b16 a[132:133], v1 offset:18432\n" \
"v_mfma_f32_16x16x32_bf16 v[104:107], a[20:23], a[148:151], v[104:107]\n" \
"buffer_load_dwordx4 v7, s[36:39], s64 offen offset:1024 sc1 lds\n" \
"v_mfma_f32_16x16x32_bf16 v[108:111], a[28:31], a[148:151], v[108:111]\n" \
"ds_read_b64_tr_b16 a[134:135], v1 offset:18944\n" \
"v_mfma_f32_16x16x32_bf16 v[108:111], a[24:27], a[144:147], v[108:111]\n" \
"v_mfma_f32_16x16x32_bf16 v[112:115], a[32:35], a[144:147], v[112:115]\n" \
"v_mfma_f32_16x16x32_bf16 v[112:115], a[36:39], a[148:151], v[112:115]\n" \
"buffer_load_dwordx4 v7, s[36:39], s64 offen offset:2048 sc1 lds\n" \
"v_mfma_f32_16x16x32_bf16 v[116:119], a[44:47], a[148:151], v[116:119]\n" \
"v_mfma_f32_16x16x32_bf16 v[116:119], a[40:43], a[144:147], v[116:119]\n" \
"v_mfma_f32_16x16x32_bf16 v[120:123], a[48:51], a[144:147], v[120:123]\n" \
"v_mfma_f32_16x16x32_bf16 v[120:123], a[52:55], a[148:151], v[120:123]\n" \
"buffer_load_dwordx4 v7, s[36:39], s64 offen offset:3072 sc1 lds\n" \
"v_mfma_f32_16x16x32_bf16 v[124:127], a[60:63], a[148:151], v[124:127]\n" \
"v_mfma_f32_16x16x32_bf16 v[124:127], a[56:59], a[144:147], v[124:127]\n" \
"s_waitcnt lgkmcnt(4)\n" \
"v_mfma_f32_16x16x32_bf16 v[156:159], a[56:59], a[152:155], v[156:159]\n" \
"ds_read_b64_tr_b16 a[136:137], v2 offset:2048\n" \
"s_add_u32 m0, s70, 36864\n" \
"s_add_u32 s74, s64, 4096\n" \
"v_mfma_f32_16x16x32_bf16 v[156:159], a[60:63], a[156:159], v[156:159]\n" \
"buffer_load_dwordx4 v7, s[36:39], s74 offen offset:0 sc1 lds\n" \
"v_mfma_f32_16x16x32_bf16 v[152:155], a[52:55], a[156:159], v[152:155]\n" \
"ds_read_b64_tr_b16 a[138:139], v2 offset:2560\n" \
"v_mfma_f32_16x16x32_bf16 v[152:155], a[48:51], a[152:155], v[152:155]\n" \
"v_mfma_f32_16x16x32_bf16 v[148:151], a[40:43], a[152:155], v[148:151]\n" \
"ds_read_b64_tr_b16 a[140:141], v2 offset:18432\n" \
"v_mfma_f32_16x16x32_bf16 v[148:151], a[44:47], a[156:159], v[148:151]\n" \
"buffer_load_dwordx4 v7, s[36:39], s74 offen offset:1024 sc1 lds\n" \
"v_mfma_f32_16x16x32_bf16 v[144:147], a[36:39], a[156:159], v[144:147]\n" \
"ds_read_b64_tr_b16 a[142:143], v2 offset:18944\n" \
"v_mfma_f32_16x16x32_bf16 v[144:147], a[32:35], a[152:155], v[144:147]\n" \
"v_mfma_f32_16x16x32_bf16 v[140:143], a[24:27], a[152:155], v[140:143]\n" \
"v_mfma_f32_16x16x32_bf16 v[140:143], a[28:31], a[156:159], v[140:143]\n" \
"buffer_load_dwordx4 v7, s[36:39], s74 offen offset:2048 sc1 lds\n" \
"v_mfma_f32_16x16x32_bf16 v[136:139], a[20:23], a[156:159], v[136:139]\n" \
"v_mfma_f32_16x16x32_bf16 v[136:139], a[16:19], a[152:155], v[136:139]\n" \
"v_mfma_f32_16x16x32_bf16 v[132:135], a[8:11], a[152:155], v[132:135]\n" \
"v_mfma_f32_16x16x32_bf16 v[132:135], a[12:15], a[156:159], v[132:135]\n" \
"buffer_load_dwordx4 v7, s[36:39], s74 offen offset:3072 sc1 lds\n" \
"s_add_u32 s64, s64, 32768\n" \
"v_mfma_f32_16x16x32_bf16 v[128:131], a[4:7], a[156:159], v[128:131]\n" \
"v_mfma_f32_16x16x32_bf16 v[128:131], a[0:3], a[152:155], v[128:131]\n" \
"s_waitcnt lgkmcnt(4)\n" \
"v_mfma_f32_16x16x32_bf16 v[160:163], a[0:3], a[128:131], v[160:163]\n" \
"ds_read_b64_tr_b16 a[144:145], v3 offset:2048\n" \
"s_mov_b32 m0, s77\n" \
"s_sub_i32 s75, s75, 1\n" \
"v_mfma_f32_16x16x32_bf16 v[160:163], a[4:7], a[132:135], v[160:163]\n" \
"s_cmp_lt_u32 s75, s27\n" \
"s_cselect_b32 s76, 1, 0\n" \
"v_mfma_f32_16x16x32_bf16 v[164:167], a[12:15], a[132:135], v[164:167]\n" \
"ds_read_b64_tr_b16 a[146:147], v3 offset:2560\n" \
"global_load_lds_dword v[12:13], off\n" \
"v_mad_u64_u32 v[12:13], vcc, v11, s76, v[12:13]\n" \
"v_mfma_f32_16x16x32_bf16 v[164:167], a[8:11], a[128:131], v[164:167]\n" \
"v_mfma_f32_16x16x32_bf16 v[168:171], a[16:19], a[128:131], v[168:171]\n" \
"ds_read_b64_tr_b16 a[148:149], v3 offset:18432\n" \
"v_mfma_f32_16x16x32_bf16 v[168:171], a[20:23], a[132:135], v[168:171]\n" \
"v_mfma_f32_16x16x32_bf16 v[172:175], a[28:31], a[132:135], v[172:175]\n" \
"ds_read_b64_tr_b16 a[150:151], v3 offset:18944\n" \
"v_mfma_f32_16x16x32_bf16 v[172:175], a[24:27], a[128:131], v[172:175]\n" \
"v_mfma_f32_16x16x32_bf16 v[176:179], a[32:35], a[128:131], v[176:179]\n" \
"v_mfma_f32_16x16x32_bf16 v[176:179], a[36:39], a[132:135], v[176:179]\n" \
"v_mfma_f32_16x16x32_bf16 v[180:183], a[44:47], a[132:135], v[180:183]\n" \
"v_mfma_f32_16x16x32_bf16 v[180:183], a[40:43], a[128:131], v[180:183]\n" \
"v_mfma_f32_16x16x32_bf16 v[184:187], a[48:51], a[128:131], v[184:187]\n" \
"v_mfma_f32_16x16x32_bf16 v[184:187], a[52:55], a[132:135], v[184:187]\n" \
"v_mfma_f32_16x16x32_bf16 v[188:191], a[60:63], a[132:135], v[188:191]\n" \
"v_mfma_f32_16x16x32_bf16 v[188:191], a[56:59], a[128:131], v[188:191]\n" \
"s_waitcnt lgkmcnt(4)\n" \
"v_mfma_f32_16x16x32_bf16 a[188:191], a[56:59], a[136:139], a[188:191]\n" \
"ds_read_b64_tr_b16 a[152:153], v4 offset:2048\n" \
"v_mfma_f32_16x16x32_bf16 a[188:191], a[60:63], a[140:143], a[188:191]\n" \
"v_mfma_f32_16x16x32_bf16 a[184:187], a[52:55], a[140:143], a[184:187]\n" \
"ds_read_b64_tr_b16 a[154:155], v4 offset:2560\n" \
"v_mfma_f32_16x16x32_bf16 a[184:187], a[48:51], a[136:139], a[184:187]\n" \
"v_mfma_f32_16x16x32_bf16 a[180:183], a[40:43], a[136:139], a[180:183]\n" \
"ds_read_b64_tr_b16 a[156:157], v4 offset:18432\n" \
"v_mfma_f32_16x16x32_bf16 a[180:183], a[44:47], a[140:143], a[180:183]\n" \
"v_mfma_f32_16x16x32_bf16 a[176:179], a[36:39], a[140:143], a[176:179]\n" \
"ds_read_b64_tr_b16 a[158:159], v4 offset:18944\n" \
"v_mfma_f32_16x16x32_bf16 a[176:179], a[32:35], a[136:139], a[176:179]\n" \
"v_mfma_f32_16x16x32_bf16 a[172:175], a[24:27], a[136:139], a[172:175]\n" \
"v_mfma_f32_16x16x32_bf16 a[172:175], a[28:31], a[140:143], a[172:175]\n" \
"v_mfma_f32_16x16x32_bf16 a[168:171], a[20:23], a[140:143], a[168:171]\n" \
"v_mfma_f32_16x16x32_bf16 a[168:171], a[16:19], a[136:139], a[168:171]\n" \
"v_mfma_f32_16x16x32_bf16 a[164:167], a[8:11], a[136:139], a[164:167]\n" \
"v_mfma_f32_16x16x32_bf16 a[164:167], a[12:15], a[140:143], a[164:167]\n" \
"v_mfma_f32_16x16x32_bf16 a[160:163], a[4:7], a[140:143], a[160:163]\n" \
"v_mfma_f32_16x16x32_bf16 a[160:163], a[0:3], a[136:139], a[160:163]\n" \
"s_waitcnt lgkmcnt(4)\n" \
"v_mfma_f32_16x16x32_bf16 a[192:195], a[0:3], a[144:147], a[192:195]\n" \
"ds_read_b64_tr_b16 a[128:129], v5 offset:0\n" \
"v_mfma_f32_16x16x32_bf16 a[192:195], a[4:7], a[148:151], a[192:195]\n" \
"v_mfma_f32_16x16x32_bf16 a[196:199], a[12:15], a[148:151], a[196:199]\n" \
"ds_read_b64_tr_b16 a[130:131], v5 offset:256\n" \
"v_mfma_f32_16x16x32_bf16 a[196:199], a[8:11], a[144:147], a[196:199]\n" \
"v_mfma_f32_16x16x32_bf16 a[200:203], a[16:19], a[144:147], a[200:203]\n" \
"ds_read_b64_tr_b16 a[132:133], v5 offset:4096\n" \
"v_mfma_f32_16x16x32_bf16 a[200:203], a[20:23], a[148:151], a[200:203]\n" \
"v_mfma_f32_16x16x32_bf16 a[204:207], a[28:31], a[148:151], a[204:207]\n" \
"ds_read_b64_tr_b16 a[134:135], v5 offset:4352\n" \
"v_mfma_f32_16x16x32_bf16 a[204:207], a[24:27], a[144:147], a[204:207]\n" \
"v_mfma_f32_16x16x32_bf16 a[208:211], a[32:35], a[144:147], a[208:211]\n" \
"v_mfma_f32_16x16x32_bf16 a[208:211], a[36:39], a[148:151], a[208:211]\n" \
"v_mfma_f32_16x16x32_bf16 a[212:215], a[44:47], a[148:151], a[212:215]\n" \
"v_mfma_f32_16x16x32_bf16 a[212:215], a[40:43], a[144:147], a[212:215]\n" \
"v_mfma_f32_16x16x32_bf16 a[216:219], a[48:51], a[144:147], a[216:219]\n" \
"v_mfma_f32_16x16x32_bf16 a[216:219], a[52:55], a[148:151], a[216:219]\n" \
"v_mfma_f32_16x16x32_bf16 a[220:223], a[60:63], a[148:151], a[220:223]\n" \
"v_mfma_f32_16x16x32_bf16 a[220:223], a[56:59], a[144:147], a[220:223]\n" \
"s_waitcnt lgkmcnt(4)\n" \
"v_mfma_f32_16x16x32_bf16 a[252:255], a[56:59], a[152:155], a[252:255]\n" \
"ds_read_b64_tr_b16 a[136:137], v6 offset:0\n" \
"v_mfma_f32_16x16x32_bf16 a[252:255], a[60:63], a[156:159], a[252:255]\n" \
"v_mfma_f32_16x16x32_bf16 a[248:251], a[52:55], a[156:159], a[248:251]\n" \
"ds_read_b64_tr_b16 a[138:139], v6 offset:256\n" \
"v_mfma_f32_16x16x32_bf16 a[248:251], a[48:51], a[152:155], a[248:251]\n" \
"v_mfma_f32_16x16x32_bf16 a[244:247], a[40:43], a[152:155], a[244:247]\n" \
"ds_read_b64_tr_b16 a[140:141], v6 offset:4096\n" \
"v_mfma_f32_16x16x32_bf16 a[244:247], a[44:47], a[156:159], a[244:247]\n" \
"v_mfma_f32_16x16x32_bf16 a[240:243], a[36:39], a[156:159], a[240:243]\n" \
"ds_read_b64_tr_b16 a[142:143], v6 offset:4352\n" \
"v_mfma_f32_16x16x32_bf16 a[240:243], a[32:35], a[152:155], a[240:243]\n" \
"v_mfma_f32_16x16x32_bf16 a[236:239], a[24:27], a[152:155], a[236:239]\n" \
"v_mfma_f32_16x16x32_bf16 a[236:239], a[28:31], a[156:159], a[236:239]\n" \
"v_mfma_f32_16x16x32_bf16 a[232:235], a[20:23], a[156:159], a[232:235]\n" \
"v_mfma_f32_16x16x32_bf16 a[232:235], a[16:19], a[152:155], a[232:235]\n" \
"v_mfma_f32_16x16x32_bf16 a[228:231], a[8:11], a[152:155], a[228:231]\n" \
"v_mfma_f32_16x16x32_bf16 a[228:231], a[12:15], a[156:159], a[228:231]\n" \
"v_mfma_f32_16x16x32_bf16 a[224:227], a[4:7], a[156:159], a[224:227]\n" \
"v_mfma_f32_16x16x32_bf16 a[224:227], a[0:3], a[152:155], a[224:227]\n" \
"s_waitcnt vmcnt(0) lgkmcnt(0)\n" \
"s_barrier\n" \
"v_mfma_f32_16x16x32_bf16 v[192:195], a[128:131], a[0:3], v[192:195]\n" \
"ds_read_b64_tr_b16 a[144:145], v1 offset:32768\n" \
"v_mfma_f32_16x16x32_bf16 v[192:195], a[132:135], a[4:7], v[192:195]\n" \
"ds_read_b64_tr_b16 a[146:147], v1 offset:33280\n" \
"v_mfma_f32_16x16x32_bf16 v[196:199], a[132:135], a[12:15], v[196:199]\n" \
"ds_read_b64_tr_b16 a[148:149], v1 offset:49152\n" \
"v_mfma_f32_16x16x32_bf16 v[196:199], a[128:131], a[8:11], v[196:199]\n" \
"ds_read_b64_tr_b16 a[150:151], v1 offset:49664\n" \
"v_mfma_f32_16x16x32_bf16 v[200:203], a[128:131], a[16:19], v[200:203]\n" \
"ds_read_b128 a[64:67], v0 offset:32768\n" \
"v_mfma_f32_16x16x32_bf16 v[200:203], a[132:135], a[20:23], v[200:203]\n" \
"ds_read_b128 a[68:71], v0 offset:33792\n" \
"v_mfma_f32_16x16x32_bf16 v[204:207], a[132:135], a[28:31], v[204:207]\n" \
"ds_read_b128 a[72:75], v0 offset:34816\n" \
"v_mfma_f32_16x16x32_bf16 v[204:207], a[128:131], a[24:27], v[204:207]\n" \
"ds_read_b128 a[76:79], v0 offset:35840\n" \
"v_mfma_f32_16x16x32_bf16 v[208:211], a[128:131], a[32:35], v[208:211]\n" \
"ds_read_b128 a[80:83], v0 offset:36864\n" \
"v_mfma_f32_16x16x32_bf16 v[208:211], a[132:135], a[36:39], v[208:211]\n" \
"ds_read_b128 a[84:87], v0 offset:37888\n" \
"v_mfma_f32_16x16x32_bf16 v[212:215], a[132:135], a[44:47], v[212:215]\n" \
"ds_read_b128 a[88:91], v0 offset:38912\n" \
"v_mfma_f32_16x16x32_bf16 v[212:215], a[128:131], a[40:43], v[212:215]\n" \
"ds_read_b128 a[92:95], v0 offset:39936\n" \
"v_mfma_f32_16x16x32_bf16 v[216:219], a[128:131], a[48:51], v[216:219]\n" \
"ds_read_b128 a[96:99], v0 offset:40960\n" \
"v_mfma_f32_16x16x32_bf16 v[216:219], a[132:135], a[52:55], v[216:219]\n" \
"ds_read_b128 a[100:103], v0 offset:41984\n" \
"v_mfma_f32_16x16x32_bf16 v[220:223], a[132:135], a[60:63], v[220:223]\n" \
"ds_read_b128 a[104:107], v0 offset:43008\n" \
"v_mfma_f32_16x16x32_bf16 v[220:223], a[128:131], a[56:59], v[220:223]\n" \
"ds_read_b128 a[108:111], v0 offset:44032\n" \
"v_mfma_f32_16x16x32_bf16 v[252:255], a[136:139], a[56:59], v[252:255]\n" \
"ds_read_b128 a[112:115], v0 offset:45056\n" \
"v_mfma_f32_16x16x32_bf16 v[252:255], a[140:143], a[60:63], v[252:255]\n" \
"ds_read_b128 a[116:119], v0 offset:46080\n" \
"v_mfma_f32_16x16x32_bf16 v[248:251], a[140:143], a[52:55], v[248:251]\n" \
"ds_read_b128 a[120:123], v0 offset:47104\n" \
"v_mfma_f32_16x16x32_bf16 v[248:251], a[136:139], a[48:51], v[248:251]\n" \
"ds_read_b128 a[124:127], v0 offset:48128\n" \
"v_mfma_f32_16x16x32_bf16 v[244:247], a[136:139], a[40:43], v[244:247]\n" \
"ds_read_b64_tr_b16 a[152:153], v2 offset:32768\n" \
"v_mfma_f32_16x16x32_bf16 v[244:247], a[140:143], a[44:47], v[244:247]\n" \
"ds_read_b64_tr_b16 a[154:155], v2 offset:33280\n" \
"v_mfma_f32_16x16x32_bf16 v[240:243], a[140:143], a[36:39], v[240:243]\n" \
"ds_read_b64_tr_b16 a[156:157], v2 offset:49152\n" \
"v_mfma_f32_16x16x32_bf16 v[240:243], a[136:139], a[32:35], v[240:243]\n" \
"ds_read_b64_tr_b16 a[158:159], v2 offset:49664\n" \
"v_mfma_f32_16x16x32_bf16 v[236:239], a[136:139], a[24:27], v[236:239]\n" \
"v_mfma_f32_16x16x32_bf16 v[236:239], a[140:143], a[28:31], v[236:239]\n" \
"v_mfma_f32_16x16x32_bf16 v[232:235], a[140:143], a[20:23], v[232:235]\n" \
"v_mfma_f32_16x16x32_bf16 v[232:235], a[136:139], a[16:19], v[232:235]\n" \
"v_mfma_f32_16x16x32_bf16 v[228:231], a[136:139], a[8:11], v[228:231]\n" \
"v_mfma_f32_16x16x32_bf16 v[228:231], a[140:143], a[12:15], v[228:231]\n" \
"v_mfma_f32_16x16x32_bf16 v[224:227], a[140:143], a[4:7], v[224:227]\n" \
"v_mfma_f32_16x16x32_bf16 v[224:227], a[136:139], a[0:3], v[224:227]\n" \
"s_waitcnt lgkmcnt(4)\n" \
"v_mfma_f32_16x16x32_bf16 v[32:35], a[64:67], a[144:147], v[32:35]\n" \
"ds_read_b64_tr_b16 a[128:129], v3 offset:32768\n" \
"v_mfma_f32_16x16x32_bf16 v[32:35], a[68:71], a[148:151], v[32:35]\n" \
"v_mfma_f32_16x16x32_bf16 v[36:39], a[76:79], a[148:151], v[36:39]\n" \
"ds_read_b64_tr_b16 a[130:131], v3 offset:33280\n" \
"s_add_u32 m0, s79, 0\n" \
"s_mov_b32 s74, 525312\n" \
"v_mfma_f32_16x16x32_bf16 v[36:39], a[72:75], a[144:147], v[36:39]\n" \
"buffer_load_dwordx4 v15, s[88:91], s74 offen offset:0 lds\n" \
"v_mfma_f32_16x16x32_bf16 v[40:43], a[80:83], a[144:147], v[40:43]\n" \
"ds_read_b64_tr_b16 a[132:133], v3 offset:49152\n" \
"v_mfma_f32_16x16x32_bf16 v[40:43], a[84:87], a[148:151], v[40:43]\n" \
"v_mfma_f32_16x16x32_bf16 v[44:47], a[92:95], a[148:151], v[44:47]\n" \
"ds_read_b64_tr_b16 a[134:135], v3 offset:49664\n" \
"v_mfma_f32_16x16x32_bf16 v[44:47], a[88:91], a[144:147], v[44:47]\n" \
"v_mfma_f32_16x16x32_bf16 v[48:51], a[96:99], a[144:147], v[48:51]\n" \
"s_mov_b32 s74, 524416\n" \
"v_mfma_f32_16x16x32_bf16 v[48:51], a[100:103], a[148:151], v[48:51]\n" \
"buffer_load_dwordx4 v15, s[88:91], s74 offen offset:1024 lds\n" \
"v_mfma_f32_16x16x32_bf16 v[52:55], a[108:111], a[148:151], v[52:55]\n" \
"v_mfma_f32_16x16x32_bf16 v[52:55], a[104:107], a[144:147], v[52:55]\n" \
"v_mfma_f32_16x16x32_bf16 v[56:59], a[112:115], a[144:147], v[56:59]\n" \
"v_mfma_f32_16x16x32_bf16 v[56:59], a[116:119], a[148:151], v[56:59]\n" \
"v_mfma_f32_16x16x32_bf16 v[60:63], a[124:127], a[148:151], v[60:63]\n" \
"s_mov_b32 s74, 539648\n" \
"v_mfma_f32_16x16x32_bf16 v[60:63], a[120:123], a[144:147], v[60:63]\n" \
"buffer_load_dwordx4 v15, s[88:91], s74 offen offset:2048 lds\n" \
"s_waitcnt lgkmcnt(4)\n" \
"v_mfma_f32_16x16x32_bf16 v[92:95], a[120:123], a[152:155], v[92:95]\n" \
"ds_read_b64_tr_b16 a[136:137], v4 offset:32768\n" \
"v_mfma_f32_16x16x32_bf16 v[92:95], a[124:127], a[156:159], v[92:95]\n" \
"v_mfma_f32_16x16x32_bf16 v[88:91], a[116:119], a[156:159], v[88:91]\n" \
"ds_read_b64_tr_b16 a[138:139], v4 offset:33280\n" \
"v_mfma_f32_16x16x32_bf16 v[88:91], a[112:115], a[152:155], v[88:91]\n" \
"v_mfma_f32_16x16x32_bf16 v[84:87], a[104:107], a[152:155], v[84:87]\n" \
"ds_read_b64_tr_b16 a[140:141], v4 offset:49152\n" \
"s_mov_b32 s74, 538752\n" \
"v_mfma_f32_16x16x32_bf16 v[84:87], a[108:111], a[156:159], v[84:87]\n" \
"buffer_load_dwordx4 v15, s[88:91], s74 offen offset:3072 lds\n" \
"v_mfma_f32_16x16x32_bf16 v[80:83], a[100:103], a[156:159], v[80:83]\n" \
"ds_read_b64_tr_b16 a[142:143], v4 offset:49664\n" \
"v_mfma_f32_16x16x32_bf16 v[80:83], a[96:99], a[152:155], v[80:83]\n" \
"v_mfma_f32_16x16x32_bf16 v[76:79], a[88:91], a[152:155], v[76:79]\n" \
"v_mfma_f32_16x16x32_bf16 v[76:79], a[92:95], a[156:159], v[76:79]\n" \
"v_mfma_f32_16x16x32_bf16 v[72:75], a[84:87], a[156:159], v[72:75]\n" \
"s_add_u32 m0, s79, 16384\n" \
"s_mov_b32 s74, 1180672\n" \
"v_mfma_f32_16x16x32_bf16 v[72:75], a[80:83], a[152:155], v[72:75]\n" \
"buffer_load_dwordx4 v15, s[88:91], s74 offen offset:0 lds\n" \
"v_mfma_f32_16x16x32_bf16 v[68:71], a[72:75], a[152:155], v[68:71]\n" \
"v_mfma_f32_16x16x32_bf16 v[68:71], a[76:79], a[156:159], v[68:71]\n" \
"v_mfma_f32_16x16x32_bf16 v[64:67], a[68:71], a[156:159], v[64:67]\n" \
"v_mfma_f32_16x16x32_bf16 v[64:67], a[64:67], a[152:155], v[64:67]\n" \
"s_waitcnt lgkmcnt(4)\n" \
"v_mfma_f32_16x16x32_bf16 v[96:99], a[64:67], a[128:131], v[96:99]\n" \
"ds_read_b64_tr_b16 a[144:145], v1 offset:34816\n" \
"s_mov_b32 s74, 1179776\n" \
"v_mfma_f32_16x16x32_bf16 v[96:99], a[68:71], a[132:135], v[96:99]\n" \
"buffer_load_dwordx4 v15, s[88:91], s74 offen offset:1024 lds\n" \
"v_mfma_f32_16x16x32_bf16 v[100:103], a[76:79], a[132:135], v[100:103]\n" \
"ds_read_b64_tr_b16 a[146:147], v1 offset:35328\n" \
"v_mfma_f32_16x16x32_bf16 v[100:103], a[72:75], a[128:131], v[100:103]\n" \
"v_mfma_f32_16x16x32_bf16 v[104:107], a[80:83], a[128:131], v[104:107]\n" \
"ds_read_b64_tr_b16 a[148:149], v1 offset:51200\n" \
"v_mfma_f32_16x16x32_bf16 v[104:107], a[84:87], a[132:135], v[104:107]\n" \
"v_mfma_f32_16x16x32_bf16 v[108:111], a[92:95], a[132:135], v[108:111]\n" \
"ds_read_b64_tr_b16 a[150:151], v1 offset:51712\n" \
"s_mov_b32 s74, 1195008\n" \
"v_mfma_f32_16x16x32_bf16 v[108:111], a[88:91], a[128:131], v[108:111]\n" \
"buffer_load_dwordx4 v15, s[88:91], s74 offen offset:2048 lds\n" \
"v_mfma_f32_16x16x32_bf16 v[112:115], a[96:99], a[128:131], v[112:115]\n" \
"v_mfma_f32_16x16x32_bf16 v[112:115], a[100:103], a[132:135], v[112:115]\n" \
"v_mfma_f32_16x16x32_bf16 v[116:119], a[108:111], a[132:135], v[116:119]\n" \
"v_mfma_f32_16x16x32_bf16 v[116:119], a[104:107], a[128:131], v[116:119]\n" \
"v_mfma_f32_16x16x32_bf16 v[120:123], a[112:115], a[128:131], v[120:123]\n" \
"s_mov_b32 s74, 1194112\n" \
"v_mfma_f32_16x16x32_bf16 v[120:123], a[116:119], a[132:135], v[120:123]\n" \
"buffer_load_dwordx4 v15, s[88:91], s74 offen offset:3072 lds\n" \
"v_mfma_f32_16x16x32_bf16 v[124:127], a[124:127], a[132:135], v[124:127]\n" \
"v_mfma_f32_16x16x32_bf16 v[124:127], a[120:123], a[128:131], v[124:127]\n" \
"s_waitcnt lgkmcnt(4)\n" \
"v_mfma_f32_16x16x32_bf16 v[156:159], a[120:123], a[136:139], v[156:159]\n" \
"ds_read_b64_tr_b16 a[152:153], v2 offset:34816\n" \
"v_mfma_f32_16x16x32_bf16 v[156:159], a[124:127], a[140:143], v[156:159]\n" \
"v_mfma_f32_16x16x32_bf16 v[152:155], a[116:119], a[140:143], v[152:155]\n" \
"ds_read_b64_tr_b16 a[154:155], v2 offset:35328\n" \
"s_add_u32 m0, s79, 65536\n" \
"s_mov_b32 s74, 1836032\n" \
"v_mfma_f32_16x16x32_bf16 v[152:155], a[112:115], a[136:139], v[152:155]\n" \
"buffer_load_dwordx4 v15, s[88:91], s74 offen offset:0 lds\n" \
"v_mfma_f32_16x16x32_bf16 v[148:151], a[104:107], a[136:139], v[148:151]\n" \
"ds_read_b64_tr_b16 a[156:157], v2 offset:51200\n" \
"v_mfma_f32_16x16x32_bf16 v[148:151], a[108:111], a[140:143], v[148:151]\n" \
"v_mfma_f32_16x16x32_bf16 v[144:147], a[100:103], a[140:143], v[144:147]\n" \
"ds_read_b64_tr_b16 a[158:159], v2 offset:51712\n" \
"v_mfma_f32_16x16x32_bf16 v[144:147], a[96:99], a[136:139], v[144:147]\n" \
"v_mfma_f32_16x16x32_bf16 v[140:143], a[88:91], a[136:139], v[140:143]\n" \
"s_mov_b32 s74, 1835136\n" \
"v_mfma_f32_16x16x32_bf16 v[140:143], a[92:95], a[140:143], v[140:143]\n" \
"buffer_load_dwordx4 v15, s[88:91], s74 offen offset:1024 lds\n" \
"v_mfma_f32_16x16x32_bf16 v[136:139], a[84:87], a[140:143], v[136:139]\n" \
"v_mfma_f32_16x16x32_bf16 v[136:139], a[80:83], a[136:139], v[136:139]\n" \
"v_mfma_f32_16x16x32_bf16 v[132:135], a[72:75], a[136:139], v[132:135]\n" \
"v_mfma_f32_16x16x32_bf16 v[132:135], a[76:79], a[140:143], v[132:135]\n" \
"v_mfma_f32_16x16x32_bf16 v[128:131], a[68:71], a[140:143], v[128:131]\n" \
"s_mov_b32 s74, 1850368\n" \
"v_mfma_f32_16x16x32_bf16 v[128:131], a[64:67], a[136:139], v[128:131]\n" \
"buffer_load_dwordx4 v15, s[88:91], s74 offen offset:2048 lds\n" \
"s_waitcnt lgkmcnt(4)\n" \
"v_mfma_f32_16x16x32_bf16 v[160:163], a[64:67], a[144:147], v[160:163]\n" \
"ds_read_b64_tr_b16 a[128:129], v3 offset:34816\n" \
"v_mfma_f32_16x16x32_bf16 v[160:163], a[68:71], a[148:151], v[160:163]\n" \
"v_mfma_f32_16x16x32_bf16 v[164:167], a[76:79], a[148:151], v[164:167]\n" \
"ds_read_b64_tr_b16 a[130:131], v3 offset:35328\n" \
"v_mfma_f32_16x16x32_bf16 v[164:167], a[72:75], a[144:147], v[164:167]\n" \
"v_mfma_f32_16x16x32_bf16 v[168:171], a[80:83], a[144:147], v[168:171]\n" \
"ds_read_b64_tr_b16 a[132:133], v3 offset:51200\n" \
"s_mov_b32 s74, 1849472\n" \
"v_mfma_f32_16x16x32_bf16 v[168:171], a[84:87], a[148:151], v[168:171]\n" \
"buffer_load_dwordx4 v15, s[88:91], s74 offen offset:3072 lds\n" \
"v_mfma_f32_16x16x32_bf16 v[172:175], a[92:95], a[148:151], v[172:175]\n" \
"ds_read_b64_tr_b16 a[134:135], v3 offset:51712\n" \
"v_mfma_f32_16x16x32_bf16 v[172:175], a[88:91], a[144:147], v[172:175]\n" \
"v_mfma_f32_16x16x32_bf16 v[176:179], a[96:99], a[144:147], v[176:179]\n" \
"v_mfma_f32_16x16x32_bf16 v[176:179], a[100:103], a[148:151], v[176:179]\n" \
"v_mfma_f32_16x16x32_bf16 v[180:183], a[108:111], a[148:151], v[180:183]\n" \
"s_add_u32 m0, s79, 81920\n" \
"s_mov_b32 s74, 2491392\n" \
"v_mfma_f32_16x16x32_bf16 v[180:183], a[104:107], a[144:147], v[180:183]\n" \
"buffer_load_dwordx4 v15, s[88:91], s74 offen offset:0 lds\n" \
"v_mfma_f32_16x16x32_bf16 v[184:187], a[112:115], a[144:147], v[184:187]\n" \
"v_mfma_f32_16x16x32_bf16 v[184:187], a[116:119], a[148:151], v[184:187]\n" \
"v_mfma_f32_16x16x32_bf16 v[188:191], a[124:127], a[148:151], v[188:191]\n" \
"v_mfma_f32_16x16x32_bf16 v[188:191], a[120:123], a[144:147], v[188:191]\n" \
"s_waitcnt lgkmcnt(4)\n" \
"v_mfma_f32_16x16x32_bf16 a[188:191], a[120:123], a[152:155], a[188:191]\n" \
"ds_read_b64_tr_b16 a[136:137], v4 offset:34816\n" \
"s_mov_b32 s74, 2490496\n" \
"v_mfma_f32_16x16x32_bf16 a[188:191], a[124:127], a[156:159], a[188:191]\n" \
"buffer_load_dwordx4 v15, s[88:91], s74 offen offset:1024 lds\n" \
"v_mfma_f32_16x16x32_bf16 a[184:187], a[116:119], a[156:159], a[184:187]\n" \
"ds_read_b64_tr_b16 a[138:139], v4 offset:35328\n" \
"v_mfma_f32_16x16x32_bf16 a[184:187], a[112:115], a[152:155], a[184:187]\n" \
"v_mfma_f32_16x16x32_bf16 a[180:183], a[104:107], a[152:155], a[180:183]\n" \
"ds_read_b64_tr_b16 a[140:141], v4 offset:51200\n" \
"v_mfma_f32_16x16x32_bf16 a[180:183], a[108:111], a[156:159], a[180:183]\n" \
"v_mfma_f32_16x16x32_bf16 a[176:179], a[100:103], a[156:159], a[176:179]\n" \
"ds_read_b64_tr_b16 a[142:143], v4 offset:51712\n" \
"s_mov_b32 s74, 2505728\n" \
"v_mfma_f32_16x16x32_bf16 a[176:179], a[96:99], a[152:155], a[176:179]\n" \
"buffer_load_dwordx4 v15, s[88:91], s74 offen offset:2048 lds\n" \
"v_mfma_f32_16x16x32_bf16 a[172:175], a[88:91], a[152:155], a[172:175]\n" \
"v_mfma_f32_16x16x32_bf16 a[172:175], a[92:95], a[156:159], a[172:175]\n" \
"v_mfma_f32_16x16x32_bf16 a[168:171], a[84:87], a[156:159], a[168:171]\n" \
"v_mfma_f32_16x16x32_bf16 a[168:171], a[80:83], a[152:155], a[168:171]\n" \
"v_mfma_f32_16x16x32_bf16 a[164:167], a[72:75], a[152:155], a[164:167]\n" \
"s_mov_b32 s74, 2504832\n" \
"v_mfma_f32_16x16x32_bf16 a[164:167], a[76:79], a[156:159], a[164:167]\n" \
"buffer_load_dwordx4 v15, s[88:91], s74 offen offset:3072 lds\n" \
"v_mfma_f32_16x16x32_bf16 a[160:163], a[68:71], a[156:159], a[160:163]\n" \
"v_mfma_f32_16x16x32_bf16 a[160:163], a[64:67], a[152:155], a[160:163]\n" \
"s_waitcnt lgkmcnt(4)\n" \
"v_mfma_f32_16x16x32_bf16 a[192:195], a[64:67], a[128:131], a[192:195]\n" \
"ds_read_b64_tr_b16 a[144:145], v5 offset:8192\n" \
"v_mfma_f32_16x16x32_bf16 a[192:195], a[68:71], a[132:135], a[192:195]\n" \
"v_mfma_f32_16x16x32_bf16 a[196:199], a[76:79], a[132:135], a[196:199]\n" \
"ds_read_b64_tr_b16 a[146:147], v5 offset:8448\n" \
"s_add_u32 m0, s79, 147456\n" \
"s_mov_b32 s74, 1180672\n" \
"v_mfma_f32_16x16x32_bf16 a[196:199], a[72:75], a[128:131], a[196:199]\n" \
"buffer_load_dwordx4 v15, s[92:95], s74 offen offset:0 lds\n" \
"v_mfma_f32_16x16x32_bf16 a[200:203], a[80:83], a[128:131], a[200:203]\n" \
"ds_read_b64_tr_b16 a[148:149], v5 offset:12288\n" \
"v_mfma_f32_16x16x32_bf16 a[200:203], a[84:87], a[132:135], a[200:203]\n" \
"v_mfma_f32_16x16x32_bf16 a[204:207], a[92:95], a[132:135], a[204:207]\n" \
"ds_read_b64_tr_b16 a[150:151], v5 offset:12544\n" \
"v_mfma_f32_16x16x32_bf16 a[204:207], a[88:91], a[128:131], a[204:207]\n" \
"v_mfma_f32_16x16x32_bf16 a[208:211], a[96:99], a[128:131], a[208:211]\n" \
"s_mov_b32 s74, 1179776\n" \
"v_mfma_f32_16x16x32_bf16 a[208:211], a[100:103], a[132:135], a[208:211]\n" \
"buffer_load_dwordx4 v15, s[92:95], s74 offen offset:1024 lds\n" \
"v_mfma_f32_16x16x32_bf16 a[212:215], a[108:111], a[132:135], a[212:215]\n" \
"v_mfma_f32_16x16x32_bf16 a[212:215], a[104:107], a[128:131], a[212:215]\n" \
"v_mfma_f32_16x16x32_bf16 a[216:219], a[112:115], a[128:131], a[216:219]\n" \
"v_mfma_f32_16x16x32_bf16 a[216:219], a[116:119], a[132:135], a[216:219]\n" \
"v_mfma_f32_16x16x32_bf16 a[220:223], a[124:127], a[132:135], a[220:223]\n" \
"s_mov_b32 s74, 1195008\n" \
"v_mfma_f32_16x16x32_bf16 a[220:223], a[120:123], a[128:131], a[220:223]\n" \
"buffer_load_dwordx4 v15, s[92:95], s74 offen offset:2048 lds\n" \
"s_waitcnt lgkmcnt(4)\n" \
"v_mfma_f32_16x16x32_bf16 a[252:255], a[120:123], a[136:139], a[252:255]\n" \
"ds_read_b64_tr_b16 a[152:153], v6 offset:8192\n" \
"v_mfma_f32_16x16x32_bf16 a[252:255], a[124:127], a[140:143], a[252:255]\n" \
"v_mfma_f32_16x16x32_bf16 a[248:251], a[116:119], a[140:143], a[248:251]\n" \
"ds_read_b64_tr_b16 a[154:155], v6 offset:8448\n" \
"v_mfma_f32_16x16x32_bf16 a[248:251], a[112:115], a[136:139], a[248:251]\n" \
"v_mfma_f32_16x16x32_bf16 a[244:247], a[104:107], a[136:139], a[244:247]\n" \
"ds_read_b64_tr_b16 a[156:157], v6 offset:12288\n" \
"s_mov_b32 s74, 1194112\n" \
"v_mfma_f32_16x16x32_bf16 a[244:247], a[108:111], a[140:143], a[244:247]\n" \
"buffer_load_dwordx4 v15, s[92:95], s74 offen offset:3072 lds\n" \
"v_mfma_f32_16x16x32_bf16 a[240:243], a[100:103], a[140:143], a[240:243]\n" \
"ds_read_b64_tr_b16 a[158:159], v6 offset:12544\n" \
"v_mfma_f32_16x16x32_bf16 a[240:243], a[96:99], a[136:139], a[240:243]\n" \
"v_mfma_f32_16x16x32_bf16 a[236:239], a[88:91], a[136:139], a[236:239]\n" \
"v_mfma_f32_16x16x32_bf16 a[236:239], a[92:95], a[140:143], a[236:239]\n" \
"v_mfma_f32_16x16x32_bf16 a[232:235], a[84:87], a[140:143], a[232:235]\n" \
"v_mfma_f32_16x16x32_bf16 a[232:235], a[80:83], a[136:139], a[232:235]\n" \
"v_mfma_f32_16x16x32_bf16 a[228:231], a[72:75], a[136:139], a[228:231]\n" \
"v_mfma_f32_16x16x32_bf16 a[228:231], a[76:79], a[140:143], a[228:231]\n" \
"v_mfma_f32_16x16x32_bf16 a[224:227], a[68:71], a[140:143], a[224:227]\n" \
"v_mfma_f32_16x16x32_bf16 a[224:227], a[64:67], a[136:139], a[224:227]\n" \
"s_waitcnt lgkmcnt(0)\n" \
"s_barrier\n" \
"v_mfma_f32_16x16x32_bf16 v[192:195], a[144:147], a[64:67], v[192:195]\n" \
"s_add_u32 m0, s79, 98304\n" \
"s_mov_b32 s74, 132096\n" \
"v_mfma_f32_16x16x32_bf16 v[192:195], a[148:151], a[68:71], v[192:195]\n" \
"buffer_load_dwordx4 v15, s[92:95], s74 offen offset:0 lds\n" \
"v_mfma_f32_16x16x32_bf16 v[196:199], a[148:151], a[76:79], v[196:199]\n" \
"v_mfma_f32_16x16x32_bf16 v[196:199], a[144:147], a[72:75], v[196:199]\n" \
"v_mfma_f32_16x16x32_bf16 v[200:203], a[144:147], a[80:83], v[200:203]\n" \
"s_mov_b32 s74, 131200\n" \
"v_mfma_f32_16x16x32_bf16 v[200:203], a[148:151], a[84:87], v[200:203]\n" \
"buffer_load_dwordx4 v15, s[92:95], s74 offen offset:1024 lds\n" \
"v_mfma_f32_16x16x32_bf16 v[204:207], a[148:151], a[92:95], v[204:207]\n" \
"v_mfma_f32_16x16x32_bf16 v[204:207], a[144:147], a[88:91], v[204:207]\n" \
"v_mfma_f32_16x16x32_bf16 v[208:211], a[144:147], a[96:99], v[208:211]\n" \
"s_mov_b32 s74, 146432\n" \
"v_mfma_f32_16x16x32_bf16 v[208:211], a[148:151], a[100:103], v[208:211]\n" \
"buffer_load_dwordx4 v15, s[92:95], s74 offen offset:2048 lds\n" \
"v_mfma_f32_16x16x32_bf16 v[212:215], a[148:151], a[108:111], v[212:215]\n" \
"v_mfma_f32_16x16x32_bf16 v[212:215], a[144:147], a[104:107], v[212:215]\n" \
"v_mfma_f32_16x16x32_bf16 v[216:219], a[144:147], a[112:115], v[216:219]\n" \
"s_mov_b32 s74, 145536\n" \
"v_mfma_f32_16x16x32_bf16 v[216:219], a[148:151], a[116:119], v[216:219]\n" \
"buffer_load_dwordx4 v15, s[92:95], s74 offen offset:3072 lds\n" \
"v_mfma_f32_16x16x32_bf16 v[220:223], a[148:151], a[124:127], v[220:223]\n" \
"v_mfma_f32_16x16x32_bf16 v[220:223], a[144:147], a[120:123], v[220:223]\n" \
"v_mfma_f32_16x16x32_bf16 v[252:255], a[152:155], a[120:123], v[252:255]\n" \
"s_add_u32 m0, s79, 114688\n" \
"s_mov_b32 s74, 656384\n" \
"v_mfma_f32_16x16x32_bf16 v[252:255], a[156:159], a[124:127], v[252:255]\n" \
"buffer_load_dwordx4 v15, s[92:95], s74 offen offset:0 lds\n" \
"v_mfma_f32_16x16x32_bf16 v[248:251], a[156:159], a[116:119], v[248:251]\n" \
"v_mfma_f32_16x16x32_bf16 v[248:251], a[152:155], a[112:115], v[248:251]\n" \
"v_mfma_f32_16x16x32_bf16 v[244:247], a[152:155], a[104:107], v[244:247]\n" \
"s_mov_b32 s74, 655488\n" \
"v_mfma_f32_16x16x32_bf16 v[244:247], a[156:159], a[108:111], v[244:247]\n" \
"buffer_load_dwordx4 v15, s[92:95], s74 offen offset:1024 lds\n" \
"v_mfma_f32_16x16x32_bf16 v[240:243], a[156:159], a[100:103], v[240:243]\n" \
"v_mfma_f32_16x16x32_bf16 v[240:243], a[152:155], a[96:99], v[240:243]\n" \
"v_mfma_f32_16x16x32_bf16 v[236:239], a[152:155], a[88:91], v[236:239]\n" \
"s_mov_b32 s74, 670720\n" \
"v_mfma_f32_16x16x32_bf16 v[236:239], a[156:159], a[92:95], v[236:239]\n" \
"buffer_load_dwordx4 v15, s[92:95], s74 offen offset:2048 lds\n" \
"v_mfma_f32_16x16x32_bf16 v[232:235], a[156:159], a[84:87], v[232:235]\n" \
"v_mfma_f32_16x16x32_bf16 v[232:235], a[152:155], a[80:83], v[232:235]\n" \
"v_mfma_f32_16x16x32_bf16 v[228:231], a[152:155], a[72:75], v[228:231]\n" \
"s_mov_b32 s74, 669824\n" \
"v_mfma_f32_16x16x32_bf16 v[228:231], a[156:159], a[76:79], v[228:231]\n" \
"buffer_load_dwordx4 v15, s[92:95], s74 offen offset:3072 lds\n" \
"v_mfma_f32_16x16x32_bf16 v[224:227], a[156:159], a[68:71], v[224:227]\n" \
"v_mfma_f32_16x16x32_bf16 v[224:227], a[152:155], a[64:67], v[224:227]\n" \
"s_mov_b32 s44, 0xbfb8aa3b\n" \
"s_mov_b32 s45, 0xbfb8aa3b\n" \
"s_mov_b32 s46, 1.0\n" \
"s_mov_b32 s47, 1.0\n" \
"s_mov_b32 s48, 0x3fb8aa3b\n" \
"s_mov_b32 s49, 0x3fb8aa3b\n" \
"s_mov_b32 s50, 0x358637bd\n" \
"s_lshl_b32 s74, s14, 11\n" \
"s_add_u32 s74, s74, 384\n" \
"v_lshrrev_b32 v30, 1, v14\n" \
"v_and_b32 v30, 56, v30\n" \
"v_add_u32 v30, s74, v30\n" \
"s_nop 1\n" \
"L_epi_kv:\n" \
"v_mov_b32 v8, 0x3f803f80\n" \
"v_mov_b32 v9, 0x3f803f80\n" \
"v_mov_b32 v10, 0x3f803f80\n" \
"v_mov_b32 v11, 0x3f803f80\n" \
"v_pk_mul_f32 v[20:21], v[32:33], s[48:49]\n" \
"v_pk_mul_f32 v[22:23], v[34:35], s[48:49]\n" \
"v_pk_mul_f32 v[24:25], v[36:37], s[48:49]\n" \
"v_pk_mul_f32 v[26:27], v[38:39], s[48:49]\n" \
"v_exp_f32_e64 v20, v20 clamp\n" \
"v_max_f32 v32, s50, v32\n" \
"v_exp_f32_e64 v21, v21 clamp\n" \
"v_max_f32 v33, s50, v33\n" \
"v_exp_f32_e64 v22, v22 clamp\n" \
"v_max_f32 v34, s50, v34\n" \
"v_exp_f32_e64 v23, v23 clamp\n" \
"v_max_f32 v35, s50, v35\n" \
"v_exp_f32_e64 v24, v24 clamp\n" \
"v_max_f32 v36, s50, v36\n" \
"v_exp_f32_e64 v25, v25 clamp\n" \
"v_max_f32 v37, s50, v37\n" \
"v_exp_f32_e64 v26, v26 clamp\n" \
"v_max_f32 v38, s50, v38\n" \
"v_exp_f32_e64 v27, v27 clamp\n" \
"v_max_f32 v39, s50, v39\n" \
"v_pk_add_f32 v[32:33], v[32:33], v[20:21]\n" \
"v_pk_add_f32 v[34:35], v[34:35], v[22:23]\n" \
"v_pk_add_f32 v[36:37], v[36:37], v[24:25]\n" \
"v_pk_add_f32 v[38:39], v[38:39], v[26:27]\n" \
"v_cvt_pk_bf16_f32 v32, v32, v33\n" \
"v_cvt_pk_bf16_f32 v33, v34, v35\n" \
"v_cvt_pk_bf16_f32 v34, v36, v37\n" \
"v_cvt_pk_bf16_f32 v35, v38, v39\n" \
"v_pk_mul_f32 v[20:21], v[64:65], s[48:49]\n" \
"v_pk_mul_f32 v[22:23], v[66:67], s[48:49]\n" \
"v_pk_mul_f32 v[24:25], v[68:69], s[48:49]\n" \
"v_pk_mul_f32 v[26:27], v[70:71], s[48:49]\n" \
"v_exp_f32_e64 v20, v20 clamp\n" \
"v_max_f32 v64, s50, v64\n" \
"v_exp_f32_e64 v21, v21 clamp\n" \
"v_max_f32 v65, s50, v65\n" \
"v_exp_f32_e64 v22, v22 clamp\n" \
"v_max_f32 v66, s50, v66\n" \
"v_exp_f32_e64 v23, v23 clamp\n" \
"v_max_f32 v67, s50, v67\n" \
"v_exp_f32_e64 v24, v24 clamp\n" \
"v_max_f32 v68, s50, v68\n" \
"v_exp_f32_e64 v25, v25 clamp\n" \
"v_max_f32 v69, s50, v69\n" \
"v_exp_f32_e64 v26, v26 clamp\n" \
"v_max_f32 v70, s50, v70\n" \
"v_exp_f32_e64 v27, v27 clamp\n" \
"v_max_f32 v71, s50, v71\n" \
"v_pk_add_f32 v[64:65], v[64:65], v[20:21]\n" \
"v_pk_add_f32 v[66:67], v[66:67], v[22:23]\n" \
"v_pk_add_f32 v[68:69], v[68:69], v[24:25]\n" \
"v_pk_add_f32 v[70:71], v[70:71], v[26:27]\n" \
"v_cvt_pk_bf16_f32 v64, v64, v65\n" \
"v_cvt_pk_bf16_f32 v65, v66, v67\n" \
"v_cvt_pk_bf16_f32 v66, v68, v69\n" \
"v_cvt_pk_bf16_f32 v67, v70, v71\n" \
"v_pk_mul_f32 v[20:21], v[96:97], s[48:49]\n" \
"v_pk_mul_f32 v[22:23], v[98:99], s[48:49]\n" \
"v_pk_mul_f32 v[24:25], v[100:101], s[48:49]\n" \
"v_pk_mul_f32 v[26:27], v[102:103], s[48:49]\n" \
"v_exp_f32_e64 v20, v20 clamp\n" \
"v_max_f32 v96, s50, v96\n" \
"v_exp_f32_e64 v21, v21 clamp\n" \
"v_max_f32 v97, s50, v97\n" \
"v_exp_f32_e64 v22, v22 clamp\n" \
"v_max_f32 v98, s50, v98\n" \
"v_exp_f32_e64 v23, v23 clamp\n" \
"v_max_f32 v99, s50, v99\n" \
"v_exp_f32_e64 v24, v24 clamp\n" \
"v_max_f32 v100, s50, v100\n" \
"v_exp_f32_e64 v25, v25 clamp\n" \
"v_max_f32 v101, s50, v101\n" \
"v_exp_f32_e64 v26, v26 clamp\n" \
"v_max_f32 v102, s50, v102\n" \
"v_exp_f32_e64 v27, v27 clamp\n" \
"v_max_f32 v103, s50, v103\n" \
"v_pk_add_f32 v[96:97], v[96:97], v[20:21]\n" \
"v_pk_add_f32 v[98:99], v[98:99], v[22:23]\n" \
"v_pk_add_f32 v[100:101], v[100:101], v[24:25]\n" \
"v_pk_add_f32 v[102:103], v[102:103], v[26:27]\n" \
"v_cvt_pk_bf16_f32 v96, v96, v97\n" \
"v_cvt_pk_bf16_f32 v97, v98, v99\n" \
"v_cvt_pk_bf16_f32 v98, v100, v101\n" \
"v_cvt_pk_bf16_f32 v99, v102, v103\n" \
"v_pk_mul_f32 v[20:21], v[128:129], s[48:49]\n" \
"v_pk_mul_f32 v[22:23], v[130:131], s[48:49]\n" \
"v_pk_mul_f32 v[24:25], v[132:133], s[48:49]\n" \
"v_pk_mul_f32 v[26:27], v[134:135], s[48:49]\n" \
"v_exp_f32_e64 v20, v20 clamp\n" \
"v_max_f32 v128, s50, v128\n" \
"v_exp_f32_e64 v21, v21 clamp\n" \
"v_max_f32 v129, s50, v129\n" \
"v_exp_f32_e64 v22, v22 clamp\n" \
"v_max_f32 v130, s50, v130\n" \
"v_exp_f32_e64 v23, v23 clamp\n" \
"v_max_f32 v131, s50, v131\n" \
"v_exp_f32_e64 v24, v24 clamp\n" \
"v_max_f32 v132, s50, v132\n" \
"v_exp_f32_e64 v25, v25 clamp\n" \
"v_max_f32 v133, s50, v133\n" \
"v_exp_f32_e64 v26, v26 clamp\n" \
"v_max_f32 v134, s50, v134\n" \
"v_exp_f32_e64 v27, v27 clamp\n" \
"v_max_f32 v135, s50, v135\n" \
"v_pk_add_f32 v[128:129], v[128:129], v[20:21]\n" \
"v_pk_add_f32 v[130:131], v[130:131], v[22:23]\n" \
"v_pk_add_f32 v[132:133], v[132:133], v[24:25]\n" \
"v_pk_add_f32 v[134:135], v[134:135], v[26:27]\n" \
"v_cvt_pk_bf16_f32 v128, v128, v129\n" \
"v_cvt_pk_bf16_f32 v129, v130, v131\n" \
"v_cvt_pk_bf16_f32 v130, v132, v133\n" \
"v_cvt_pk_bf16_f32 v131, v134, v135\n" \
"v_cvt_pk_bf16_f32 v160, v160, v161\n" \
"v_cvt_pk_bf16_f32 v161, v162, v163\n" \
"v_cvt_pk_bf16_f32 v162, v164, v165\n" \
"v_cvt_pk_bf16_f32 v163, v166, v167\n" \
"v_accvgpr_read_b32 v0, a160\n" \
"v_accvgpr_read_b32 v1, a161\n" \
"v_accvgpr_read_b32 v2, a162\n" \
"v_accvgpr_read_b32 v3, a163\n" \
"v_cvt_pk_bf16_f32 v36, v0, v1\n" \
"v_cvt_pk_bf16_f32 v37, v2, v3\n" \
"v_accvgpr_read_b32 v4, a164\n" \
"v_accvgpr_read_b32 v5, a165\n" \
"v_accvgpr_read_b32 v6, a166\n" \
"v_accvgpr_read_b32 v7, a167\n" \
"v_cvt_pk_bf16_f32 v38, v4, v5\n" \
"v_cvt_pk_bf16_f32 v39, v6, v7\n" \
"v_accvgpr_read_b32 v0, a192\n" \
"v_accvgpr_read_b32 v1, a193\n" \
"v_accvgpr_read_b32 v2, a194\n" \
"v_accvgpr_read_b32 v3, a195\n" \
"v_cvt_pk_bf16_f32 v68, v0, v1\n" \
"v_cvt_pk_bf16_f32 v69, v2, v3\n" \
"v_accvgpr_read_b32 v4, a196\n" \
"v_accvgpr_read_b32 v5, a197\n" \
"v_accvgpr_read_b32 v6, a198\n" \
"v_accvgpr_read_b32 v7, a199\n" \
"v_cvt_pk_bf16_f32 v70, v4, v5\n" \
"v_cvt_pk_bf16_f32 v71, v6, v7\n" \
"v_accvgpr_read_b32 v0, a224\n" \
"v_accvgpr_read_b32 v1, a225\n" \
"v_accvgpr_read_b32 v2, a226\n" \
"v_accvgpr_read_b32 v3, a227\n" \
"v_cvt_pk_bf16_f32 v100, v0, v1\n" \
"v_cvt_pk_bf16_f32 v101, v2, v3\n" \
"v_accvgpr_read_b32 v4, a228\n" \
"v_accvgpr_read_b32 v5, a229\n" \
"v_accvgpr_read_b32 v6, a230\n" \
"v_accvgpr_read_b32 v7, a231\n" \
"v_cvt_pk_bf16_f32 v102, v4, v5\n" \
"v_cvt_pk_bf16_f32 v103, v6, v7\n" \
"v_pk_mul_f32 v[20:21], v[40:41], s[48:49]\n" \
"v_pk_mul_f32 v[22:23], v[42:43], s[48:49]\n" \
"v_pk_mul_f32 v[24:25], v[44:45], s[48:49]\n" \
"v_pk_mul_f32 v[26:27], v[46:47], s[48:49]\n" \
"v_exp_f32_e64 v20, v20 clamp\n" \
"v_max_f32 v40, s50, v40\n" \
"v_exp_f32_e64 v21, v21 clamp\n" \
"v_max_f32 v41, s50, v41\n" \
"v_exp_f32_e64 v22, v22 clamp\n" \
"v_max_f32 v42, s50, v42\n" \
"v_exp_f32_e64 v23, v23 clamp\n" \
"v_max_f32 v43, s50, v43\n" \
"v_exp_f32_e64 v24, v24 clamp\n" \
"v_max_f32 v44, s50, v44\n" \
"v_exp_f32_e64 v25, v25 clamp\n" \
"v_max_f32 v45, s50, v45\n" \
"v_exp_f32_e64 v26, v26 clamp\n" \
"v_max_f32 v46, s50, v46\n" \
"v_exp_f32_e64 v27, v27 clamp\n" \
"v_max_f32 v47, s50, v47\n" \
"v_pk_add_f32 v[40:41], v[40:41], v[20:21]\n" \
"v_pk_add_f32 v[42:43], v[42:43], v[22:23]\n" \
"v_pk_add_f32 v[44:45], v[44:45], v[24:25]\n" \
"v_pk_add_f32 v[46:47], v[46:47], v[26:27]\n" \
"v_cvt_pk_bf16_f32 v40, v40, v41\n" \
"v_cvt_pk_bf16_f32 v41, v42, v43\n" \
"v_cvt_pk_bf16_f32 v42, v44, v45\n" \
"v_cvt_pk_bf16_f32 v43, v46, v47\n" \
"v_pk_mul_f32 v[20:21], v[72:73], s[48:49]\n" \
"v_pk_mul_f32 v[22:23], v[74:75], s[48:49]\n" \
"v_pk_mul_f32 v[24:25], v[76:77], s[48:49]\n" \
"v_pk_mul_f32 v[26:27], v[78:79], s[48:49]\n" \
"v_exp_f32_e64 v20, v20 clamp\n" \
"v_max_f32 v72, s50, v72\n" \
"v_exp_f32_e64 v21, v21 clamp\n" \
"v_max_f32 v73, s50, v73\n" \
"v_exp_f32_e64 v22, v22 clamp\n" \
"v_max_f32 v74, s50, v74\n" \
"v_exp_f32_e64 v23, v23 clamp\n" \
"v_max_f32 v75, s50, v75\n" \
"v_exp_f32_e64 v24, v24 clamp\n" \
"v_max_f32 v76, s50, v76\n" \
"v_exp_f32_e64 v25, v25 clamp\n" \
"v_max_f32 v77, s50, v77\n" \
"v_exp_f32_e64 v26, v26 clamp\n" \
"v_max_f32 v78, s50, v78\n" \
"v_exp_f32_e64 v27, v27 clamp\n" \
"v_max_f32 v79, s50, v79\n" \
"v_pk_add_f32 v[72:73], v[72:73], v[20:21]\n" \
"v_pk_add_f32 v[74:75], v[74:75], v[22:23]\n" \
"v_pk_add_f32 v[76:77], v[76:77], v[24:25]\n" \
"v_pk_add_f32 v[78:79], v[78:79], v[26:27]\n" \
"v_cvt_pk_bf16_f32 v72, v72, v73\n" \
"v_cvt_pk_bf16_f32 v73, v74, v75\n" \
"v_cvt_pk_bf16_f32 v74, v76, v77\n" \
"v_cvt_pk_bf16_f32 v75, v78, v79\n" \
"v_pk_mul_f32 v[20:21], v[104:105], s[48:49]\n" \
"v_pk_mul_f32 v[22:23], v[106:107], s[48:49]\n" \
"v_pk_mul_f32 v[24:25], v[108:109], s[48:49]\n" \
"v_pk_mul_f32 v[26:27], v[110:111], s[48:49]\n" \
"v_exp_f32_e64 v20, v20 clamp\n" \
"v_max_f32 v104, s50, v104\n" \
"v_exp_f32_e64 v21, v21 clamp\n" \
"v_max_f32 v105, s50, v105\n" \
"v_exp_f32_e64 v22, v22 clamp\n" \
"v_max_f32 v106, s50, v106\n" \
"v_exp_f32_e64 v23, v23 clamp\n" \
"v_max_f32 v107, s50, v107\n" \
"v_exp_f32_e64 v24, v24 clamp\n" \
"v_max_f32 v108, s50, v108\n" \
"v_exp_f32_e64 v25, v25 clamp\n" \
"v_max_f32 v109, s50, v109\n" \
"v_exp_f32_e64 v26, v26 clamp\n" \
"v_max_f32 v110, s50, v110\n" \
"v_exp_f32_e64 v27, v27 clamp\n" \
"v_max_f32 v111, s50, v111\n" \
"v_pk_add_f32 v[104:105], v[104:105], v[20:21]\n" \
"v_pk_add_f32 v[106:107], v[106:107], v[22:23]\n" \
"v_pk_add_f32 v[108:109], v[108:109], v[24:25]\n" \
"v_pk_add_f32 v[110:111], v[110:111], v[26:27]\n" \
"v_cvt_pk_bf16_f32 v104, v104, v105\n" \
"v_cvt_pk_bf16_f32 v105, v106, v107\n" \
"v_cvt_pk_bf16_f32 v106, v108, v109\n" \
"v_cvt_pk_bf16_f32 v107, v110, v111\n" \
"v_pk_mul_f32 v[20:21], v[136:137], s[48:49]\n" \
"v_pk_mul_f32 v[22:23], v[138:139], s[48:49]\n" \
"v_pk_mul_f32 v[24:25], v[140:141], s[48:49]\n" \
"v_pk_mul_f32 v[26:27], v[142:143], s[48:49]\n" \
"v_exp_f32_e64 v20, v20 clamp\n" \
"v_max_f32 v136, s50, v136\n" \
"v_exp_f32_e64 v21, v21 clamp\n" \
"v_max_f32 v137, s50, v137\n" \
"v_exp_f32_e64 v22, v22 clamp\n" \
"v_max_f32 v138, s50, v138\n" \
"v_exp_f32_e64 v23, v23 clamp\n" \
"v_max_f32 v139, s50, v139\n" \
"v_exp_f32_e64 v24, v24 clamp\n" \
"v_max_f32 v140, s50, v140\n" \
"v_exp_f32_e64 v25, v25 clamp\n" \
"v_max_f32 v141, s50, v141\n" \
"v_exp_f32_e64 v26, v26 clamp\n" \
"v_max_f32 v142, s50, v142\n" \
"v_exp_f32_e64 v27, v27 clamp\n" \
"v_max_f32 v143, s50, v143\n" \
"v_pk_add_f32 v[136:137], v[136:137], v[20:21]\n" \
"v_pk_add_f32 v[138:139], v[138:139], v[22:23]\n" \
"v_pk_add_f32 v[140:141], v[140:141], v[24:25]\n" \
"v_pk_add_f32 v[142:143], v[142:143], v[26:27]\n" \
"v_cvt_pk_bf16_f32 v136, v136, v137\n" \
"v_cvt_pk_bf16_f32 v137, v138, v139\n" \
"v_cvt_pk_bf16_f32 v138, v140, v141\n" \
"v_cvt_pk_bf16_f32 v139, v142, v143\n" \
"v_cvt_pk_bf16_f32 v168, v168, v169\n" \
"v_cvt_pk_bf16_f32 v169, v170, v171\n" \
"v_cvt_pk_bf16_f32 v170, v172, v173\n" \
"v_cvt_pk_bf16_f32 v171, v174, v175\n" \
"v_accvgpr_read_b32 v0, a168\n" \
"v_accvgpr_read_b32 v1, a169\n" \
"v_accvgpr_read_b32 v2, a170\n" \
"v_accvgpr_read_b32 v3, a171\n" \
"v_cvt_pk_bf16_f32 v44, v0, v1\n" \
"v_cvt_pk_bf16_f32 v45, v2, v3\n" \
"v_accvgpr_read_b32 v4, a172\n" \
"v_accvgpr_read_b32 v5, a173\n" \
"v_accvgpr_read_b32 v6, a174\n" \
"v_accvgpr_read_b32 v7, a175\n" \
"v_cvt_pk_bf16_f32 v46, v4, v5\n" \
"v_cvt_pk_bf16_f32 v47, v6, v7\n" \
"v_accvgpr_read_b32 v0, a200\n" \
"v_accvgpr_read_b32 v1, a201\n" \
"v_accvgpr_read_b32 v2, a202\n" \
"v_accvgpr_read_b32 v3, a203\n" \
"v_cvt_pk_bf16_f32 v76, v0, v1\n" \
"v_cvt_pk_bf16_f32 v77, v2, v3\n" \
"v_accvgpr_read_b32 v4, a204\n" \
"v_accvgpr_read_b32 v5, a205\n" \
"v_accvgpr_read_b32 v6, a206\n" \
"v_accvgpr_read_b32 v7, a207\n" \
"v_cvt_pk_bf16_f32 v78, v4, v5\n" \
"v_cvt_pk_bf16_f32 v79, v6, v7\n" \
"v_accvgpr_read_b32 v0, a232\n" \
"v_accvgpr_read_b32 v1, a233\n" \
"v_accvgpr_read_b32 v2, a234\n" \
"v_accvgpr_read_b32 v3, a235\n" \
"v_cvt_pk_bf16_f32 v108, v0, v1\n" \
"v_cvt_pk_bf16_f32 v109, v2, v3\n" \
"v_accvgpr_read_b32 v4, a236\n" \
"v_accvgpr_read_b32 v5, a237\n" \
"v_accvgpr_read_b32 v6, a238\n" \
"v_accvgpr_read_b32 v7, a239\n" \
"v_cvt_pk_bf16_f32 v110, v4, v5\n" \
"v_cvt_pk_bf16_f32 v111, v6, v7\n" \
"v_pk_mul_f32 v[20:21], v[48:49], s[48:49]\n" \
"v_pk_mul_f32 v[22:23], v[50:51], s[48:49]\n" \
"v_pk_mul_f32 v[24:25], v[52:53], s[48:49]\n" \
"v_pk_mul_f32 v[26:27], v[54:55], s[48:49]\n" \
"v_exp_f32_e64 v20, v20 clamp\n" \
"v_max_f32 v48, s50, v48\n" \
"v_exp_f32_e64 v21, v21 clamp\n" \
"v_max_f32 v49, s50, v49\n" \
"v_exp_f32_e64 v22, v22 clamp\n" \
"v_max_f32 v50, s50, v50\n" \
"v_exp_f32_e64 v23, v23 clamp\n" \
"v_max_f32 v51, s50, v51\n" \
"v_exp_f32_e64 v24, v24 clamp\n" \
"v_max_f32 v52, s50, v52\n" \
"v_exp_f32_e64 v25, v25 clamp\n" \
"v_max_f32 v53, s50, v53\n" \
"v_exp_f32_e64 v26, v26 clamp\n" \
"v_max_f32 v54, s50, v54\n" \
"v_exp_f32_e64 v27, v27 clamp\n" \
"v_max_f32 v55, s50, v55\n" \
"v_pk_add_f32 v[48:49], v[48:49], v[20:21]\n" \
"v_pk_add_f32 v[50:51], v[50:51], v[22:23]\n" \
"v_pk_add_f32 v[52:53], v[52:53], v[24:25]\n" \
"v_pk_add_f32 v[54:55], v[54:55], v[26:27]\n" \
"v_cvt_pk_bf16_f32 v48, v48, v49\n" \
"v_cvt_pk_bf16_f32 v49, v50, v51\n" \
"v_cvt_pk_bf16_f32 v50, v52, v53\n" \
"v_cvt_pk_bf16_f32 v51, v54, v55\n" \
"v_pk_mul_f32 v[20:21], v[80:81], s[48:49]\n" \
"v_pk_mul_f32 v[22:23], v[82:83], s[48:49]\n" \
"v_pk_mul_f32 v[24:25], v[84:85], s[48:49]\n" \
"v_pk_mul_f32 v[26:27], v[86:87], s[48:49]\n" \
"v_exp_f32_e64 v20, v20 clamp\n" \
"v_max_f32 v80, s50, v80\n" \
"v_exp_f32_e64 v21, v21 clamp\n" \
"v_max_f32 v81, s50, v81\n" \
"v_exp_f32_e64 v22, v22 clamp\n" \
"v_max_f32 v82, s50, v82\n" \
"v_exp_f32_e64 v23, v23 clamp\n" \
"v_max_f32 v83, s50, v83\n" \
"v_exp_f32_e64 v24, v24 clamp\n" \
"v_max_f32 v84, s50, v84\n" \
"v_exp_f32_e64 v25, v25 clamp\n" \
"v_max_f32 v85, s50, v85\n" \
"v_exp_f32_e64 v26, v26 clamp\n" \
"v_max_f32 v86, s50, v86\n" \
"v_exp_f32_e64 v27, v27 clamp\n" \
"v_max_f32 v87, s50, v87\n" \
"v_pk_add_f32 v[80:81], v[80:81], v[20:21]\n" \
"v_pk_add_f32 v[82:83], v[82:83], v[22:23]\n" \
"v_pk_add_f32 v[84:85], v[84:85], v[24:25]\n" \
"v_pk_add_f32 v[86:87], v[86:87], v[26:27]\n" \
"v_cvt_pk_bf16_f32 v80, v80, v81\n" \
"v_cvt_pk_bf16_f32 v81, v82, v83\n" \
"v_cvt_pk_bf16_f32 v82, v84, v85\n" \
"v_cvt_pk_bf16_f32 v83, v86, v87\n" \
"v_pk_mul_f32 v[20:21], v[112:113], s[48:49]\n" \
"v_pk_mul_f32 v[22:23], v[114:115], s[48:49]\n" \
"v_pk_mul_f32 v[24:25], v[116:117], s[48:49]\n" \
"v_pk_mul_f32 v[26:27], v[118:119], s[48:49]\n" \
"v_exp_f32_e64 v20, v20 clamp\n" \
"v_max_f32 v112, s50, v112\n" \
"v_exp_f32_e64 v21, v21 clamp\n" \
"v_max_f32 v113, s50, v113\n" \
"v_exp_f32_e64 v22, v22 clamp\n" \
"v_max_f32 v114, s50, v114\n" \
"v_exp_f32_e64 v23, v23 clamp\n" \
"v_max_f32 v115, s50, v115\n" \
"v_exp_f32_e64 v24, v24 clamp\n" \
"v_max_f32 v116, s50, v116\n" \
"v_exp_f32_e64 v25, v25 clamp\n" \
"v_max_f32 v117, s50, v117\n" \
"v_exp_f32_e64 v26, v26 clamp\n" \
"v_max_f32 v118, s50, v118\n" \
"v_exp_f32_e64 v27, v27 clamp\n" \
"v_max_f32 v119, s50, v119\n" \
"v_pk_add_f32 v[112:113], v[112:113], v[20:21]\n" \
"v_pk_add_f32 v[114:115], v[114:115], v[22:23]\n" \
"v_pk_add_f32 v[116:117], v[116:117], v[24:25]\n" \
"v_pk_add_f32 v[118:119], v[118:119], v[26:27]\n" \
"v_cvt_pk_bf16_f32 v112, v112, v113\n" \
"v_cvt_pk_bf16_f32 v113, v114, v115\n" \
"v_cvt_pk_bf16_f32 v114, v116, v117\n" \
"v_cvt_pk_bf16_f32 v115, v118, v119\n" \
"v_pk_mul_f32 v[20:21], v[144:145], s[48:49]\n" \
"v_pk_mul_f32 v[22:23], v[146:147], s[48:49]\n" \
"v_pk_mul_f32 v[24:25], v[148:149], s[48:49]\n" \
"v_pk_mul_f32 v[26:27], v[150:151], s[48:49]\n" \
"v_exp_f32_e64 v20, v20 clamp\n" \
"v_max_f32 v144, s50, v144\n" \
"v_exp_f32_e64 v21, v21 clamp\n" \
"v_max_f32 v145, s50, v145\n" \
"v_exp_f32_e64 v22, v22 clamp\n" \
"v_max_f32 v146, s50, v146\n" \
"v_exp_f32_e64 v23, v23 clamp\n" \
"v_max_f32 v147, s50, v147\n" \
"v_exp_f32_e64 v24, v24 clamp\n" \
"v_max_f32 v148, s50, v148\n" \
"v_exp_f32_e64 v25, v25 clamp\n" \
"v_max_f32 v149, s50, v149\n" \
"v_exp_f32_e64 v26, v26 clamp\n" \
"v_max_f32 v150, s50, v150\n" \
"v_exp_f32_e64 v27, v27 clamp\n" \
"v_max_f32 v151, s50, v151\n" \
"v_pk_add_f32 v[144:145], v[144:145], v[20:21]\n" \
"v_pk_add_f32 v[146:147], v[146:147], v[22:23]\n" \
"v_pk_add_f32 v[148:149], v[148:149], v[24:25]\n" \
"v_pk_add_f32 v[150:151], v[150:151], v[26:27]\n" \
"v_cvt_pk_bf16_f32 v144, v144, v145\n" \
"v_cvt_pk_bf16_f32 v145, v146, v147\n" \
"v_cvt_pk_bf16_f32 v146, v148, v149\n" \
"v_cvt_pk_bf16_f32 v147, v150, v151\n" \
"v_cvt_pk_bf16_f32 v176, v176, v177\n" \
"v_cvt_pk_bf16_f32 v177, v178, v179\n" \
"v_cvt_pk_bf16_f32 v178, v180, v181\n" \
"v_cvt_pk_bf16_f32 v179, v182, v183\n" \
"v_accvgpr_read_b32 v0, a176\n" \
"v_accvgpr_read_b32 v1, a177\n" \
"v_accvgpr_read_b32 v2, a178\n" \
"v_accvgpr_read_b32 v3, a179\n" \
"v_cvt_pk_bf16_f32 v52, v0, v1\n" \
"v_cvt_pk_bf16_f32 v53, v2, v3\n" \
"v_accvgpr_read_b32 v4, a180\n" \
"v_accvgpr_read_b32 v5, a181\n" \
"v_accvgpr_read_b32 v6, a182\n" \
"v_accvgpr_read_b32 v7, a183\n" \
"v_cvt_pk_bf16_f32 v54, v4, v5\n" \
"v_cvt_pk_bf16_f32 v55, v6, v7\n" \
"v_accvgpr_read_b32 v0, a208\n" \
"v_accvgpr_read_b32 v1, a209\n" \
"v_accvgpr_read_b32 v2, a210\n" \
"v_accvgpr_read_b32 v3, a211\n" \
"v_cvt_pk_bf16_f32 v84, v0, v1\n" \
"v_cvt_pk_bf16_f32 v85, v2, v3\n" \
"v_accvgpr_read_b32 v4, a212\n" \
"v_accvgpr_read_b32 v5, a213\n" \
"v_accvgpr_read_b32 v6, a214\n" \
"v_accvgpr_read_b32 v7, a215\n" \
"v_cvt_pk_bf16_f32 v86, v4, v5\n" \
"v_cvt_pk_bf16_f32 v87, v6, v7\n" \
"v_accvgpr_read_b32 v0, a240\n" \
"v_accvgpr_read_b32 v1, a241\n" \
"v_accvgpr_read_b32 v2, a242\n" \
"v_accvgpr_read_b32 v3, a243\n" \
"v_cvt_pk_bf16_f32 v116, v0, v1\n" \
"v_cvt_pk_bf16_f32 v117, v2, v3\n" \
"v_accvgpr_read_b32 v4, a244\n" \
"v_accvgpr_read_b32 v5, a245\n" \
"v_accvgpr_read_b32 v6, a246\n" \
"v_accvgpr_read_b32 v7, a247\n" \
"v_cvt_pk_bf16_f32 v118, v4, v5\n" \
"v_cvt_pk_bf16_f32 v119, v6, v7\n" \
"v_pk_mul_f32 v[20:21], v[56:57], s[48:49]\n" \
"v_pk_mul_f32 v[22:23], v[58:59], s[48:49]\n" \
"v_pk_mul_f32 v[24:25], v[60:61], s[48:49]\n" \
"v_pk_mul_f32 v[26:27], v[62:63], s[48:49]\n" \
"v_exp_f32_e64 v20, v20 clamp\n" \
"v_max_f32 v56, s50, v56\n" \
"v_exp_f32_e64 v21, v21 clamp\n" \
"v_max_f32 v57, s50, v57\n" \
"v_exp_f32_e64 v22, v22 clamp\n" \
"v_max_f32 v58, s50, v58\n" \
"v_exp_f32_e64 v23, v23 clamp\n" \
"v_max_f32 v59, s50, v59\n" \
"v_exp_f32_e64 v24, v24 clamp\n" \
"v_max_f32 v60, s50, v60\n" \
"v_exp_f32_e64 v25, v25 clamp\n" \
"v_max_f32 v61, s50, v61\n" \
"v_exp_f32_e64 v26, v26 clamp\n" \
"v_max_f32 v62, s50, v62\n" \
"v_exp_f32_e64 v27, v27 clamp\n" \
"v_max_f32 v63, s50, v63\n" \
"v_pk_add_f32 v[56:57], v[56:57], v[20:21]\n" \
"v_pk_add_f32 v[58:59], v[58:59], v[22:23]\n" \
"v_pk_add_f32 v[60:61], v[60:61], v[24:25]\n" \
"v_pk_add_f32 v[62:63], v[62:63], v[26:27]\n" \
"v_cvt_pk_bf16_f32 v56, v56, v57\n" \
"v_cvt_pk_bf16_f32 v57, v58, v59\n" \
"v_cvt_pk_bf16_f32 v58, v60, v61\n" \
"v_cvt_pk_bf16_f32 v59, v62, v63\n" \
"v_pk_mul_f32 v[20:21], v[88:89], s[48:49]\n" \
"v_pk_mul_f32 v[22:23], v[90:91], s[48:49]\n" \
"v_pk_mul_f32 v[24:25], v[92:93], s[48:49]\n" \
"v_pk_mul_f32 v[26:27], v[94:95], s[48:49]\n" \
"v_exp_f32_e64 v20, v20 clamp\n" \
"v_max_f32 v88, s50, v88\n" \
"v_exp_f32_e64 v21, v21 clamp\n" \
"v_max_f32 v89, s50, v89\n" \
"v_exp_f32_e64 v22, v22 clamp\n" \
"v_max_f32 v90, s50, v90\n" \
"v_exp_f32_e64 v23, v23 clamp\n" \
"v_max_f32 v91, s50, v91\n" \
"v_exp_f32_e64 v24, v24 clamp\n" \
"v_max_f32 v92, s50, v92\n" \
"v_exp_f32_e64 v25, v25 clamp\n" \
"v_max_f32 v93, s50, v93\n" \
"v_exp_f32_e64 v26, v26 clamp\n" \
"v_max_f32 v94, s50, v94\n" \
"v_exp_f32_e64 v27, v27 clamp\n" \
"v_max_f32 v95, s50, v95\n" \
"v_pk_add_f32 v[88:89], v[88:89], v[20:21]\n" \
"v_pk_add_f32 v[90:91], v[90:91], v[22:23]\n" \
"v_pk_add_f32 v[92:93], v[92:93], v[24:25]\n" \
"v_pk_add_f32 v[94:95], v[94:95], v[26:27]\n" \
"v_cvt_pk_bf16_f32 v88, v88, v89\n" \
"v_cvt_pk_bf16_f32 v89, v90, v91\n" \
"v_cvt_pk_bf16_f32 v90, v92, v93\n" \
"v_cvt_pk_bf16_f32 v91, v94, v95\n" \
"v_pk_mul_f32 v[20:21], v[120:121], s[48:49]\n" \
"v_pk_mul_f32 v[22:23], v[122:123], s[48:49]\n" \
"v_pk_mul_f32 v[24:25], v[124:125], s[48:49]\n" \
"v_pk_mul_f32 v[26:27], v[126:127], s[48:49]\n" \
"v_exp_f32_e64 v20, v20 clamp\n" \
"v_max_f32 v120, s50, v120\n" \
"v_exp_f32_e64 v21, v21 clamp\n" \
"v_max_f32 v121, s50, v121\n" \
"v_exp_f32_e64 v22, v22 clamp\n" \
"v_max_f32 v122, s50, v122\n" \
"v_exp_f32_e64 v23, v23 clamp\n" \
"v_max_f32 v123, s50, v123\n" \
"v_exp_f32_e64 v24, v24 clamp\n" \
"v_max_f32 v124, s50, v124\n" \
"v_exp_f32_e64 v25, v25 clamp\n" \
"v_max_f32 v125, s50, v125\n" \
"v_exp_f32_e64 v26, v26 clamp\n" \
"v_max_f32 v126, s50, v126\n" \
"v_exp_f32_e64 v27, v27 clamp\n" \
"v_max_f32 v127, s50, v127\n" \
"v_pk_add_f32 v[120:121], v[120:121], v[20:21]\n" \
"v_pk_add_f32 v[122:123], v[122:123], v[22:23]\n" \
"v_pk_add_f32 v[124:125], v[124:125], v[24:25]\n" \
"v_pk_add_f32 v[126:127], v[126:127], v[26:27]\n" \
"v_cvt_pk_bf16_f32 v120, v120, v121\n" \
"v_cvt_pk_bf16_f32 v121, v122, v123\n" \
"v_cvt_pk_bf16_f32 v122, v124, v125\n" \
"v_cvt_pk_bf16_f32 v123, v126, v127\n" \
"v_pk_mul_f32 v[20:21], v[152:153], s[48:49]\n" \
"v_pk_mul_f32 v[22:23], v[154:155], s[48:49]\n" \
"v_pk_mul_f32 v[24:25], v[156:157], s[48:49]\n" \
"v_pk_mul_f32 v[26:27], v[158:159], s[48:49]\n" \
"v_exp_f32_e64 v20, v20 clamp\n" \
"v_max_f32 v152, s50, v152\n" \
"v_exp_f32_e64 v21, v21 clamp\n" \
"v_max_f32 v153, s50, v153\n" \
"v_exp_f32_e64 v22, v22 clamp\n" \
"v_max_f32 v154, s50, v154\n" \
"v_exp_f32_e64 v23, v23 clamp\n" \
"v_max_f32 v155, s50, v155\n" \
"v_exp_f32_e64 v24, v24 clamp\n" \
"v_max_f32 v156, s50, v156\n" \
"v_exp_f32_e64 v25, v25 clamp\n" \
"v_max_f32 v157, s50, v157\n" \
"v_exp_f32_e64 v26, v26 clamp\n" \
"v_max_f32 v158, s50, v158\n" \
"v_exp_f32_e64 v27, v27 clamp\n" \
"v_max_f32 v159, s50, v159\n" \
"v_pk_add_f32 v[152:153], v[152:153], v[20:21]\n" \
"v_pk_add_f32 v[154:155], v[154:155], v[22:23]\n" \
"v_pk_add_f32 v[156:157], v[156:157], v[24:25]\n" \
"v_pk_add_f32 v[158:159], v[158:159], v[26:27]\n" \
"v_cvt_pk_bf16_f32 v152, v152, v153\n" \
"v_cvt_pk_bf16_f32 v153, v154, v155\n" \
"v_cvt_pk_bf16_f32 v154, v156, v157\n" \
"v_cvt_pk_bf16_f32 v155, v158, v159\n" \
"v_cvt_pk_bf16_f32 v184, v184, v185\n" \
"v_cvt_pk_bf16_f32 v185, v186, v187\n" \
"v_cvt_pk_bf16_f32 v186, v188, v189\n" \
"v_cvt_pk_bf16_f32 v187, v190, v191\n" \
"v_accvgpr_read_b32 v0, a184\n" \
"v_accvgpr_read_b32 v1, a185\n" \
"v_accvgpr_read_b32 v2, a186\n" \
"v_accvgpr_read_b32 v3, a187\n" \
"v_cvt_pk_bf16_f32 v60, v0, v1\n" \
"v_cvt_pk_bf16_f32 v61, v2, v3\n" \
"v_accvgpr_read_b32 v4, a188\n" \
"v_accvgpr_read_b32 v5, a189\n" \
"v_accvgpr_read_b32 v6, a190\n" \
"v_accvgpr_read_b32 v7, a191\n" \
"v_cvt_pk_bf16_f32 v62, v4, v5\n" \
"v_cvt_pk_bf16_f32 v63, v6, v7\n" \
"v_accvgpr_read_b32 v0, a216\n" \
"v_accvgpr_read_b32 v1, a217\n" \
"v_accvgpr_read_b32 v2, a218\n" \
"v_accvgpr_read_b32 v3, a219\n" \
"v_cvt_pk_bf16_f32 v92, v0, v1\n" \
"v_cvt_pk_bf16_f32 v93, v2, v3\n" \
"v_accvgpr_read_b32 v4, a220\n" \
"v_accvgpr_read_b32 v5, a221\n" \
"v_accvgpr_read_b32 v6, a222\n" \
"v_accvgpr_read_b32 v7, a223\n" \
"v_cvt_pk_bf16_f32 v94, v4, v5\n" \
"v_cvt_pk_bf16_f32 v95, v6, v7\n" \
"v_accvgpr_read_b32 v0, a248\n" \
"v_accvgpr_read_b32 v1, a249\n" \
"v_accvgpr_read_b32 v2, a250\n" \
"v_accvgpr_read_b32 v3, a251\n" \
"v_cvt_pk_bf16_f32 v124, v0, v1\n" \
"v_cvt_pk_bf16_f32 v125, v2, v3\n" \
"v_accvgpr_read_b32 v4, a252\n" \
"v_accvgpr_read_b32 v5, a253\n" \
"v_accvgpr_read_b32 v6, a254\n" \
"v_accvgpr_read_b32 v7, a255\n" \
"v_cvt_pk_bf16_f32 v126, v4, v5\n" \
"v_cvt_pk_bf16_f32 v127, v6, v7\n" \
"s_nop 1\n" \
"v_mfma_f32_16x16x32_bf16 v[132:135], v[32:35], v[160:163], 0\n" \
"v_mfma_f32_16x16x32_bf16 v[140:143], v[64:67], v[160:163], 0\n" \
"v_mfma_f32_16x16x32_bf16 v[148:151], v[32:35], v[36:39], 0\n" \
"v_mfma_f32_16x16x32_bf16 v[156:159], v[64:67], v[36:39], 0\n" \
"v_mfma_f32_16x16x32_bf16 v[164:167], v[32:35], v[8:11], 0\n" \
"v_mfma_f32_16x16x32_bf16 v[172:175], v[64:67], v[8:11], 0\n" \
"v_mfma_f32_16x16x32_bf16 v[180:183], v[96:99], v[68:71], 0\n" \
"v_mfma_f32_16x16x32_bf16 v[188:191], v[128:131], v[68:71], 0\n" \
"v_mfma_f32_16x16x32_bf16 v[0:3], v[96:99], v[100:103], 0\n" \
"v_mfma_f32_16x16x32_bf16 v[4:7], v[128:131], v[100:103], 0\n" \
"v_mfma_f32_16x16x32_bf16 v[20:23], v[96:99], v[8:11], 0\n" \
"v_mfma_f32_16x16x32_bf16 v[24:27], v[128:131], v[8:11], 0\n" \
"v_mfma_f32_16x16x32_bf16 v[132:135], v[40:43], v[168:171], v[132:135]\n" \
"v_mfma_f32_16x16x32_bf16 v[140:143], v[72:75], v[168:171], v[140:143]\n" \
"v_mfma_f32_16x16x32_bf16 v[148:151], v[40:43], v[44:47], v[148:151]\n" \
"v_mfma_f32_16x16x32_bf16 v[156:159], v[72:75], v[44:47], v[156:159]\n" \
"v_mfma_f32_16x16x32_bf16 v[164:167], v[40:43], v[8:11], v[164:167]\n" \
"v_mfma_f32_16x16x32_bf16 v[172:175], v[72:75], v[8:11], v[172:175]\n" \
"v_mfma_f32_16x16x32_bf16 v[180:183], v[104:107], v[76:79], v[180:183]\n" \
"v_mfma_f32_16x16x32_bf16 v[188:191], v[136:139], v[76:79], v[188:191]\n" \
"v_mfma_f32_16x16x32_bf16 v[0:3], v[104:107], v[108:111], v[0:3]\n" \
"v_mfma_f32_16x16x32_bf16 v[4:7], v[136:139], v[108:111], v[4:7]\n" \
"v_mfma_f32_16x16x32_bf16 v[20:23], v[104:107], v[8:11], v[20:23]\n" \
"v_mfma_f32_16x16x32_bf16 v[24:27], v[136:139], v[8:11], v[24:27]\n" \
"v_mfma_f32_16x16x32_bf16 v[132:135], v[48:51], v[176:179], v[132:135]\n" \
"v_mfma_f32_16x16x32_bf16 v[140:143], v[80:83], v[176:179], v[140:143]\n" \
"v_mfma_f32_16x16x32_bf16 v[148:151], v[48:51], v[52:55], v[148:151]\n" \
"v_mfma_f32_16x16x32_bf16 v[156:159], v[80:83], v[52:55], v[156:159]\n" \
"v_mfma_f32_16x16x32_bf16 v[164:167], v[48:51], v[8:11], v[164:167]\n" \
"v_mfma_f32_16x16x32_bf16 v[172:175], v[80:83], v[8:11], v[172:175]\n" \
"v_mfma_f32_16x16x32_bf16 v[180:183], v[112:115], v[84:87], v[180:183]\n" \
"v_mfma_f32_16x16x32_bf16 v[188:191], v[144:147], v[84:87], v[188:191]\n" \
"v_mfma_f32_16x16x32_bf16 v[0:3], v[112:115], v[116:119], v[0:3]\n" \
"v_mfma_f32_16x16x32_bf16 v[4:7], v[144:147], v[116:119], v[4:7]\n" \
"v_mfma_f32_16x16x32_bf16 v[20:23], v[112:115], v[8:11], v[20:23]\n" \
"v_mfma_f32_16x16x32_bf16 v[24:27], v[144:147], v[8:11], v[24:27]\n" \
"v_mfma_f32_16x16x32_bf16 v[132:135], v[56:59], v[184:187], v[132:135]\n" \
"v_mfma_f32_16x16x32_bf16 v[140:143], v[88:91], v[184:187], v[140:143]\n" \
"v_mfma_f32_16x16x32_bf16 v[148:151], v[56:59], v[60:63], v[148:151]\n" \
"v_mfma_f32_16x16x32_bf16 v[156:159], v[88:91], v[60:63], v[156:159]\n" \
"v_mfma_f32_16x16x32_bf16 v[164:167], v[56:59], v[8:11], v[164:167]\n" \
"v_mfma_f32_16x16x32_bf16 v[172:175], v[88:91], v[8:11], v[172:175]\n" \
"v_mfma_f32_16x16x32_bf16 v[180:183], v[120:123], v[92:95], v[180:183]\n" \
"v_mfma_f32_16x16x32_bf16 v[188:191], v[152:155], v[92:95], v[188:191]\n" \
"v_mfma_f32_16x16x32_bf16 v[0:3], v[120:123], v[124:127], v[0:3]\n" \
"v_mfma_f32_16x16x32_bf16 v[4:7], v[152:155], v[124:127], v[4:7]\n" \
"v_mfma_f32_16x16x32_bf16 v[20:23], v[120:123], v[8:11], v[20:23]\n" \
"v_mfma_f32_16x16x32_bf16 v[24:27], v[152:155], v[8:11], v[24:27]\n" \
"s_nop 7\n" \
"s_cmp_lg_u32 s7, 0\n" \
"s_cbranch_scc1 L_kx1_%=\n" \
"ds_write_b128 v19, v[180:183] offset:0\n" \
"ds_write_b128 v19, v[188:191] offset:1024\n" \
"ds_write_b128 v19, v[0:3] offset:2048\n" \
"ds_write_b128 v19, v[4:7] offset:3072\n" \
"ds_write_b128 v19, v[20:23] offset:4096\n" \
"ds_write_b128 v19, v[24:27] offset:5120\n" \
"v_add_u32 v19, 0x3000, v19\n" \
"s_branch L_kx2_%=\n" \
"L_kx1_%=:\n" \
"ds_write_b128 v19, v[132:135] offset:0\n" \
"ds_write_b128 v19, v[140:143] offset:1024\n" \
"ds_write_b128 v19, v[148:151] offset:2048\n" \
"ds_write_b128 v19, v[156:159] offset:3072\n" \
"ds_write_b128 v19, v[164:167] offset:4096\n" \
"ds_write_b128 v19, v[172:175] offset:5120\n" \
"v_add_u32 v19, 0xffffd000, v19\n" \
"L_kx2_%=:\n" \
"v_pk_mul_f32 v[32:33], v[192:193], s[48:49]\n" \
"v_pk_mul_f32 v[34:35], v[194:195], s[48:49]\n" \
"v_pk_mul_f32 v[64:65], v[224:225], s[48:49]\n" \
"v_pk_mul_f32 v[66:67], v[226:227], s[48:49]\n" \
"v_exp_f32_e64 v32, v32 clamp\n" \
"v_max_f32 v192, s50, v192\n" \
"v_exp_f32_e64 v33, v33 clamp\n" \
"v_max_f32 v193, s50, v193\n" \
"v_exp_f32_e64 v34, v34 clamp\n" \
"v_max_f32 v194, s50, v194\n" \
"v_exp_f32_e64 v35, v35 clamp\n" \
"v_max_f32 v195, s50, v195\n" \
"v_exp_f32_e64 v64, v64 clamp\n" \
"v_max_f32 v224, s50, v224\n" \
"v_exp_f32_e64 v65, v65 clamp\n" \
"v_max_f32 v225, s50, v225\n" \
"v_exp_f32_e64 v66, v66 clamp\n" \
"v_max_f32 v226, s50, v226\n" \
"v_exp_f32_e64 v67, v67 clamp\n" \
"v_max_f32 v227, s50, v227\n" \
"v_pk_add_f32 v[192:193], v[192:193], v[32:33]\n" \
"v_pk_add_f32 v[194:195], v[194:195], v[34:35]\n" \
"v_pk_add_f32 v[224:225], v[224:225], v[64:65]\n" \
"v_pk_add_f32 v[226:227], v[226:227], v[66:67]\n" \
"v_cvt_pk_bf16_f32 v192, v192, v193\n" \
"v_cvt_pk_bf16_f32 v193, v194, v195\n" \
"v_cvt_pk_bf16_f32 v194, v224, v225\n" \
"v_cvt_pk_bf16_f32 v195, v226, v227\n" \
"buffer_store_dwordx4 v[192:195], v14, s[84:87], s28 offen offset:0 sc1\n" \
"v_pk_mul_f32 v[32:33], v[196:197], s[48:49]\n" \
"v_pk_mul_f32 v[34:35], v[198:199], s[48:49]\n" \
"v_pk_mul_f32 v[64:65], v[228:229], s[48:49]\n" \
"v_pk_mul_f32 v[66:67], v[230:231], s[48:49]\n" \
"v_exp_f32_e64 v32, v32 clamp\n" \
"v_max_f32 v196, s50, v196\n" \
"v_exp_f32_e64 v33, v33 clamp\n" \
"v_max_f32 v197, s50, v197\n" \
"v_exp_f32_e64 v34, v34 clamp\n" \
"v_max_f32 v198, s50, v198\n" \
"v_exp_f32_e64 v35, v35 clamp\n" \
"v_max_f32 v199, s50, v199\n" \
"v_exp_f32_e64 v64, v64 clamp\n" \
"v_max_f32 v228, s50, v228\n" \
"v_exp_f32_e64 v65, v65 clamp\n" \
"v_max_f32 v229, s50, v229\n" \
"v_exp_f32_e64 v66, v66 clamp\n" \
"v_max_f32 v230, s50, v230\n" \
"v_exp_f32_e64 v67, v67 clamp\n" \
"v_max_f32 v231, s50, v231\n" \
"v_pk_add_f32 v[196:197], v[196:197], v[32:33]\n" \
"v_pk_add_f32 v[198:199], v[198:199], v[34:35]\n" \
"v_pk_add_f32 v[228:229], v[228:229], v[64:65]\n" \
"v_pk_add_f32 v[230:231], v[230:231], v[66:67]\n" \
"v_cvt_pk_bf16_f32 v196, v196, v197\n" \
"v_cvt_pk_bf16_f32 v197, v198, v199\n" \
"v_cvt_pk_bf16_f32 v198, v228, v229\n" \
"v_cvt_pk_bf16_f32 v199, v230, v231\n" \
"buffer_store_dwordx4 v[196:199], v14, s[84:87], s28 offen offset:2048 sc1\n" \
"v_pk_mul_f32 v[32:33], v[200:201], s[48:49]\n" \
"v_pk_mul_f32 v[34:35], v[202:203], s[48:49]\n" \
"v_pk_mul_f32 v[64:65], v[232:233], s[48:49]\n" \
"v_pk_mul_f32 v[66:67], v[234:235], s[48:49]\n" \
"v_exp_f32_e64 v32, v32 clamp\n" \
"v_max_f32 v200, s50, v200\n" \
"v_exp_f32_e64 v33, v33 clamp\n" \
"v_max_f32 v201, s50, v201\n" \
"v_exp_f32_e64 v34, v34 clamp\n" \
"v_max_f32 v202, s50, v202\n" \
"v_exp_f32_e64 v35, v35 clamp\n" \
"v_max_f32 v203, s50, v203\n" \
"v_exp_f32_e64 v64, v64 clamp\n" \
"v_max_f32 v232, s50, v232\n" \
"v_exp_f32_e64 v65, v65 clamp\n" \
"v_max_f32 v233, s50, v233\n" \
"v_exp_f32_e64 v66, v66 clamp\n" \
"v_max_f32 v234, s50, v234\n" \
"v_exp_f32_e64 v67, v67 clamp\n" \
"v_max_f32 v235, s50, v235\n" \
"v_pk_add_f32 v[200:201], v[200:201], v[32:33]\n" \
"v_pk_add_f32 v[202:203], v[202:203], v[34:35]\n" \
"v_pk_add_f32 v[232:233], v[232:233], v[64:65]\n" \
"v_pk_add_f32 v[234:235], v[234:235], v[66:67]\n" \
"v_cvt_pk_bf16_f32 v200, v200, v201\n" \
"v_cvt_pk_bf16_f32 v201, v202, v203\n" \
"v_cvt_pk_bf16_f32 v202, v232, v233\n" \
"v_cvt_pk_bf16_f32 v203, v234, v235\n" \
"s_add_u32 s74, s28, 4096\n" \
"buffer_store_dwordx4 v[200:203], v14, s[84:87], s74 offen offset:0 sc1\n" \
"v_pk_mul_f32 v[32:33], v[204:205], s[48:49]\n" \
"v_pk_mul_f32 v[34:35], v[206:207], s[48:49]\n" \
"v_pk_mul_f32 v[64:65], v[236:237], s[48:49]\n" \
"v_pk_mul_f32 v[66:67], v[238:239], s[48:49]\n" \
"v_exp_f32_e64 v32, v32 clamp\n" \
"v_max_f32 v204, s50, v204\n" \
"v_exp_f32_e64 v33, v33 clamp\n" \
"v_max_f32 v205, s50, v205\n" \
"v_exp_f32_e64 v34, v34 clamp\n" \
"v_max_f32 v206, s50, v206\n" \
"v_exp_f32_e64 v35, v35 clamp\n" \
"v_max_f32 v207, s50, v207\n" \
"v_exp_f32_e64 v64, v64 clamp\n" \
"v_max_f32 v236, s50, v236\n" \
"v_exp_f32_e64 v65, v65 clamp\n" \
"v_max_f32 v237, s50, v237\n" \
"v_exp_f32_e64 v66, v66 clamp\n" \
"v_max_f32 v238, s50, v238\n" \
"v_exp_f32_e64 v67, v67 clamp\n" \
"v_max_f32 v239, s50, v239\n" \
"v_pk_add_f32 v[204:205], v[204:205], v[32:33]\n" \
"v_pk_add_f32 v[206:207], v[206:207], v[34:35]\n" \
"v_pk_add_f32 v[236:237], v[236:237], v[64:65]\n" \
"v_pk_add_f32 v[238:239], v[238:239], v[66:67]\n" \
"v_cvt_pk_bf16_f32 v204, v204, v205\n" \
"v_cvt_pk_bf16_f32 v205, v206, v207\n" \
"v_cvt_pk_bf16_f32 v206, v236, v237\n" \
"v_cvt_pk_bf16_f32 v207, v238, v239\n" \
"s_add_u32 s74, s28, 4096\n" \
"buffer_store_dwordx4 v[204:207], v14, s[84:87], s74 offen offset:2048 sc1\n" \
"v_pk_mul_f32 v[32:33], v[208:209], s[48:49]\n" \
"v_pk_mul_f32 v[34:35], v[210:211], s[48:49]\n" \
"v_pk_mul_f32 v[64:65], v[240:241], s[48:49]\n" \
"v_pk_mul_f32 v[66:67], v[242:243], s[48:49]\n" \
"v_exp_f32_e64 v32, v32 clamp\n" \
"v_max_f32 v208, s50, v208\n" \
"v_exp_f32_e64 v33, v33 clamp\n" \
"v_max_f32 v209, s50, v209\n" \
"v_exp_f32_e64 v34, v34 clamp\n" \
"v_max_f32 v210, s50, v210\n" \
"v_exp_f32_e64 v35, v35 clamp\n" \
"v_max_f32 v211, s50, v211\n" \
"v_exp_f32_e64 v64, v64 clamp\n" \
"v_max_f32 v240, s50, v240\n" \
"v_exp_f32_e64 v65, v65 clamp\n" \
"v_max_f32 v241, s50, v241\n" \
"v_exp_f32_e64 v66, v66 clamp\n" \
"v_max_f32 v242, s50, v242\n" \
"v_exp_f32_e64 v67, v67 clamp\n" \
"v_max_f32 v243, s50, v243\n" \
"v_pk_add_f32 v[208:209], v[208:209], v[32:33]\n" \
"v_pk_add_f32 v[210:211], v[210:211], v[34:35]\n" \
"v_pk_add_f32 v[240:241], v[240:241], v[64:65]\n" \
"v_pk_add_f32 v[242:243], v[242:243], v[66:67]\n" \
"v_cvt_pk_bf16_f32 v208, v208, v209\n" \
"v_cvt_pk_bf16_f32 v209, v210, v211\n" \
"v_cvt_pk_bf16_f32 v210, v240, v241\n" \
"v_cvt_pk_bf16_f32 v211, v242, v243\n" \
"s_add_u32 s74, s28, 8192\n" \
"buffer_store_dwordx4 v[208:211], v14, s[84:87], s74 offen offset:0 sc1\n" \
"s_cmp_lg_u32 s15, 0\n" \
"s_cbranch_scc1 L_ep4527_%=\n" \
"buffer_load_dwordx2 v[12:13], v30, s[96:99], 0 offen sc1\n" \
"L_ep4527_%=:\n" \
"v_pk_mul_f32 v[32:33], v[212:213], s[48:49]\n" \
"v_pk_mul_f32 v[34:35], v[214:215], s[48:49]\n" \
"v_pk_mul_f32 v[64:65], v[244:245], s[48:49]\n" \
"v_pk_mul_f32 v[66:67], v[246:247], s[48:49]\n" \
"v_exp_f32_e64 v32, v32 clamp\n" \
"v_max_f32 v212, s50, v212\n" \
"v_exp_f32_e64 v33, v33 clamp\n" \
"v_max_f32 v213, s50, v213\n" \
"v_exp_f32_e64 v34, v34 clamp\n" \
"v_max_f32 v214, s50, v214\n" \
"v_exp_f32_e64 v35, v35 clamp\n" \
"v_max_f32 v215, s50, v215\n" \
"v_exp_f32_e64 v64, v64 clamp\n" \
"v_max_f32 v244, s50, v244\n" \
"v_exp_f32_e64 v65, v65 clamp\n" \
"v_max_f32 v245, s50, v245\n" \
"v_exp_f32_e64 v66, v66 clamp\n" \
"v_max_f32 v246, s50, v246\n" \
"v_exp_f32_e64 v67, v67 clamp\n" \
"v_max_f32 v247, s50, v247\n" \
"v_pk_add_f32 v[212:213], v[212:213], v[32:33]\n" \
"v_pk_add_f32 v[214:215], v[214:215], v[34:35]\n" \
"v_pk_add_f32 v[244:245], v[244:245], v[64:65]\n" \
"v_pk_add_f32 v[246:247], v[246:247], v[66:67]\n" \
"v_cvt_pk_bf16_f32 v212, v212, v213\n" \
"v_cvt_pk_bf16_f32 v213, v214, v215\n" \
"v_cvt_pk_bf16_f32 v214, v244, v245\n" \
"v_cvt_pk_bf16_f32 v215, v246, v247\n" \
"s_add_u32 s74, s28, 8192\n" \
"buffer_store_dwordx4 v[212:215], v14, s[84:87], s74 offen offset:2048 sc1\n" \
"v_pk_mul_f32 v[32:33], v[216:217], s[48:49]\n" \
"v_pk_mul_f32 v[34:35], v[218:219], s[48:49]\n" \
"v_pk_mul_f32 v[64:65], v[248:249], s[48:49]\n" \
"v_pk_mul_f32 v[66:67], v[250:251], s[48:49]\n" \
"v_exp_f32_e64 v32, v32 clamp\n" \
"v_max_f32 v216, s50, v216\n" \
"v_exp_f32_e64 v33, v33 clamp\n" \
"v_max_f32 v217, s50, v217\n" \
"v_exp_f32_e64 v34, v34 clamp\n" \
"v_max_f32 v218, s50, v218\n" \
"v_exp_f32_e64 v35, v35 clamp\n" \
"v_max_f32 v219, s50, v219\n" \
"v_exp_f32_e64 v64, v64 clamp\n" \
"v_max_f32 v248, s50, v248\n" \
"v_exp_f32_e64 v65, v65 clamp\n" \
"v_max_f32 v249, s50, v249\n" \
"v_exp_f32_e64 v66, v66 clamp\n" \
"v_max_f32 v250, s50, v250\n" \
"v_exp_f32_e64 v67, v67 clamp\n" \
"v_max_f32 v251, s50, v251\n" \
"v_pk_add_f32 v[216:217], v[216:217], v[32:33]\n" \
"v_pk_add_f32 v[218:219], v[218:219], v[34:35]\n" \
"v_pk_add_f32 v[248:249], v[248:249], v[64:65]\n" \
"v_pk_add_f32 v[250:251], v[250:251], v[66:67]\n" \
"v_cvt_pk_bf16_f32 v216, v216, v217\n" \
"v_cvt_pk_bf16_f32 v217, v218, v219\n" \
"v_cvt_pk_bf16_f32 v218, v248, v249\n" \
"v_cvt_pk_bf16_f32 v219, v250, v251\n" \
"s_add_u32 s74, s28, 12288\n" \
"buffer_store_dwordx4 v[216:219], v14, s[84:87], s74 offen offset:0 sc1\n" \
"v_pk_mul_f32 v[32:33], v[220:221], s[48:49]\n" \
"v_pk_mul_f32 v[34:35], v[222:223], s[48:49]\n" \
"v_pk_mul_f32 v[64:65], v[252:253], s[48:49]\n" \
"v_pk_mul_f32 v[66:67], v[254:255], s[48:49]\n" \
"v_exp_f32_e64 v32, v32 clamp\n" \
"v_max_f32 v220, s50, v220\n" \
"v_exp_f32_e64 v33, v33 clamp\n" \
"v_max_f32 v221, s50, v221\n" \
"v_exp_f32_e64 v34, v34 clamp\n" \
"v_max_f32 v222, s50, v222\n" \
"v_exp_f32_e64 v35, v35 clamp\n" \
"v_max_f32 v223, s50, v223\n" \
"v_exp_f32_e64 v64, v64 clamp\n" \
"v_max_f32 v252, s50, v252\n" \
"v_exp_f32_e64 v65, v65 clamp\n" \
"v_max_f32 v253, s50, v253\n" \
"v_exp_f32_e64 v66, v66 clamp\n" \
"v_max_f32 v254, s50, v254\n" \
"v_exp_f32_e64 v67, v67 clamp\n" \
"v_max_f32 v255, s50, v255\n" \
"v_pk_add_f32 v[220:221], v[220:221], v[32:33]\n" \
"v_pk_add_f32 v[222:223], v[222:223], v[34:35]\n" \
"v_pk_add_f32 v[252:253], v[252:253], v[64:65]\n" \
"v_pk_add_f32 v[254:255], v[254:255], v[66:67]\n" \
"v_cvt_pk_bf16_f32 v220, v220, v221\n" \
"v_cvt_pk_bf16_f32 v221, v222, v223\n" \
"v_cvt_pk_bf16_f32 v222, v252, v253\n" \
"v_cvt_pk_bf16_f32 v223, v254, v255\n" \
"s_add_u32 s74, s28, 12288\n" \
"buffer_store_dwordx4 v[220:223], v14, s[84:87], s74 offen offset:2048 sc1\n" \
"s_waitcnt lgkmcnt(0)\n" \
"s_barrier\n" \
"ds_read_b128 v[96:99], v19 offset:0\n" \
"ds_read_b128 v[100:103], v19 offset:1024\n" \
"ds_read_b128 v[104:107], v19 offset:2048\n" \
"ds_read_b128 v[108:111], v19 offset:3072\n" \
"ds_read_b128 v[112:115], v19 offset:4096\n" \
"ds_read_b128 v[116:119], v19 offset:5120\n" \
"s_cmp_eq_u32 s7, 0\n" \
"s_cbranch_scc1 L_kx3_%=\n" \
"v_mov_b32 v132, v180\n" \
"v_mov_b32 v133, v181\n" \
"v_mov_b32 v134, v182\n" \
"v_mov_b32 v135, v183\n" \
"v_mov_b32 v140, v188\n" \
"v_mov_b32 v141, v189\n" \
"v_mov_b32 v142, v190\n" \
"v_mov_b32 v143, v191\n" \
"v_mov_b32 v148, v0\n" \
"v_mov_b32 v149, v1\n" \
"v_mov_b32 v150, v2\n" \
"v_mov_b32 v151, v3\n" \
"v_mov_b32 v156, v4\n" \
"v_mov_b32 v157, v5\n" \
"v_mov_b32 v158, v6\n" \
"v_mov_b32 v159, v7\n" \
"v_mov_b32 v164, v20\n" \
"v_mov_b32 v165, v21\n" \
"v_mov_b32 v166, v22\n" \
"v_mov_b32 v167, v23\n" \
"v_mov_b32 v172, v24\n" \
"v_mov_b32 v173, v25\n" \
"v_mov_b32 v174, v26\n" \
"v_mov_b32 v175, v27\n" \
"L_kx3_%=:\n" \
"s_waitcnt lgkmcnt(0)\n" \
"v_pk_add_f32 v[96:97], v[96:97], v[132:133]\n" \
"v_pk_add_f32 v[98:99], v[98:99], v[134:135]\n" \
"buffer_store_dwordx4 v[96:99], v14, s[80:83], s29 offen offset:0 sc1\n" \
"v_pk_add_f32 v[100:101], v[100:101], v[140:141]\n" \
"v_pk_add_f32 v[102:103], v[102:103], v[142:143]\n" \
"buffer_store_dwordx4 v[100:103], v14, s[80:83], s29 offen offset:1024 sc1\n" \
"v_pk_add_f32 v[104:105], v[104:105], v[148:149]\n" \
"v_pk_add_f32 v[106:107], v[106:107], v[150:151]\n" \
"buffer_store_dwordx4 v[104:107], v14, s[80:83], s29 offen offset:2048 sc1\n" \
"v_pk_add_f32 v[108:109], v[108:109], v[156:157]\n" \
"v_pk_add_f32 v[110:111], v[110:111], v[158:159]\n" \
"buffer_store_dwordx4 v[108:111], v14, s[80:83], s29 offen offset:3072 sc1\n" \
"v_pk_add_f32 v[112:113], v[112:113], v[164:165]\n" \
"v_pk_add_f32 v[114:115], v[114:115], v[166:167]\n" \
"s_add_u32 s74, s29, 4096\n" \
"buffer_store_dwordx4 v[112:115], v18, s[80:83], s74 offen offset:0 sc1\n" \
"v_pk_add_f32 v[116:117], v[116:117], v[172:173]\n" \
"v_pk_add_f32 v[118:119], v[118:119], v[174:175]\n" \
"s_add_u32 s74, s29, 4096\n" \
"buffer_store_dwordx4 v[116:119], v18, s[80:83], s74 offen offset:1024 sc1\n" \
"s_waitcnt vmcnt(9)\n" \
"L_end_%=:\n" \
"L_g2_code:\n" \
"s_load_dwordx2 s[4:5], s[12:13], 0x0\n" \
"s_load_dwordx2 s[8:9], s[12:13], 0x38\n" \
"s_mov_b32 s62, s15\n" \
"v_mov_b32 v122, 0\n" \
"v_mov_b32 v123, 0\n" \
"s_and_b32 s2, s14, 1\n" \
"s_lshr_b32 s66, s14, 3\n" \
"s_and_b32 s67, s14, 6\n" \
"s_lshl_b32 s67, s67, 1\n" \
"s_and_b32 s24, s66, 3\n" \
"s_add_u32 s24, s24, s67\n" \
"s_mul_i32 s74, s24, 72\n" \
"s_mul_i32 s76, s2, 20\n" \
"s_add_u32 s74, s74, s76\n" \
"s_lshl_b32 s74, s74, 14\n" \
"s_add_u32 s74, s74, 0x400000\n" \
"s_add_u32 s36, s10, s74\n" \
"s_addc_u32 s37, s11, 0\n" \
"s_and_b32 s37, s37, 0xffff\n" \
"s_sub_u32 s38, 72, s76\n" \
"s_lshl_b32 s38, s38, 14\n" \
"s_mov_b32 s39, 0x20000\n" \
"s_mov_b32 s70, s79\n" \
"s_cmp_lg_u32 s62, 0\n" \
"s_cbranch_scc1 L_w0se_%=\n" \
"v_cmp_eq_u64 vcc, s[34:35], v[12:13]\n" \
"s_nop 1\n" \
"s_cmp_eq_u64 vcc, -1\n" \
"s_cbranch_scc1 L_oke_%=\n" \
"s_mov_b32 s76, 0x40000\n" \
"L_spe_%=:\n" \
"buffer_load_dwordx2 v[12:13], v30, s[96:99], 0 offen offset:0 sc1\n" \
"s_waitcnt vmcnt(0)\n" \
"v_cmp_eq_u64 vcc, s[34:35], v[12:13]\n" \
"s_nop 1\n" \
"s_cmp_eq_u64 vcc, -1\n" \
"s_cbranch_scc1 L_oke_%=\n" \
"s_sleep 1\n" \
"s_sub_u32 s76, s76, 1\n" \
"s_cmp_lg_u32 s76, 0\n" \
"s_cbranch_scc1 L_spe_%=\n" \
"s_mov_b32 s33, 1\n" \
"L_oke_%=:\n" \
"buffer_store_dwordx2 v[122:123], v30, s[96:99], 0 offen offset:0 sc1\n" \
"L_w0se_%=:\n" \
"v_add_u32 v12, s70, v14\n" \
"s_waitcnt lgkmcnt(0)\n" \
"s_barrier\n" \
"s_add_u32 m0, s70, 32768\n" \
"s_mov_b32 s64, 65536\n" \
"s_nop 0\n" \
"buffer_load_dwordx4 v12, s[36:39], s64 offen offset:0 sc1 lds\n" \
"s_mov_b32 s77, s33\n" \
"s_mov_b32 s28, s34\n" \
"s_mov_b32 s29, s35\n" \
"s_mov_b32 s35, s24\n" \
"s_lshl_b32 s60, s14, 11\n" \
"s_mov_b64 s[80:81], s[96:97]\n" \
"s_mov_b64 s[82:83], s[98:99]\n" \
"s_mov_b64 s[40:41], s[88:89]\n" \
"s_mov_b64 s[42:43], s[90:91]\n" \
"s_mov_b64 s[44:45], s[92:93]\n" \
"s_mov_b64 s[46:47], s[94:95]\n" \
"s_lshr_b32 s72, s79, 1\n" \
"s_sub_u32 s69, s38, 262144\n" \
"s_mul_i32 s73, s2, 196608\n" \
"s_mul_i32 s75, s2, 1572864\n" \
"s_lshl_b32 s74, s35, 3\n" \
"s_add_u32 s74, s74, s3\n" \
"s_lshl_b32 s74, s74, 1\n" \
"s_add_u32 s74, s74, s2\n" \
"s_lshl_b32 s74, s74, 2\n" \
"buffer_load_dwordx4 v12, s[36:39], s64 offen offset:1024 sc1 lds\n" \
"s_add_u32 s74, s74, s15\n" \
"s_lshl_b32 s74, s74, 13\n" \
"s_add_u32 s74, s74, 0x1c00000\n" \
"s_add_u32 s48, s10, s74\n" \
"s_addc_u32 s49, s11, 0\n" \
"s_and_b32 s49, s49, 0xffff\n" \
"s_xor_b32 s74, s74, 0x8000\n" \
"s_add_u32 s52, s10, s74\n" \
"s_addc_u32 s53, s11, 0\n" \
"s_and_b32 s53, s53, 0xffff\n" \
"s_mul_i32 s74, s66, 6144\n" \
"s_add_u32 s74, s74, 0x1a00000\n" \
"s_add_u32 s84, s10, s74\n" \
"s_addc_u32 s85, s11, 0\n" \
"s_and_b32 s85, s85, 0xffff\n" \
"s_lshr_b32 s76, s66, 1\n" \
"s_lshl_b32 s76, s76, 14\n" \
"s_and_b32 s74, s66, 1\n" \
"s_lshl_b32 s74, s74, 10\n" \
"s_add_u32 s76, s76, s74\n" \
"buffer_load_dwordx4 v12, s[36:39], s64 offen offset:2048 sc1 lds\n" \
"s_lshl_b32 s20, s2, 12\n" \
"s_add_u32 s76, s76, s20\n" \
"s_add_u32 s67, s67, s15\n" \
"s_lshl_b32 s74, s67, 18\n" \
"s_add_u32 s74, s74, s76\n" \
"s_add_u32 s74, s74, 0x1600000\n" \
"s_add_u32 s88, s10, s74\n" \
"s_addc_u32 s89, s11, 0\n" \
"s_and_b32 s89, s89, 0xffff\n" \
"s_mul_i32 s74, s67, 1179648\n" \
"s_add_u32 s74, s74, s76\n" \
"s_add_u32 s74, s74, 0x4e0000\n" \
"s_add_u32 s96, s10, s74\n" \
"s_addc_u32 s97, s11, 0\n" \
"s_and_b32 s97, s97, 0xffff\n" \
"s_lshl_b32 s21, s20, 0\n" \
"s_lshl_b32 s74, s7, 13\n" \
"s_add_u32 s20, s20, s74\n" \
"s_lshl_b32 s74, s6, 11\n" \
"s_add_u32 s21, s20, s74\n" \
"buffer_load_dwordx4 v12, s[36:39], s64 offen offset:3072 sc1 lds\n" \
"s_lshl_b32 s22, s7, 12\n" \
"s_lshl_b32 s23, s6, 7\n" \
"s_mov_b32 s25, 10\n" \
"s_and_b32 s27, s14, 7\n" \
"s_lshl_b32 s27, s27, 11\n" \
"s_lshl_b32 s74, s66, 3\n" \
"s_add_u32 s27, s27, s74\n" \
"s_add_u32 s27, s27, 768\n" \
"s_xor_b32 s34, s60, 2048\n" \
"s_add_u32 s34, s34, 1024\n" \
"s_cmp_eq_u32 s15, 0\n" \
"s_cselect_b32 s26, 32, 0\n" \
"s_cmp_eq_u32 s15, 2\n" \
"s_cselect_b32 s61, 4, 0\n" \
"s_cselect_b32 s63, 48, 0xffff\n" \
"s_cmp_eq_u32 s15, 3\n" \
"s_cselect_b32 s63, 0, s63\n" \
"s_cselect_b32 s92, 0x7ffffff0, s72\n" \
"s_lshl_b32 s74, s35, 18\n" \
"s_lshl_b32 s76, s20, 4\n" \
"s_waitcnt vmcnt(4)\n" \
"s_barrier\n" \
"buffer_store_dwordx2 v[28:29], v16, s[80:83], 0 offen sc1\n" \
"s_add_u32 m0, s70, 49152\n" \
"s_mov_b32 s64, 147456\n" \
"s_nop 0\n" \
"buffer_load_dwordx4 v12, s[36:39], s64 offen offset:0 sc1 lds\n" \
"s_add_u32 s74, s74, s76\n" \
"s_lshl_b32 s76, s3, 8\n" \
"s_add_u32 s74, s74, s76\n" \
"s_sub_u32 s58, 0x10000, s76\n" \
"s_mov_b32 s18, s58\n" \
"s_mov_b32 s50, 8192\n" \
"s_mov_b32 s54, 8192\n" \
"s_mov_b32 s86, 1382400\n" \
"s_mov_b32 s90, 12288\n" \
"s_mov_b32 s98, 12288\n" \
"s_mov_b32 s78, 4096\n" \
"s_mov_b32 s79, 32768\n" \
"s_mov_b32 s94, 8192\n" \
"s_mov_b32 s19, 0x20000\n" \
"s_mov_b32 s51, 0x20000\n" \
"s_mov_b32 s55, 0x20000\n" \
"s_mov_b32 s59, 0x20000\n" \
"s_add_u32 s56, s4, s74\n" \
"s_addc_u32 s57, s5, 0\n" \
"s_and_b32 s57, s57, 0xffff\n" \
"buffer_load_dwordx4 v12, s[36:39], s64 offen offset:1024 sc1 lds\n" \
"s_add_u32 s16, s8, s74\n" \
"s_addc_u32 s17, s9, 0\n" \
"s_and_b32 s17, s17, 0xffff\n" \
"v_mov_b32 v13, v15\n" \
"v_mov_b32 v15, v14\n" \
"v_lshrrev_b32 v62, 4, v15\n" \
"v_add_u32 v0, s20, v15\n" \
"v_add_u32 v1, 0x10000, v0\n" \
"v_xor_b32 v2, 0x1000, v0\n" \
"v_add_u32 v3, 0x10000, v2\n" \
"v_mov_b32 v4, v17\n" \
"v_xor_b32 v5, 32, v4\n" \
"v_xor_b32 v6, 64, v4\n" \
"v_xor_b32 v7, 96, v4\n" \
"v_add_u32 v8, 0x10000, v4\n" \
"v_add_u32 v9, 0x10000, v5\n" \
"v_add_u32 v10, 0x10000, v6\n" \
"v_add_u32 v11, 0x10000, v7\n" \
"v_add_u32 v14, s21, v15\n" \
"v_add_u32 v126, s22, v15\n" \
"buffer_load_dwordx4 v12, s[36:39], s64 offen offset:2048 sc1 lds\n" \
"v_add_u32 v127, 0x10000, v126\n" \
"v_add_u32 v115, 0x20000, v15\n" \
"s_nop 0\n" \
"v_and_b32 v61, 15, v62\n" \
"v_lshlrev_b32 v114, 11, v61\n" \
"v_add_u32 v114, s23, v114\n" \
"v_and_b32 v116, 16, v62\n" \
"v_lshl_add_u32 v114, v116, 1, v114\n" \
"v_lshrrev_b32 v116, 1, v62\n" \
"v_and_b32 v116, 16, v116\n" \
"v_add_u32 v114, v114, v116\n" \
"v_and_b32 v117, 31, v62\n" \
"v_lshlrev_b32 v117, 3, v117\n" \
"v_cmp_gt_u32 vcc, s25, v62\n" \
"v_lshlrev_b32 v116, 3, v62\n" \
"v_mov_b32 v119, 0x7ffffff0\n" \
"v_cndmask_b32 v116, 0, v116, vcc\n" \
"v_cmp_gt_u32 vcc, s26, v62\n" \
"v_lshlrev_b32 v118, 14, v62\n" \
"v_add_u32 v118, s27, v118\n" \
"buffer_load_dwordx4 v12, s[36:39], s64 offen offset:3072 sc1 lds\n" \
"v_cndmask_b32 v118, v119, v118, vcc\n" \
"s_lshr_b32 s74, s26, 5\n" \
"v_mov_b32 v61, s34\n" \
"v_cmp_gt_u32 vcc, s74, v62\n" \
"s_nop 1\n" \
"v_cndmask_b32 v119, v119, v61, vcc\n" \
"v_mov_b32 v120, s28\n" \
"v_mov_b32 v121, s29\n" \
"v_and_b32 v61, 15, v62\n" \
"v_cmp_gt_u32 vcc, 12, v61\n" \
"v_lshlrev_b32 v124, 3, v61\n" \
"s_nop 0\n" \
"v_cndmask_b32 v124, 0, v124, vcc\n" \
"v_add_u32 v196, 0x20000, v4\n" \
"v_add_u32 v197, 0x20000, v5\n" \
"v_add_u32 v198, 0x20000, v6\n" \
"v_add_u32 v199, 0x20000, v7\n" \
"ds_read_b64_tr_b16 a[0:1], v8 offset:32768\n" \
"ds_read_b64_tr_b16 a[2:3], v8 offset:36864\n" \
"ds_read_b64_tr_b16 a[4:5], v8 offset:40960\n" \
"ds_read_b64_tr_b16 a[6:7], v8 offset:45056\n" \
"ds_read_b64_tr_b16 a[8:9], v9 offset:32768\n" \
"ds_read_b64_tr_b16 a[10:11], v9 offset:36864\n" \
"ds_read_b64_tr_b16 a[12:13], v9 offset:40960\n" \
"ds_read_b64_tr_b16 a[14:15], v9 offset:45056\n" \
"ds_read_b64_tr_b16 a[16:17], v10 offset:32768\n" \
"ds_read_b64_tr_b16 a[18:19], v10 offset:36864\n" \
"ds_read_b64_tr_b16 a[20:21], v10 offset:40960\n" \
"ds_read_b64_tr_b16 a[22:23], v10 offset:45056\n" \
"ds_read_b64_tr_b16 a[24:25], v11 offset:32768\n" \
"ds_read_b64_tr_b16 a[26:27], v11 offset:36864\n" \
"ds_read_b64_tr_b16 a[28:29], v11 offset:40960\n" \
"ds_read_b64_tr_b16 a[30:31], v11 offset:45056\n" \
"ds_read_b64_tr_b16 a[32:33], v8 offset:49152\n" \
"ds_read_b64_tr_b16 a[34:35], v8 offset:53248\n" \
"ds_read_b64_tr_b16 a[36:37], v8 offset:57344\n" \
"ds_read_b64_tr_b16 a[38:39], v8 offset:61440\n" \
"ds_read_b64_tr_b16 a[40:41], v9 offset:49152\n" \
"ds_read_b64_tr_b16 a[42:43], v9 offset:53248\n" \
"ds_read_b64_tr_b16 a[44:45], v9 offset:57344\n" \
"ds_read_b64_tr_b16 a[46:47], v9 offset:61440\n" \
"ds_read_b64_tr_b16 a[48:49], v10 offset:49152\n" \
"ds_read_b64_tr_b16 a[50:51], v10 offset:53248\n" \
"ds_read_b64_tr_b16 a[52:53], v10 offset:57344\n" \
"ds_read_b64_tr_b16 a[54:55], v10 offset:61440\n" \
"ds_read_b64_tr_b16 a[56:57], v11 offset:49152\n" \
"ds_read_b64_tr_b16 a[58:59], v11 offset:53248\n" \
"ds_read_b64_tr_b16 a[60:61], v11 offset:57344\n" \
"ds_read_b64_tr_b16 a[62:63], v11 offset:61440\n" \
"ds_read_b64_tr_b16 a[64:65], v196 offset:16384\n" \
"ds_read_b64_tr_b16 a[66:67], v196 offset:20480\n" \
"ds_read_b64_tr_b16 a[68:69], v196 offset:24576\n" \
"ds_read_b64_tr_b16 a[70:71], v196 offset:28672\n" \
"ds_read_b64_tr_b16 a[72:73], v197 offset:16384\n" \
"ds_read_b64_tr_b16 a[74:75], v197 offset:20480\n" \
"ds_read_b64_tr_b16 a[76:77], v197 offset:24576\n" \
"ds_read_b64_tr_b16 a[78:79], v197 offset:28672\n" \
"ds_read_b64_tr_b16 a[80:81], v198 offset:16384\n" \
"ds_read_b64_tr_b16 a[82:83], v198 offset:20480\n" \
"ds_read_b64_tr_b16 a[84:85], v198 offset:24576\n" \
"ds_read_b64_tr_b16 a[86:87], v198 offset:28672\n" \
"ds_read_b64_tr_b16 a[88:89], v199 offset:16384\n" \
"ds_read_b64_tr_b16 a[90:91], v199 offset:20480\n" \
"ds_read_b64_tr_b16 a[92:93], v199 offset:24576\n" \
"ds_read_b64_tr_b16 a[94:95], v199 offset:28672\n" \
"s_waitcnt vmcnt(5) lgkmcnt(0)\n" \
"s_barrier\n" \
"ds_read_b64_tr_b16 v[80:81], v4 offset:0\n" \
"ds_read_b64_tr_b16 v[82:83], v4 offset:4096\n" \
"ds_read_b64_tr_b16 v[84:85], v4 offset:8192\n" \
"ds_read_b64_tr_b16 v[86:87], v4 offset:12288\n" \
"ds_read_b128 v[16:19], v0 offset:32768\n" \
"ds_read_b128 v[20:23], v0 offset:33792\n" \
"ds_read_b128 v[24:27], v0 offset:34816\n" \
"ds_read_b128 v[28:31], v0 offset:35840\n" \
"ds_read_b128 v[32:35], v2 offset:32768\n" \
"ds_read_b128 v[36:39], v2 offset:33792\n" \
"ds_read_b128 v[40:43], v2 offset:34816\n" \
"ds_read_b128 v[44:47], v2 offset:35840\n" \
"ds_read_b64_tr_b16 v[88:89], v5 offset:0\n" \
"ds_read_b64_tr_b16 v[90:91], v5 offset:4096\n" \
"ds_read_b64_tr_b16 v[92:93], v5 offset:8192\n" \
"ds_read_b64_tr_b16 v[94:95], v5 offset:12288\n" \
"s_waitcnt lgkmcnt(4)\n" \
"v_mfma_f32_16x16x32_bf16 v[128:131], v[80:83], v[16:19], 0\n" \
"ds_read_b64_tr_b16 v[96:97], v6 offset:0\n" \
"s_add_u32 m0, s70, 98304\n" \
"s_mov_b32 s64, 229376\n" \
"v_mfma_f32_16x16x32_bf16 v[128:131], v[84:87], v[20:23], v[128:131]\n" \
"ds_read_b64_tr_b16 v[98:99], v6 offset:4096\n" \
"buffer_load_dwordx4 v12, s[36:39], s64 offen offset:0 sc1 lds\n" \
"v_mfma_f32_16x16x32_bf16 v[144:147], v[84:87], v[28:31], 0\n" \
"ds_read_b64_tr_b16 v[100:101], v6 offset:8192\n" \
"v_mfma_f32_16x16x32_bf16 v[144:147], v[80:83], v[24:27], v[144:147]\n" \
"ds_read_b64_tr_b16 v[102:103], v6 offset:12288\n" \
"v_mfma_f32_16x16x32_bf16 v[160:163], v[80:83], v[32:35], 0\n" \
"buffer_load_dwordx4 v12, s[36:39], s64 offen offset:1024 sc1 lds\n" \
"v_mfma_f32_16x16x32_bf16 v[160:163], v[84:87], v[36:39], v[160:163]\n" \
"v_mfma_f32_16x16x32_bf16 v[176:179], v[84:87], v[44:47], 0\n" \
"v_mfma_f32_16x16x32_bf16 v[176:179], v[80:83], v[40:43], v[176:179]\n" \
"buffer_load_dwordx4 v12, s[36:39], s64 offen offset:2048 sc1 lds\n" \
"s_waitcnt lgkmcnt(4)\n" \
"v_mfma_f32_16x16x32_bf16 v[180:183], v[88:91], v[40:43], 0\n" \
"ds_read_b64_tr_b16 v[104:105], v7 offset:0\n" \
"v_mfma_f32_16x16x32_bf16 v[180:183], v[92:95], v[44:47], v[180:183]\n" \
"ds_read_b64_tr_b16 v[106:107], v7 offset:4096\n" \
"v_mfma_f32_16x16x32_bf16 v[164:167], v[92:95], v[36:39], 0\n" \
"ds_read_b64_tr_b16 v[108:109], v7 offset:8192\n" \
"buffer_load_dwordx4 v12, s[36:39], s64 offen offset:3072 sc1 lds\n" \
"v_mfma_f32_16x16x32_bf16 v[164:167], v[88:91], v[32:35], v[164:167]\n" \
"ds_read_b64_tr_b16 v[110:111], v7 offset:12288\n" \
"v_mfma_f32_16x16x32_bf16 v[148:151], v[88:91], v[24:27], 0\n" \
"s_add_u32 m0, s70, 114688\n" \
"s_mov_b32 s64, 311296\n" \
"v_mfma_f32_16x16x32_bf16 v[148:151], v[92:95], v[28:31], v[148:151]\n" \
"buffer_load_dwordx4 v12, s[36:39], s64 offen offset:0 sc1 lds\n" \
"v_mfma_f32_16x16x32_bf16 v[132:135], v[92:95], v[20:23], 0\n" \
"v_mfma_f32_16x16x32_bf16 v[132:135], v[88:91], v[16:19], v[132:135]\n" \
"s_waitcnt vmcnt(5) lgkmcnt(0)\n" \
"s_barrier\n" \
"v_mfma_f32_16x16x32_bf16 v[136:139], v[96:99], v[16:19], 0\n" \
"ds_read_b64_tr_b16 v[80:81], v4 offset:16384\n" \
"ds_read_b64_tr_b16 v[82:83], v4 offset:20480\n" \
"v_mfma_f32_16x16x32_bf16 v[136:139], v[100:103], v[20:23], v[136:139]\n" \
"ds_read_b64_tr_b16 v[84:85], v4 offset:24576\n" \
"ds_read_b64_tr_b16 v[86:87], v4 offset:28672\n" \
"buffer_load_dwordx4 v12, s[36:39], s64 offen offset:1024 sc1 lds\n" \
"v_mfma_f32_16x16x32_bf16 v[152:155], v[100:103], v[28:31], 0\n" \
"ds_read_b128 v[48:51], v0 offset:49152\n" \
"ds_read_b128 v[52:55], v0 offset:50176\n" \
"v_mfma_f32_16x16x32_bf16 v[152:155], v[96:99], v[24:27], v[152:155]\n" \
"ds_read_b128 v[56:59], v0 offset:51200\n" \
"ds_read_b128 v[60:63], v0 offset:52224\n" \
"v_mfma_f32_16x16x32_bf16 v[168:171], v[96:99], v[32:35], 0\n" \
"ds_read_b128 v[64:67], v2 offset:49152\n" \
"buffer_load_dwordx4 v12, s[36:39], s64 offen offset:2048 sc1 lds\n" \
"v_mfma_f32_16x16x32_bf16 v[168:171], v[100:103], v[36:39], v[168:171]\n" \
"ds_read_b128 v[68:71], v2 offset:50176\n" \
"v_mfma_f32_16x16x32_bf16 v[184:187], v[100:103], v[44:47], 0\n" \
"ds_read_b128 v[72:75], v2 offset:51200\n" \
"v_mfma_f32_16x16x32_bf16 v[184:187], v[96:99], v[40:43], v[184:187]\n" \
"ds_read_b128 v[76:79], v2 offset:52224\n" \
"buffer_load_dwordx4 v12, s[36:39], s64 offen offset:3072 sc1 lds\n" \
"v_mfma_f32_16x16x32_bf16 v[188:191], v[104:107], v[40:43], 0\n" \
"ds_read_b64_tr_b16 v[88:89], v5 offset:16384\n" \
"s_add_u32 m0, s70, 131072\n" \
"s_mov_b32 s74, 1704960\n" \
"v_mfma_f32_16x16x32_bf16 v[188:191], v[108:111], v[44:47], v[188:191]\n" \
"ds_read_b64_tr_b16 v[90:91], v5 offset:20480\n" \
"buffer_load_dwordx4 v13, s[44:47], s74 offen offset:0 lds\n" \
"v_mfma_f32_16x16x32_bf16 v[172:175], v[108:111], v[36:39], 0\n" \
"ds_read_b64_tr_b16 v[92:93], v5 offset:24576\n" \
"s_mov_b32 s74, 1704064\n" \
"v_mfma_f32_16x16x32_bf16 v[172:175], v[104:107], v[32:35], v[172:175]\n" \
"ds_read_b64_tr_b16 v[94:95], v5 offset:28672\n" \
"buffer_load_dwordx4 v13, s[44:47], s74 offen offset:1024 lds\n" \
"v_mfma_f32_16x16x32_bf16 v[156:159], v[104:107], v[24:27], 0\n" \
"s_mov_b32 s74, 1719296\n" \
"v_mfma_f32_16x16x32_bf16 v[156:159], v[108:111], v[28:31], v[156:159]\n" \
"buffer_load_dwordx4 v13, s[44:47], s74 offen offset:2048 lds\n" \
"v_mfma_f32_16x16x32_bf16 v[140:143], v[108:111], v[20:23], 0\n" \
"s_mov_b32 s74, 1718400\n" \
"v_mfma_f32_16x16x32_bf16 v[140:143], v[104:107], v[16:19], v[140:143]\n" \
"buffer_load_dwordx4 v13, s[44:47], s74 offen offset:3072 lds\n" \
"s_cmp_lg_u32 s62, 0\n" \
"s_cbranch_scc1 L_w0ph_%=\n" \
"buffer_load_dwordx2 v[112:113], v124, s[80:83], s60 offen offset:256 sc1\n" \
"L_w0ph_%=:\n" \
"s_waitcnt lgkmcnt(4)\n" \
"v_mfma_f32_16x16x32_bf16 v[128:131], v[80:83], v[48:51], v[128:131]\n" \
"ds_read_b64_tr_b16 v[96:97], v6 offset:16384\n" \
"v_mfma_f32_16x16x32_bf16 v[128:131], v[84:87], v[52:55], v[128:131]\n" \
"ds_read_b64_tr_b16 v[98:99], v6 offset:20480\n" \
"s_add_u32 m0, s70, 0\n" \
"s_mov_b32 s65, 1024\n" \
"v_mfma_f32_16x16x32_bf16 v[144:147], v[84:87], v[60:63], v[144:147]\n" \
"ds_read_b64_tr_b16 v[100:101], v6 offset:24576\n" \
"buffer_load_dwordx4 v13, s[40:43], s65 offen offset:0 lds\n" \
"v_mfma_f32_16x16x32_bf16 v[144:147], v[80:83], v[56:59], v[144:147]\n" \
"ds_read_b64_tr_b16 v[102:103], v6 offset:28672\n" \
"v_mfma_f32_16x16x32_bf16 v[160:163], v[80:83], v[64:67], v[160:163]\n" \
"buffer_load_dwordx4 a[224:227], v114, s[56:59], 0 offen offset:0\n" \
"v_mfma_f32_16x16x32_bf16 v[160:163], v[84:87], v[68:71], v[160:163]\n" \
"s_mov_b32 s66, 128\n" \
"v_mfma_f32_16x16x32_bf16 v[176:179], v[84:87], v[76:79], v[176:179]\n" \
"buffer_load_dwordx4 v13, s[40:43], s66 offen offset:1024 lds\n" \
"v_mfma_f32_16x16x32_bf16 v[176:179], v[80:83], v[72:75], v[176:179]\n" \
"s_waitcnt lgkmcnt(4)\n" \
"v_mfma_f32_16x16x32_bf16 v[180:183], v[88:91], v[72:75], v[180:183]\n" \
"ds_read_b64_tr_b16 v[104:105], v7 offset:16384\n" \
"buffer_load_dwordx4 a[228:231], v114, s[56:59], 0 offen offset:64\n" \
"v_mfma_f32_16x16x32_bf16 v[180:183], v[92:95], v[76:79], v[180:183]\n" \
"ds_read_b64_tr_b16 v[106:107], v7 offset:20480\n" \
"s_mov_b32 s67, 15360\n" \
"v_mfma_f32_16x16x32_bf16 v[164:167], v[92:95], v[68:71], v[164:167]\n" \
"ds_read_b64_tr_b16 v[108:109], v7 offset:24576\n" \
"buffer_load_dwordx4 v13, s[40:43], s67 offen offset:2048 lds\n" \
"v_mfma_f32_16x16x32_bf16 v[164:167], v[88:91], v[64:67], v[164:167]\n" \
"ds_read_b64_tr_b16 v[110:111], v7 offset:28672\n" \
"v_mfma_f32_16x16x32_bf16 v[148:151], v[88:91], v[56:59], v[148:151]\n" \
"buffer_load_dwordx4 a[232:235], v114, s[56:59], s79 offen offset:0\n" \
"v_mfma_f32_16x16x32_bf16 v[148:151], v[92:95], v[60:63], v[148:151]\n" \
"s_mov_b32 s68, 14464\n" \
"v_mfma_f32_16x16x32_bf16 v[132:135], v[92:95], v[52:55], v[132:135]\n" \
"buffer_load_dwordx4 v13, s[40:43], s68 offen offset:3072 lds\n" \
"v_mfma_f32_16x16x32_bf16 v[132:135], v[88:91], v[48:51], v[132:135]\n" \
"s_waitcnt vmcnt(15) lgkmcnt(0)\n" \
"s_barrier\n" \
"v_mfma_f32_16x16x32_bf16 v[136:139], v[96:99], v[48:51], v[136:139]\n" \
"ds_read_b64_tr_b16 v[80:81], v8 offset:0\n" \
"ds_read_b64_tr_b16 v[82:83], v8 offset:4096\n" \
"v_mfma_f32_16x16x32_bf16 v[136:139], v[100:103], v[52:55], v[136:139]\n" \
"ds_read_b64_tr_b16 v[84:85], v8 offset:8192\n" \
"ds_read_b64_tr_b16 v[86:87], v8 offset:12288\n" \
"v_mfma_f32_16x16x32_bf16 v[152:155], v[100:103], v[60:63], v[152:155]\n" \
"ds_read_b128 v[16:19], v1 offset:32768\n" \
"ds_read_b128 v[20:23], v1 offset:33792\n" \
"v_mfma_f32_16x16x32_bf16 v[152:155], v[96:99], v[56:59], v[152:155]\n" \
"ds_read_b128 v[24:27], v1 offset:34816\n" \
"ds_read_b128 v[28:31], v1 offset:35840\n" \
"v_mfma_f32_16x16x32_bf16 v[168:171], v[96:99], v[64:67], v[168:171]\n" \
"ds_read_b128 v[32:35], v3 offset:32768\n" \
"v_mfma_f32_16x16x32_bf16 v[168:171], v[100:103], v[68:71], v[168:171]\n" \
"ds_read_b128 v[36:39], v3 offset:33792\n" \
"v_mfma_f32_16x16x32_bf16 v[184:187], v[100:103], v[76:79], v[184:187]\n" \
"ds_read_b128 v[40:43], v3 offset:34816\n" \
"v_mfma_f32_16x16x32_bf16 v[184:187], v[96:99], v[72:75], v[184:187]\n" \
"ds_read_b128 v[44:47], v3 offset:35840\n" \
"v_mfma_f32_16x16x32_bf16 v[188:191], v[104:107], v[72:75], v[188:191]\n" \
"ds_read_b64_tr_b16 v[88:89], v9 offset:0\n" \
"v_mfma_f32_16x16x32_bf16 v[188:191], v[108:111], v[76:79], v[188:191]\n" \
"ds_read_b64_tr_b16 v[90:91], v9 offset:4096\n" \
"v_mfma_f32_16x16x32_bf16 v[172:175], v[108:111], v[68:71], v[172:175]\n" \
"ds_read_b64_tr_b16 v[92:93], v9 offset:8192\n" \
"v_mfma_f32_16x16x32_bf16 v[172:175], v[104:107], v[64:67], v[172:175]\n" \
"ds_read_b64_tr_b16 v[94:95], v9 offset:12288\n" \
"v_mfma_f32_16x16x32_bf16 v[156:159], v[104:107], v[56:59], v[156:159]\n" \
"v_mfma_f32_16x16x32_bf16 v[156:159], v[108:111], v[60:63], v[156:159]\n" \
"buffer_load_dwordx4 a[236:239], v114, s[56:59], s79 offen offset:64\n" \
"v_mfma_f32_16x16x32_bf16 v[140:143], v[108:111], v[52:55], v[140:143]\n" \
"v_mfma_f32_16x16x32_bf16 v[140:143], v[104:107], v[48:51], v[140:143]\n" \
"s_waitcnt lgkmcnt(4)\n" \
"v_mfma_f32_16x16x32_bf16 v[128:131], v[80:83], v[16:19], v[128:131]\n" \
"ds_read_b64_tr_b16 v[96:97], v10 offset:0\n" \
"v_mfma_f32_16x16x32_bf16 v[128:131], v[84:87], v[20:23], v[128:131]\n" \
"ds_read_b64_tr_b16 v[98:99], v10 offset:4096\n" \
"s_add_u32 m0, s70, 16384\n" \
"s_mov_b32 s65, 132096\n" \
"v_mfma_f32_16x16x32_bf16 v[144:147], v[84:87], v[28:31], v[144:147]\n" \
"ds_read_b64_tr_b16 v[100:101], v10 offset:8192\n" \
"buffer_load_dwordx4 v13, s[40:43], s65 offen offset:0 lds\n" \
"v_mfma_f32_16x16x32_bf16 v[144:147], v[80:83], v[24:27], v[144:147]\n" \
"ds_read_b64_tr_b16 v[102:103], v10 offset:12288\n" \
"v_mfma_f32_16x16x32_bf16 v[160:163], v[80:83], v[32:35], v[160:163]\n" \
"v_mfma_f32_16x16x32_bf16 v[160:163], v[84:87], v[36:39], v[160:163]\n" \
"s_mov_b32 s66, 131200\n" \
"v_mfma_f32_16x16x32_bf16 v[176:179], v[84:87], v[44:47], v[176:179]\n" \
"buffer_load_dwordx4 v13, s[40:43], s66 offen offset:1024 lds\n" \
"v_mfma_f32_16x16x32_bf16 v[176:179], v[80:83], v[40:43], v[176:179]\n" \
"s_waitcnt lgkmcnt(4)\n" \
"v_mfma_f32_16x16x32_bf16 v[180:183], v[88:91], v[40:43], v[180:183]\n" \
"ds_read_b64_tr_b16 v[104:105], v11 offset:0\n" \
"v_mfma_f32_16x16x32_bf16 v[180:183], v[92:95], v[44:47], v[180:183]\n" \
"ds_read_b64_tr_b16 v[106:107], v11 offset:4096\n" \
"s_mov_b32 s67, 146432\n" \
"v_mfma_f32_16x16x32_bf16 v[164:167], v[92:95], v[36:39], v[164:167]\n" \
"ds_read_b64_tr_b16 v[108:109], v11 offset:8192\n" \
"buffer_load_dwordx4 v13, s[40:43], s67 offen offset:2048 lds\n" \
"v_mfma_f32_16x16x32_bf16 v[164:167], v[88:91], v[32:35], v[164:167]\n" \
"ds_read_b64_tr_b16 v[110:111], v11 offset:12288\n" \
"v_mfma_f32_16x16x32_bf16 v[148:151], v[88:91], v[24:27], v[148:151]\n" \
"v_mfma_f32_16x16x32_bf16 v[148:151], v[92:95], v[28:31], v[148:151]\n" \
"s_mov_b32 s68, 145536\n" \
"v_mfma_f32_16x16x32_bf16 v[132:135], v[92:95], v[20:23], v[132:135]\n" \
"buffer_load_dwordx4 v13, s[40:43], s68 offen offset:3072 lds\n" \
"v_mfma_f32_16x16x32_bf16 v[132:135], v[88:91], v[16:19], v[132:135]\n" \
"s_waitcnt vmcnt(12) lgkmcnt(0)\n" \
"s_cmp_lg_u32 s62, 0\n" \
"s_cbranch_scc1 L_w0sh_%=\n" \
"v_cmp_eq_u64 vcc, s[28:29], v[112:113]\n" \
"s_nop 1\n" \
"s_cmp_eq_u64 vcc, -1\n" \
"s_cbranch_scc1 L_okh_%=\n" \
"s_mov_b32 s76, 0x40000\n" \
"L_sph_%=:\n" \
"buffer_load_dwordx2 v[112:113], v124, s[80:83], s60 offen offset:256 sc1\n" \
"s_waitcnt vmcnt(0)\n" \
"v_cmp_eq_u64 vcc, s[28:29], v[112:113]\n" \
"s_nop 1\n" \
"s_cmp_eq_u64 vcc, -1\n" \
"s_cbranch_scc1 L_okh_%=\n" \
"s_sleep 1\n" \
"s_sub_u32 s76, s76, 1\n" \
"s_cmp_lg_u32 s76, 0\n" \
"s_cbranch_scc1 L_sph_%=\n" \
"s_mov_b32 s77, 1\n" \
"L_okh_%=:\n" \
"buffer_store_dwordx2 v[122:123], v124, s[80:83], s60 offen offset:256 sc1\n" \
"L_w0sh_%=:\n" \
"s_barrier\n" \
"s_cmp_lg_u32 s62, 0\n" \
"s_cbranch_scc1 L_w0pq_%=\n" \
"buffer_load_dwordx2 v[112:113], v116, s[80:83], s60 offen offset:512 sc1\n" \
"L_w0pq_%=:\n" \
"s_lshl_b32 s74, s62, 10\n" \
"v_add_u32 v124, s74, v115\n" \
"v_mfma_f32_16x16x32_bf16 v[136:139], v[96:99], v[16:19], v[136:139]\n" \
"ds_read_b64_tr_b16 v[80:81], v8 offset:16384\n" \
"ds_read_b64_tr_b16 v[82:83], v8 offset:20480\n" \
"s_add_u32 m0, s70, 32768\n" \
"s_mov_b32 s64, 0\n" \
"v_mfma_f32_16x16x32_bf16 v[136:139], v[100:103], v[20:23], v[136:139]\n" \
"ds_read_b64_tr_b16 v[84:85], v8 offset:24576\n" \
"ds_read_b64_tr_b16 v[86:87], v8 offset:28672\n" \
"buffer_load_dwordx4 v12, s[36:39], s64 offen offset:0 sc1 lds\n" \
"v_mfma_f32_16x16x32_bf16 v[152:155], v[100:103], v[28:31], v[152:155]\n" \
"ds_read_b128 v[48:51], v1 offset:49152\n" \
"ds_read_b128 v[52:55], v1 offset:50176\n" \
"ds_read_b64_tr_b16 a[96:97], v196 offset:0\n" \
"v_mfma_f32_16x16x32_bf16 v[152:155], v[96:99], v[24:27], v[152:155]\n" \
"ds_read_b128 v[56:59], v1 offset:51200\n" \
"ds_read_b128 v[60:63], v1 offset:52224\n" \
"buffer_load_dwordx4 v12, s[36:39], s64 offen offset:1024 sc1 lds\n" \
"v_mfma_f32_16x16x32_bf16 v[168:171], v[96:99], v[32:35], v[168:171]\n" \
"ds_read_b128 v[64:67], v3 offset:49152\n" \
"ds_read_b64_tr_b16 a[98:99], v196 offset:4096\n" \
"ds_read_b64_tr_b16 a[100:101], v196 offset:8192\n" \
"v_mfma_f32_16x16x32_bf16 v[168:171], v[100:103], v[36:39], v[168:171]\n" \
"ds_read_b128 v[68:71], v3 offset:50176\n" \
"buffer_load_dwordx4 v12, s[36:39], s64 offen offset:2048 sc1 lds\n" \
"ds_read_b64_tr_b16 a[102:103], v196 offset:12288\n" \
"v_mfma_f32_16x16x32_bf16 v[184:187], v[100:103], v[44:47], v[184:187]\n" \
"ds_read_b128 v[72:75], v3 offset:51200\n" \
"ds_read_b64_tr_b16 a[104:105], v197 offset:0\n" \
"ds_read_b64_tr_b16 a[106:107], v197 offset:4096\n" \
"v_mfma_f32_16x16x32_bf16 v[184:187], v[96:99], v[40:43], v[184:187]\n" \
"ds_read_b128 v[76:79], v3 offset:52224\n" \
"buffer_load_dwordx4 v12, s[36:39], s64 offen offset:3072 sc1 lds\n" \
"ds_read_b64_tr_b16 a[108:109], v197 offset:8192\n" \
"v_mfma_f32_16x16x32_bf16 v[188:191], v[104:107], v[40:43], v[188:191]\n" \
"ds_read_b64_tr_b16 v[88:89], v9 offset:16384\n" \
"s_add_u32 m0, s70, 49152\n" \
"s_mov_b32 s64, 16384\n" \
"v_mfma_f32_16x16x32_bf16 v[188:191], v[108:111], v[44:47], v[188:191]\n" \
"ds_read_b64_tr_b16 v[90:91], v9 offset:20480\n" \
"buffer_load_dwordx4 v12, s[36:39], s64 offen offset:0 sc1 lds\n" \
"ds_read_b64_tr_b16 a[110:111], v197 offset:12288\n" \
"v_mfma_f32_16x16x32_bf16 v[172:175], v[108:111], v[36:39], v[172:175]\n" \
"ds_read_b64_tr_b16 v[92:93], v9 offset:24576\n" \
"ds_read_b64_tr_b16 a[112:113], v198 offset:0\n" \
"ds_read_b64_tr_b16 a[114:115], v198 offset:4096\n" \
"v_mfma_f32_16x16x32_bf16 v[172:175], v[104:107], v[32:35], v[172:175]\n" \
"ds_read_b64_tr_b16 v[94:95], v9 offset:28672\n" \
"buffer_load_dwordx4 v12, s[36:39], s64 offen offset:1024 sc1 lds\n" \
"ds_read_b64_tr_b16 a[116:117], v198 offset:8192\n" \
"v_mfma_f32_16x16x32_bf16 v[156:159], v[104:107], v[24:27], v[156:159]\n" \
"ds_read_b64_tr_b16 a[118:119], v198 offset:12288\n" \
"ds_read_b64_tr_b16 a[120:121], v199 offset:0\n" \
"ds_read_b64_tr_b16 a[122:123], v199 offset:4096\n" \
"v_mfma_f32_16x16x32_bf16 v[156:159], v[108:111], v[28:31], v[156:159]\n" \
"buffer_load_dwordx4 v12, s[36:39], s64 offen offset:2048 sc1 lds\n" \
"ds_read_b64_tr_b16 a[124:125], v199 offset:8192\n" \
"ds_read_b64_tr_b16 a[126:127], v199 offset:12288\n" \
"v_mfma_f32_16x16x32_bf16 v[140:143], v[108:111], v[20:23], v[140:143]\n" \
"v_mfma_f32_16x16x32_bf16 v[140:143], v[104:107], v[16:19], v[140:143]\n" \
"buffer_load_dwordx4 v12, s[36:39], s64 offen offset:3072 sc1 lds\n" \
"s_waitcnt lgkmcnt(14)\n" \
"v_mfma_f32_16x16x32_bf16 v[128:131], v[80:83], v[48:51], v[128:131]\n" \
"ds_read_b64_tr_b16 v[96:97], v10 offset:16384\n" \
"v_mfma_f32_16x16x32_bf16 v[128:131], v[84:87], v[52:55], v[128:131]\n" \
"ds_read_b64_tr_b16 v[98:99], v10 offset:20480\n" \
"s_add_u32 m0, s70, 65536\n" \
"s_mov_b32 s65, 263168\n" \
"v_mfma_f32_16x16x32_bf16 v[144:147], v[84:87], v[60:63], v[144:147]\n" \
"ds_read_b64_tr_b16 v[100:101], v10 offset:24576\n" \
"buffer_load_dwordx4 v13, s[40:43], s65 offen offset:0 lds\n" \
"v_mfma_f32_16x16x32_bf16 v[144:147], v[80:83], v[56:59], v[144:147]\n" \
"ds_read_b64_tr_b16 v[102:103], v10 offset:28672\n" \
"v_mfma_f32_16x16x32_bf16 v[160:163], v[80:83], v[64:67], v[160:163]\n" \
"v_mfma_f32_16x16x32_bf16 v[160:163], v[84:87], v[68:71], v[160:163]\n" \
"s_mov_b32 s66, 262272\n" \
"v_mfma_f32_16x16x32_bf16 v[176:179], v[84:87], v[76:79], v[176:179]\n" \
"buffer_load_dwordx4 v13, s[40:43], s66 offen offset:1024 lds\n" \
"v_mfma_f32_16x16x32_bf16 v[176:179], v[80:83], v[72:75], v[176:179]\n" \
"s_waitcnt lgkmcnt(10)\n" \
"v_mfma_f32_16x16x32_bf16 v[180:183], v[88:91], v[72:75], v[180:183]\n" \
"ds_read_b64_tr_b16 v[104:105], v11 offset:16384\n" \
"v_mfma_f32_16x16x32_bf16 v[180:183], v[92:95], v[76:79], v[180:183]\n" \
"ds_read_b64_tr_b16 v[106:107], v11 offset:20480\n" \
"s_mov_b32 s67, 277504\n" \
"v_mfma_f32_16x16x32_bf16 v[164:167], v[92:95], v[68:71], v[164:167]\n" \
"ds_read_b64_tr_b16 v[108:109], v11 offset:24576\n" \
"buffer_load_dwordx4 v13, s[40:43], s67 offen offset:2048 lds\n" \
"v_mfma_f32_16x16x32_bf16 v[164:167], v[88:91], v[64:67], v[164:167]\n" \
"ds_read_b64_tr_b16 v[110:111], v11 offset:28672\n" \
"v_mfma_f32_16x16x32_bf16 v[148:151], v[88:91], v[56:59], v[148:151]\n" \
"v_mfma_f32_16x16x32_bf16 v[148:151], v[92:95], v[60:63], v[148:151]\n" \
"s_mov_b32 s68, 276608\n" \
"v_mfma_f32_16x16x32_bf16 v[132:135], v[92:95], v[52:55], v[132:135]\n" \
"buffer_load_dwordx4 v13, s[40:43], s68 offen offset:3072 lds\n" \
"v_mfma_f32_16x16x32_bf16 v[132:135], v[88:91], v[48:51], v[132:135]\n" \
"s_waitcnt vmcnt(8) lgkmcnt(0)\n" \
"s_barrier\n" \
"v_mfma_f32_16x16x32_bf16 v[136:139], v[96:99], v[48:51], v[136:139]\n" \
"ds_read_b64_tr_b16 v[80:81], v4 offset:0\n" \
"ds_read_b64_tr_b16 v[82:83], v4 offset:4096\n" \
"s_add_u32 m0, s70, 98304\n" \
"s_mov_b32 s64, 32768\n" \
"v_mfma_f32_16x16x32_bf16 v[136:139], v[100:103], v[52:55], v[136:139]\n" \
"ds_read_b64_tr_b16 v[84:85], v4 offset:8192\n" \
"ds_read_b64_tr_b16 v[86:87], v4 offset:12288\n" \
"buffer_load_dwordx4 v12, s[36:39], s64 offen offset:0 sc1 lds\n" \
"v_mfma_f32_16x16x32_bf16 v[152:155], v[100:103], v[60:63], v[152:155]\n" \
"ds_read_b128 v[16:19], v0 offset:32768\n" \
"ds_read_b128 v[20:23], v0 offset:33792\n" \
"v_mfma_f32_16x16x32_bf16 v[152:155], v[96:99], v[56:59], v[152:155]\n" \
"ds_read_b128 v[24:27], v0 offset:34816\n" \
"ds_read_b128 v[28:31], v0 offset:35840\n" \
"buffer_load_dwordx4 v12, s[36:39], s64 offen offset:1024 sc1 lds\n" \
"v_mfma_f32_16x16x32_bf16 v[168:171], v[96:99], v[64:67], v[168:171]\n" \
"ds_read_b128 v[32:35], v2 offset:32768\n" \
"v_mfma_f32_16x16x32_bf16 v[168:171], v[100:103], v[68:71], v[168:171]\n" \
"ds_read_b128 v[36:39], v2 offset:33792\n" \
"buffer_load_dwordx4 v12, s[36:39], s64 offen offset:2048 sc1 lds\n" \
"v_mfma_f32_16x16x32_bf16 v[184:187], v[100:103], v[76:79], v[184:187]\n" \
"ds_read_b128 v[40:43], v2 offset:34816\n" \
"v_mfma_f32_16x16x32_bf16 v[184:187], v[96:99], v[72:75], v[184:187]\n" \
"ds_read_b128 v[44:47], v2 offset:35840\n" \
"buffer_load_dwordx4 v12, s[36:39], s64 offen offset:3072 sc1 lds\n" \
"v_mfma_f32_16x16x32_bf16 v[188:191], v[104:107], v[72:75], v[188:191]\n" \
"ds_read_b64_tr_b16 v[88:89], v5 offset:0\n" \
"s_add_u32 m0, s70, 114688\n" \
"s_mov_b32 s64, 49152\n" \
"v_mfma_f32_16x16x32_bf16 v[188:191], v[108:111], v[76:79], v[188:191]\n" \
"ds_read_b64_tr_b16 v[90:91], v5 offset:4096\n" \
"buffer_load_dwordx4 v12, s[36:39], s64 offen offset:0 sc1 lds\n" \
"v_mfma_f32_16x16x32_bf16 v[172:175], v[108:111], v[68:71], v[172:175]\n" \
"ds_read_b64_tr_b16 v[92:93], v5 offset:8192\n" \
"v_mfma_f32_16x16x32_bf16 v[172:175], v[104:107], v[64:67], v[172:175]\n" \
"ds_read_b64_tr_b16 v[94:95], v5 offset:12288\n" \
"buffer_load_dwordx4 v12, s[36:39], s64 offen offset:1024 sc1 lds\n" \
"v_mfma_f32_16x16x32_bf16 v[156:159], v[104:107], v[56:59], v[156:159]\n" \
"v_mfma_f32_16x16x32_bf16 v[156:159], v[108:111], v[60:63], v[156:159]\n" \
"buffer_load_dwordx4 v12, s[36:39], s64 offen offset:2048 sc1 lds\n" \
"v_mfma_f32_16x16x32_bf16 v[140:143], v[108:111], v[52:55], v[140:143]\n" \
"v_mfma_f32_16x16x32_bf16 v[140:143], v[104:107], v[48:51], v[140:143]\n" \
"buffer_load_dwordx4 v12, s[36:39], s64 offen offset:3072 sc1 lds\n" \
"s_waitcnt lgkmcnt(4)\n" \
"v_mfma_f32_16x16x32_bf16 v[128:131], v[80:83], v[16:19], v[128:131]\n" \
"ds_read_b64_tr_b16 v[96:97], v6 offset:0\n" \
"v_mfma_f32_16x16x32_bf16 v[128:131], v[84:87], v[20:23], v[128:131]\n" \
"ds_read_b64_tr_b16 v[98:99], v6 offset:4096\n" \
"s_add_u32 m0, s70, 81920\n" \
"s_mov_b32 s65, 394240\n" \
"v_mfma_f32_16x16x32_bf16 v[144:147], v[84:87], v[28:31], v[144:147]\n" \
"ds_read_b64_tr_b16 v[100:101], v6 offset:8192\n" \
"buffer_load_dwordx4 v13, s[40:43], s65 offen offset:0 lds\n" \
"v_mfma_f32_16x16x32_bf16 v[144:147], v[80:83], v[24:27], v[144:147]\n" \
"ds_read_b64_tr_b16 v[102:103], v6 offset:12288\n" \
"v_mfma_f32_16x16x32_bf16 v[160:163], v[80:83], v[32:35], v[160:163]\n" \
"v_mfma_f32_16x16x32_bf16 v[160:163], v[84:87], v[36:39], v[160:163]\n" \
"s_mov_b32 s66, 393344\n" \
"v_mfma_f32_16x16x32_bf16 v[176:179], v[84:87], v[44:47], v[176:179]\n" \
"buffer_load_dwordx4 v13, s[40:43], s66 offen offset:1024 lds\n" \
"v_mfma_f32_16x16x32_bf16 v[176:179], v[80:83], v[40:43], v[176:179]\n" \
"s_waitcnt lgkmcnt(4)\n" \
"v_mfma_f32_16x16x32_bf16 v[180:183], v[88:91], v[40:43], v[180:183]\n" \
"ds_read_b64_tr_b16 v[104:105], v7 offset:0\n" \
"v_mfma_f32_16x16x32_bf16 v[180:183], v[92:95], v[44:47], v[180:183]\n" \
"ds_read_b64_tr_b16 v[106:107], v7 offset:4096\n" \
"s_mov_b32 s67, 408576\n" \
"v_mfma_f32_16x16x32_bf16 v[164:167], v[92:95], v[36:39], v[164:167]\n" \
"ds_read_b64_tr_b16 v[108:109], v7 offset:8192\n" \
"buffer_load_dwordx4 v13, s[40:43], s67 offen offset:2048 lds\n" \
"v_mfma_f32_16x16x32_bf16 v[164:167], v[88:91], v[32:35], v[164:167]\n" \
"ds_read_b64_tr_b16 v[110:111], v7 offset:12288\n" \
"v_mfma_f32_16x16x32_bf16 v[148:151], v[88:91], v[24:27], v[148:151]\n" \
"v_mfma_f32_16x16x32_bf16 v[148:151], v[92:95], v[28:31], v[148:151]\n" \
"s_mov_b32 s68, 407680\n" \
"v_mfma_f32_16x16x32_bf16 v[132:135], v[92:95], v[20:23], v[132:135]\n" \
"buffer_load_dwordx4 v13, s[40:43], s68 offen offset:3072 lds\n" \
"v_mfma_f32_16x16x32_bf16 v[132:135], v[88:91], v[16:19], v[132:135]\n" \
"s_waitcnt vmcnt(16) lgkmcnt(0)\n" \
"s_cmp_lg_u32 s62, 0\n" \
"s_cbranch_scc1 L_w0sq_%=\n" \
"v_cmp_eq_u64 vcc, s[28:29], v[112:113]\n" \
"s_nop 1\n" \
"s_cmp_eq_u64 vcc, -1\n" \
"s_cbranch_scc1 L_okq_%=\n" \
"s_mov_b32 s76, 0x40000\n" \
"L_spq_%=:\n" \
"buffer_load_dwordx2 v[112:113], v116, s[80:83], s60 offen offset:512 sc1\n" \
"s_waitcnt vmcnt(0)\n" \
"v_cmp_eq_u64 vcc, s[28:29], v[112:113]\n" \
"s_nop 1\n" \
"s_cmp_eq_u64 vcc, -1\n" \
"s_cbranch_scc1 L_okq_%=\n" \
"s_sleep 1\n" \
"s_sub_u32 s76, s76, 1\n" \
"s_cmp_lg_u32 s76, 0\n" \
"s_cbranch_scc1 L_spq_%=\n" \
"s_mov_b32 s77, 1\n" \
"L_okq_%=:\n" \
"buffer_store_dwordx2 v[122:123], v116, s[80:83], s60 offen offset:512 sc1\n" \
"L_w0sq_%=:\n" \
"s_barrier\n" \
"v_mfma_f32_16x16x32_bf16 v[136:139], v[96:99], v[16:19], v[136:139]\n" \
"ds_read_b64_tr_b16 v[80:81], v4 offset:16384\n" \
"ds_read_b64_tr_b16 v[82:83], v4 offset:20480\n" \
"v_mfma_f32_16x16x32_bf16 v[136:139], v[100:103], v[20:23], v[136:139]\n" \
"ds_read_b64_tr_b16 v[84:85], v4 offset:24576\n" \
"ds_read_b64_tr_b16 v[86:87], v4 offset:28672\n" \
"v_mfma_f32_16x16x32_bf16 v[152:155], v[100:103], v[28:31], v[152:155]\n" \
"ds_read_b128 v[48:51], v0 offset:49152\n" \
"ds_read_b128 v[52:55], v0 offset:50176\n" \
"v_mfma_f32_16x16x32_bf16 v[152:155], v[96:99], v[24:27], v[152:155]\n" \
"ds_read_b128 v[56:59], v0 offset:51200\n" \
"ds_read_b128 v[60:63], v0 offset:52224\n" \
"v_mfma_f32_16x16x32_bf16 v[168:171], v[96:99], v[32:35], v[168:171]\n" \
"ds_read_b128 v[64:67], v2 offset:49152\n" \
"s_add_u32 m0, s70, 32768\n" \
"s_mov_b32 s64, 81920\n" \
"v_mfma_f32_16x16x32_bf16 v[168:171], v[100:103], v[36:39], v[168:171]\n" \
"ds_read_b128 v[68:71], v2 offset:50176\n" \
"buffer_load_dwordx4 v12, s[36:39], s64 offen offset:0 sc1 lds\n" \
"v_mfma_f32_16x16x32_bf16 v[184:187], v[100:103], v[44:47], v[184:187]\n" \
"ds_read_b128 v[72:75], v2 offset:51200\n" \
"v_mfma_f32_16x16x32_bf16 v[184:187], v[96:99], v[40:43], v[184:187]\n" \
"ds_read_b128 v[76:79], v2 offset:52224\n" \
"v_mfma_f32_16x16x32_bf16 v[188:191], v[104:107], v[40:43], v[188:191]\n" \
"ds_read_b64_tr_b16 v[88:89], v5 offset:16384\n" \
"buffer_load_dwordx4 v12, s[36:39], s64 offen offset:1024 sc1 lds\n" \
"v_mfma_f32_16x16x32_bf16 v[188:191], v[108:111], v[44:47], v[188:191]\n" \
"ds_read_b64_tr_b16 v[90:91], v5 offset:20480\n" \
"v_mfma_f32_16x16x32_bf16 v[172:175], v[108:111], v[36:39], v[172:175]\n" \
"ds_read_b64_tr_b16 v[92:93], v5 offset:24576\n" \
"v_mfma_f32_16x16x32_bf16 v[172:175], v[104:107], v[32:35], v[172:175]\n" \
"ds_read_b64_tr_b16 v[94:95], v5 offset:28672\n" \
"buffer_load_dwordx4 v12, s[36:39], s64 offen offset:2048 sc1 lds\n" \
"v_mfma_f32_16x16x32_bf16 v[156:159], v[104:107], v[24:27], v[156:159]\n" \
"v_lshrrev_b32 v116, s61, v15\n" \
"v_mfma_f32_16x16x32_bf16 v[156:159], v[108:111], v[28:31], v[156:159]\n" \
"v_and_b32 v116, s63, v116\n" \
"v_mfma_f32_16x16x32_bf16 v[140:143], v[108:111], v[20:23], v[140:143]\n" \
"buffer_load_dwordx4 v12, s[36:39], s64 offen offset:3072 sc1 lds\n" \
"v_add_u32 v116, s92, v116\n" \
"v_mfma_f32_16x16x32_bf16 v[140:143], v[104:107], v[16:19], v[140:143]\n" \
"s_waitcnt lgkmcnt(4)\n" \
"v_mfma_f32_16x16x32_bf16 v[128:131], v[80:83], v[48:51], v[128:131]\n" \
"ds_read_b64_tr_b16 v[96:97], v6 offset:16384\n" \
"v_mfma_f32_16x16x32_bf16 v[128:131], v[84:87], v[52:55], v[128:131]\n" \
"ds_read_b64_tr_b16 v[98:99], v6 offset:20480\n" \
"s_add_u32 m0, s70, 0\n" \
"s_mov_b32 s65, 656384\n" \
"v_mfma_f32_16x16x32_bf16 v[144:147], v[84:87], v[60:63], v[144:147]\n" \
"ds_read_b64_tr_b16 v[100:101], v6 offset:24576\n" \
"buffer_load_dwordx4 v13, s[40:43], s65 offen offset:0 lds\n" \
"v_mfma_f32_16x16x32_bf16 v[144:147], v[80:83], v[56:59], v[144:147]\n" \
"ds_read_b64_tr_b16 v[102:103], v6 offset:28672\n" \
"s_mov_b32 s93, 0\n" \
"v_mfma_f32_16x16x32_bf16 v[160:163], v[80:83], v[64:67], v[160:163]\n" \
"buffer_load_dwordx4 v[192:195], v116, s[84:87], s93 offen offset:0 sc1\n" \
"v_mfma_f32_16x16x32_bf16 v[160:163], v[84:87], v[68:71], v[160:163]\n" \
"s_mov_b32 s66, 655488\n" \
"v_mfma_f32_16x16x32_bf16 v[176:179], v[84:87], v[76:79], v[176:179]\n" \
"buffer_load_dwordx4 v13, s[40:43], s66 offen offset:1024 lds\n" \
"v_mfma_f32_16x16x32_bf16 v[176:179], v[80:83], v[72:75], v[176:179]\n" \
"s_waitcnt lgkmcnt(4)\n" \
"v_mfma_f32_16x16x32_bf16 v[180:183], v[88:91], v[72:75], v[180:183]\n" \
"ds_read_b64_tr_b16 v[104:105], v7 offset:16384\n" \
"v_mfma_f32_16x16x32_bf16 v[180:183], v[92:95], v[76:79], v[180:183]\n" \
"ds_read_b64_tr_b16 v[106:107], v7 offset:20480\n" \
"s_mov_b32 s67, 670720\n" \
"v_mfma_f32_16x16x32_bf16 v[164:167], v[92:95], v[68:71], v[164:167]\n" \
"ds_read_b64_tr_b16 v[108:109], v7 offset:24576\n" \
"buffer_load_dwordx4 v13, s[40:43], s67 offen offset:2048 lds\n" \
"v_mfma_f32_16x16x32_bf16 v[164:167], v[88:91], v[64:67], v[164:167]\n" \
"ds_read_b64_tr_b16 v[110:111], v7 offset:28672\n" \
"s_mov_b32 s93, 0\n" \
"v_mfma_f32_16x16x32_bf16 v[148:151], v[88:91], v[56:59], v[148:151]\n" \
"buffer_load_dwordx4 v[196:199], v116, s[84:87], s93 offen offset:1024 sc1\n" \
"v_mfma_f32_16x16x32_bf16 v[148:151], v[92:95], v[60:63], v[148:151]\n" \
"s_mov_b32 s68, 669824\n" \
"v_mfma_f32_16x16x32_bf16 v[132:135], v[92:95], v[52:55], v[132:135]\n" \
"buffer_load_dwordx4 v13, s[40:43], s68 offen offset:3072 lds\n" \
"v_mfma_f32_16x16x32_bf16 v[132:135], v[88:91], v[48:51], v[132:135]\n" \
"s_waitcnt vmcnt(18) lgkmcnt(0)\n" \
"s_barrier\n" \
"v_mfma_f32_16x16x32_bf16 v[136:139], v[96:99], v[48:51], v[136:139]\n" \
"ds_read_b64_tr_b16 v[80:81], v8 offset:0\n" \
"ds_read_b64_tr_b16 v[82:83], v8 offset:4096\n" \
"v_mfma_f32_16x16x32_bf16 v[136:139], v[100:103], v[52:55], v[136:139]\n" \
"ds_read_b64_tr_b16 v[84:85], v8 offset:8192\n" \
"ds_read_b64_tr_b16 v[86:87], v8 offset:12288\n" \
"v_mfma_f32_16x16x32_bf16 v[152:155], v[100:103], v[60:63], v[152:155]\n" \
"ds_read_b128 v[16:19], v1 offset:32768\n" \
"ds_read_b128 v[20:23], v1 offset:33792\n" \
"s_mov_b32 s93, 196608\n" \
"v_mfma_f32_16x16x32_bf16 v[152:155], v[96:99], v[56:59], v[152:155]\n" \
"ds_read_b128 v[24:27], v1 offset:34816\n" \
"ds_read_b128 v[28:31], v1 offset:35840\n" \
"buffer_load_dwordx4 v[200:203], v116, s[84:87], s93 offen offset:0 sc1\n" \
"v_mfma_f32_16x16x32_bf16 v[168:171], v[96:99], v[64:67], v[168:171]\n" \
"ds_read_b128 v[32:35], v3 offset:32768\n" \
"s_add_u32 m0, s70, 49152\n" \
"s_mov_b32 s64, 98304\n" \
"v_mfma_f32_16x16x32_bf16 v[168:171], v[100:103], v[68:71], v[168:171]\n" \
"ds_read_b128 v[36:39], v3 offset:33792\n" \
"buffer_load_dwordx4 v12, s[36:39], s64 offen offset:0 sc1 lds\n" \
"v_mfma_f32_16x16x32_bf16 v[184:187], v[100:103], v[76:79], v[184:187]\n" \
"ds_read_b128 v[40:43], v3 offset:34816\n" \
"v_mfma_f32_16x16x32_bf16 v[184:187], v[96:99], v[72:75], v[184:187]\n" \
"ds_read_b128 v[44:47], v3 offset:35840\n" \
"v_mfma_f32_16x16x32_bf16 v[188:191], v[104:107], v[72:75], v[188:191]\n" \
"ds_read_b64_tr_b16 v[88:89], v9 offset:0\n" \
"buffer_load_dwordx4 v12, s[36:39], s64 offen offset:1024 sc1 lds\n" \
"v_mfma_f32_16x16x32_bf16 v[188:191], v[108:111], v[76:79], v[188:191]\n" \
"ds_read_b64_tr_b16 v[90:91], v9 offset:4096\n" \
"s_mov_b32 s93, 196608\n" \
"v_mfma_f32_16x16x32_bf16 v[172:175], v[108:111], v[68:71], v[172:175]\n" \
"ds_read_b64_tr_b16 v[92:93], v9 offset:8192\n" \
"buffer_load_dwordx4 v[204:207], v116, s[84:87], s93 offen offset:1024 sc1\n" \
"v_mfma_f32_16x16x32_bf16 v[172:175], v[104:107], v[64:67], v[172:175]\n" \
"ds_read_b64_tr_b16 v[94:95], v9 offset:12288\n" \
"buffer_load_dwordx4 v12, s[36:39], s64 offen offset:2048 sc1 lds\n" \
"v_mfma_f32_16x16x32_bf16 v[156:159], v[104:107], v[56:59], v[156:159]\n" \
"v_mfma_f32_16x16x32_bf16 v[156:159], v[108:111], v[60:63], v[156:159]\n" \
"v_mfma_f32_16x16x32_bf16 v[140:143], v[108:111], v[52:55], v[140:143]\n" \
"buffer_load_dwordx4 v12, s[36:39], s64 offen offset:3072 sc1 lds\n" \
"v_mfma_f32_16x16x32_bf16 v[140:143], v[104:107], v[48:51], v[140:143]\n" \
"s_waitcnt lgkmcnt(4)\n" \
"v_mfma_f32_16x16x32_bf16 v[128:131], v[80:83], v[16:19], v[128:131]\n" \
"ds_read_b64_tr_b16 v[96:97], v10 offset:0\n" \
"v_mfma_f32_16x16x32_bf16 v[128:131], v[84:87], v[20:23], v[128:131]\n" \
"ds_read_b64_tr_b16 v[98:99], v10 offset:4096\n" \
"s_add_u32 m0, s70, 16384\n" \
"s_mov_b32 s65, 787456\n" \
"v_mfma_f32_16x16x32_bf16 v[144:147], v[84:87], v[28:31], v[144:147]\n" \
"ds_read_b64_tr_b16 v[100:101], v10 offset:8192\n" \
"buffer_load_dwordx4 v13, s[40:43], s65 offen offset:0 lds\n" \
"v_mfma_f32_16x16x32_bf16 v[144:147], v[80:83], v[24:27], v[144:147]\n" \
"ds_read_b64_tr_b16 v[102:103], v10 offset:12288\n" \
"s_mov_b32 s93, 393216\n" \
"v_mfma_f32_16x16x32_bf16 v[160:163], v[80:83], v[32:35], v[160:163]\n" \
"buffer_load_dwordx4 v[208:211], v116, s[84:87], s93 offen offset:0 sc1\n" \
"v_mfma_f32_16x16x32_bf16 v[160:163], v[84:87], v[36:39], v[160:163]\n" \
"s_mov_b32 s66, 786560\n" \
"v_mfma_f32_16x16x32_bf16 v[176:179], v[84:87], v[44:47], v[176:179]\n" \
"buffer_load_dwordx4 v13, s[40:43], s66 offen offset:1024 lds\n" \
"v_mfma_f32_16x16x32_bf16 v[176:179], v[80:83], v[40:43], v[176:179]\n" \
"s_waitcnt lgkmcnt(4)\n" \
"v_mfma_f32_16x16x32_bf16 v[180:183], v[88:91], v[40:43], v[180:183]\n" \
"ds_read_b64_tr_b16 v[104:105], v11 offset:0\n" \
"v_mfma_f32_16x16x32_bf16 v[180:183], v[92:95], v[44:47], v[180:183]\n" \
"ds_read_b64_tr_b16 v[106:107], v11 offset:4096\n" \
"s_mov_b32 s67, 801792\n" \
"v_mfma_f32_16x16x32_bf16 v[164:167], v[92:95], v[36:39], v[164:167]\n" \
"ds_read_b64_tr_b16 v[108:109], v11 offset:8192\n" \
"buffer_load_dwordx4 v13, s[40:43], s67 offen offset:2048 lds\n" \
"v_mfma_f32_16x16x32_bf16 v[164:167], v[88:91], v[32:35], v[164:167]\n" \
"ds_read_b64_tr_b16 v[110:111], v11 offset:12288\n" \
"s_mov_b32 s93, 393216\n" \
"v_mfma_f32_16x16x32_bf16 v[148:151], v[88:91], v[24:27], v[148:151]\n" \
"buffer_load_dwordx4 v[212:215], v116, s[84:87], s93 offen offset:1024 sc1\n" \
"v_mfma_f32_16x16x32_bf16 v[148:151], v[92:95], v[28:31], v[148:151]\n" \
"s_mov_b32 s68, 800896\n" \
"v_mfma_f32_16x16x32_bf16 v[132:135], v[92:95], v[20:23], v[132:135]\n" \
"buffer_load_dwordx4 v13, s[40:43], s68 offen offset:3072 lds\n" \
"v_mfma_f32_16x16x32_bf16 v[132:135], v[88:91], v[16:19], v[132:135]\n" \
"s_waitcnt vmcnt(22) lgkmcnt(0)\n" \
"s_barrier\n" \
"v_mfma_f32_16x16x32_bf16 v[136:139], v[96:99], v[16:19], v[136:139]\n" \
"ds_read_b64_tr_b16 v[80:81], v8 offset:16384\n" \
"ds_read_b64_tr_b16 v[82:83], v8 offset:20480\n" \
"v_mfma_f32_16x16x32_bf16 v[136:139], v[100:103], v[20:23], v[136:139]\n" \
"ds_read_b64_tr_b16 v[84:85], v8 offset:24576\n" \
"ds_read_b64_tr_b16 v[86:87], v8 offset:28672\n" \
"v_mfma_f32_16x16x32_bf16 v[152:155], v[100:103], v[28:31], v[152:155]\n" \
"ds_read_b128 v[48:51], v1 offset:49152\n" \
"ds_read_b128 v[52:55], v1 offset:50176\n" \
"s_mov_b32 s93, 589824\n" \
"v_mfma_f32_16x16x32_bf16 v[152:155], v[96:99], v[24:27], v[152:155]\n" \
"ds_read_b128 v[56:59], v1 offset:51200\n" \
"ds_read_b128 v[60:63], v1 offset:52224\n" \
"buffer_load_dwordx4 v[216:219], v116, s[84:87], s93 offen offset:0 sc1\n" \
"v_mfma_f32_16x16x32_bf16 v[168:171], v[96:99], v[32:35], v[168:171]\n" \
"ds_read_b128 v[64:67], v3 offset:49152\n" \
"s_add_u32 m0, s70, 98304\n" \
"s_mov_b32 s64, 114688\n" \
"v_mfma_f32_16x16x32_bf16 v[168:171], v[100:103], v[36:39], v[168:171]\n" \
"ds_read_b128 v[68:71], v3 offset:50176\n" \
"buffer_load_dwordx4 v12, s[36:39], s64 offen offset:0 sc1 lds\n" \
"v_mfma_f32_16x16x32_bf16 v[184:187], v[100:103], v[44:47], v[184:187]\n" \
"ds_read_b128 v[72:75], v3 offset:51200\n" \
"v_mfma_f32_16x16x32_bf16 v[184:187], v[96:99], v[40:43], v[184:187]\n" \
"ds_read_b128 v[76:79], v3 offset:52224\n" \
"v_mfma_f32_16x16x32_bf16 v[188:191], v[104:107], v[40:43], v[188:191]\n" \
"ds_read_b64_tr_b16 v[88:89], v9 offset:16384\n" \
"buffer_load_dwordx4 v12, s[36:39], s64 offen offset:1024 sc1 lds\n" \
"v_mfma_f32_16x16x32_bf16 v[188:191], v[108:111], v[44:47], v[188:191]\n" \
"ds_read_b64_tr_b16 v[90:91], v9 offset:20480\n" \
"s_mov_b32 s93, 589824\n" \
"v_mfma_f32_16x16x32_bf16 v[172:175], v[108:111], v[36:39], v[172:175]\n" \
"ds_read_b64_tr_b16 v[92:93], v9 offset:24576\n" \
"buffer_load_dwordx4 v[220:223], v116, s[84:87], s93 offen offset:1024 sc1\n" \
"v_mfma_f32_16x16x32_bf16 v[172:175], v[104:107], v[32:35], v[172:175]\n" \
"ds_read_b64_tr_b16 v[94:95], v9 offset:28672\n" \
"buffer_load_dwordx4 v12, s[36:39], s64 offen offset:2048 sc1 lds\n" \
"v_mfma_f32_16x16x32_bf16 v[156:159], v[104:107], v[24:27], v[156:159]\n" \
"v_mfma_f32_16x16x32_bf16 v[156:159], v[108:111], v[28:31], v[156:159]\n" \
"v_mfma_f32_16x16x32_bf16 v[140:143], v[108:111], v[20:23], v[140:143]\n" \
"buffer_load_dwordx4 v12, s[36:39], s64 offen offset:3072 sc1 lds\n" \
"v_mfma_f32_16x16x32_bf16 v[140:143], v[104:107], v[16:19], v[140:143]\n" \
"s_waitcnt lgkmcnt(4)\n" \
"v_mfma_f32_16x16x32_bf16 v[128:131], v[80:83], v[48:51], v[128:131]\n" \
"ds_read_b64_tr_b16 v[96:97], v10 offset:16384\n" \
"v_mfma_f32_16x16x32_bf16 v[128:131], v[84:87], v[52:55], v[128:131]\n" \
"ds_read_b64_tr_b16 v[98:99], v10 offset:20480\n" \
"s_add_u32 m0, s70, 65536\n" \
"s_mov_b32 s65, 918528\n" \
"v_mfma_f32_16x16x32_bf16 v[144:147], v[84:87], v[60:63], v[144:147]\n" \
"ds_read_b64_tr_b16 v[100:101], v10 offset:24576\n" \
"buffer_load_dwordx4 v13, s[40:43], s65 offen offset:0 lds\n" \
"v_mfma_f32_16x16x32_bf16 v[144:147], v[80:83], v[56:59], v[144:147]\n" \
"ds_read_b64_tr_b16 v[102:103], v10 offset:28672\n" \
"s_mov_b32 s93, 786432\n" \
"v_mfma_f32_16x16x32_bf16 v[160:163], v[80:83], v[64:67], v[160:163]\n" \
"buffer_load_dwordx4 v[224:227], v116, s[84:87], s93 offen offset:0 sc1\n" \
"v_mfma_f32_16x16x32_bf16 v[160:163], v[84:87], v[68:71], v[160:163]\n" \
"s_mov_b32 s66, 917632\n" \
"v_mfma_f32_16x16x32_bf16 v[176:179], v[84:87], v[76:79], v[176:179]\n" \
"buffer_load_dwordx4 v13, s[40:43], s66 offen offset:1024 lds\n" \
"v_mfma_f32_16x16x32_bf16 v[176:179], v[80:83], v[72:75], v[176:179]\n" \
"s_waitcnt lgkmcnt(4)\n" \
"v_mfma_f32_16x16x32_bf16 v[180:183], v[88:91], v[72:75], v[180:183]\n" \
"ds_read_b64_tr_b16 v[104:105], v11 offset:16384\n" \
"v_mfma_f32_16x16x32_bf16 v[180:183], v[92:95], v[76:79], v[180:183]\n" \
"ds_read_b64_tr_b16 v[106:107], v11 offset:20480\n" \
"s_mov_b32 s67, 932864\n" \
"v_mfma_f32_16x16x32_bf16 v[164:167], v[92:95], v[68:71], v[164:167]\n" \
"ds_read_b64_tr_b16 v[108:109], v11 offset:24576\n" \
"buffer_load_dwordx4 v13, s[40:43], s67 offen offset:2048 lds\n" \
"v_mfma_f32_16x16x32_bf16 v[164:167], v[88:91], v[64:67], v[164:167]\n" \
"ds_read_b64_tr_b16 v[110:111], v11 offset:28672\n" \
"s_mov_b32 s93, 786432\n" \
"v_mfma_f32_16x16x32_bf16 v[148:151], v[88:91], v[56:59], v[148:151]\n" \
"buffer_load_dwordx4 v[228:231], v116, s[84:87], s93 offen offset:1024 sc1\n" \
"v_mfma_f32_16x16x32_bf16 v[148:151], v[92:95], v[60:63], v[148:151]\n" \
"s_mov_b32 s68, 931968\n" \
"v_mfma_f32_16x16x32_bf16 v[132:135], v[92:95], v[52:55], v[132:135]\n" \
"buffer_load_dwordx4 v13, s[40:43], s68 offen offset:3072 lds\n" \
"v_mfma_f32_16x16x32_bf16 v[132:135], v[88:91], v[48:51], v[132:135]\n" \
"s_waitcnt vmcnt(24) lgkmcnt(0)\n" \
"s_barrier\n" \
"v_mfma_f32_16x16x32_bf16 v[136:139], v[96:99], v[48:51], v[136:139]\n" \
"ds_read_b64_tr_b16 v[80:81], v4 offset:0\n" \
"ds_read_b64_tr_b16 v[82:83], v4 offset:4096\n" \
"v_mfma_f32_16x16x32_bf16 v[136:139], v[100:103], v[52:55], v[136:139]\n" \
"ds_read_b64_tr_b16 v[84:85], v4 offset:8192\n" \
"ds_read_b64_tr_b16 v[86:87], v4 offset:12288\n" \
"v_mfma_f32_16x16x32_bf16 v[152:155], v[100:103], v[60:63], v[152:155]\n" \
"ds_read_b128 v[16:19], v0 offset:32768\n" \
"ds_read_b128 v[20:23], v0 offset:33792\n" \
"s_mov_b32 s93, 983040\n" \
"v_mfma_f32_16x16x32_bf16 v[152:155], v[96:99], v[56:59], v[152:155]\n" \
"ds_read_b128 v[24:27], v0 offset:34816\n" \
"ds_read_b128 v[28:31], v0 offset:35840\n" \
"buffer_load_dwordx4 v[232:235], v116, s[84:87], s93 offen offset:0 sc1\n" \
"v_mfma_f32_16x16x32_bf16 v[168:171], v[96:99], v[64:67], v[168:171]\n" \
"ds_read_b128 v[32:35], v2 offset:32768\n" \
"s_add_u32 m0, s70, 114688\n" \
"s_mov_b32 s64, 131072\n" \
"v_mfma_f32_16x16x32_bf16 v[168:171], v[100:103], v[68:71], v[168:171]\n" \
"ds_read_b128 v[36:39], v2 offset:33792\n" \
"buffer_load_dwordx4 v12, s[36:39], s64 offen offset:0 sc1 lds\n" \
"v_mfma_f32_16x16x32_bf16 v[184:187], v[100:103], v[76:79], v[184:187]\n" \
"ds_read_b128 v[40:43], v2 offset:34816\n" \
"v_mfma_f32_16x16x32_bf16 v[184:187], v[96:99], v[72:75], v[184:187]\n" \
"ds_read_b128 v[44:47], v2 offset:35840\n" \
"v_mfma_f32_16x16x32_bf16 v[188:191], v[104:107], v[72:75], v[188:191]\n" \
"ds_read_b64_tr_b16 v[88:89], v5 offset:0\n" \
"buffer_load_dwordx4 v12, s[36:39], s64 offen offset:1024 sc1 lds\n" \
"v_mfma_f32_16x16x32_bf16 v[188:191], v[108:111], v[76:79], v[188:191]\n" \
"ds_read_b64_tr_b16 v[90:91], v5 offset:4096\n" \
"s_mov_b32 s93, 983040\n" \
"v_mfma_f32_16x16x32_bf16 v[172:175], v[108:111], v[68:71], v[172:175]\n" \
"ds_read_b64_tr_b16 v[92:93], v5 offset:8192\n" \
"buffer_load_dwordx4 v[236:239], v116, s[84:87], s93 offen offset:1024 sc1\n" \
"v_mfma_f32_16x16x32_bf16 v[172:175], v[104:107], v[64:67], v[172:175]\n" \
"ds_read_b64_tr_b16 v[94:95], v5 offset:12288\n" \
"buffer_load_dwordx4 v12, s[36:39], s64 offen offset:2048 sc1 lds\n" \
"v_mfma_f32_16x16x32_bf16 v[156:159], v[104:107], v[56:59], v[156:159]\n" \
"v_mfma_f32_16x16x32_bf16 v[156:159], v[108:111], v[60:63], v[156:159]\n" \
"v_mfma_f32_16x16x32_bf16 v[140:143], v[108:111], v[52:55], v[140:143]\n" \
"buffer_load_dwordx4 v12, s[36:39], s64 offen offset:3072 sc1 lds\n" \
"v_mfma_f32_16x16x32_bf16 v[140:143], v[104:107], v[48:51], v[140:143]\n" \
"s_waitcnt lgkmcnt(4)\n" \
"v_mfma_f32_16x16x32_bf16 v[128:131], v[80:83], v[16:19], v[128:131]\n" \
"ds_read_b64_tr_b16 v[96:97], v6 offset:0\n" \
"v_mfma_f32_16x16x32_bf16 v[128:131], v[84:87], v[20:23], v[128:131]\n" \
"ds_read_b64_tr_b16 v[98:99], v6 offset:4096\n" \
"s_add_u32 m0, s70, 81920\n" \
"s_mov_b32 s65, 1049600\n" \
"v_mfma_f32_16x16x32_bf16 v[144:147], v[84:87], v[28:31], v[144:147]\n" \
"ds_read_b64_tr_b16 v[100:101], v6 offset:8192\n" \
"buffer_load_dwordx4 v13, s[40:43], s65 offen offset:0 lds\n" \
"v_mfma_f32_16x16x32_bf16 v[144:147], v[80:83], v[24:27], v[144:147]\n" \
"ds_read_b64_tr_b16 v[102:103], v6 offset:12288\n" \
"s_mov_b32 s93, 1179648\n" \
"v_mfma_f32_16x16x32_bf16 v[160:163], v[80:83], v[32:35], v[160:163]\n" \
"buffer_load_dwordx4 v[240:243], v116, s[84:87], s93 offen offset:0 sc1\n" \
"v_mfma_f32_16x16x32_bf16 v[160:163], v[84:87], v[36:39], v[160:163]\n" \
"s_mov_b32 s66, 1048704\n" \
"v_mfma_f32_16x16x32_bf16 v[176:179], v[84:87], v[44:47], v[176:179]\n" \
"buffer_load_dwordx4 v13, s[40:43], s66 offen offset:1024 lds\n" \
"v_mfma_f32_16x16x32_bf16 v[176:179], v[80:83], v[40:43], v[176:179]\n" \
"s_waitcnt lgkmcnt(4)\n" \
"v_mfma_f32_16x16x32_bf16 v[180:183], v[88:91], v[40:43], v[180:183]\n" \
"ds_read_b64_tr_b16 v[104:105], v7 offset:0\n" \
"v_mfma_f32_16x16x32_bf16 v[180:183], v[92:95], v[44:47], v[180:183]\n" \
"ds_read_b64_tr_b16 v[106:107], v7 offset:4096\n" \
"s_mov_b32 s67, 1063936\n" \
"v_mfma_f32_16x16x32_bf16 v[164:167], v[92:95], v[36:39], v[164:167]\n" \
"ds_read_b64_tr_b16 v[108:109], v7 offset:8192\n" \
"buffer_load_dwordx4 v13, s[40:43], s67 offen offset:2048 lds\n" \
"v_mfma_f32_16x16x32_bf16 v[164:167], v[88:91], v[32:35], v[164:167]\n" \
"ds_read_b64_tr_b16 v[110:111], v7 offset:12288\n" \
"s_mov_b32 s93, 1179648\n" \
"v_mfma_f32_16x16x32_bf16 v[148:151], v[88:91], v[24:27], v[148:151]\n" \
"buffer_load_dwordx4 v[244:247], v116, s[84:87], s93 offen offset:1024 sc1\n" \
"v_mfma_f32_16x16x32_bf16 v[148:151], v[92:95], v[28:31], v[148:151]\n" \
"s_mov_b32 s68, 1063040\n" \
"v_mfma_f32_16x16x32_bf16 v[132:135], v[92:95], v[20:23], v[132:135]\n" \
"buffer_load_dwordx4 v13, s[40:43], s68 offen offset:3072 lds\n" \
"v_mfma_f32_16x16x32_bf16 v[132:135], v[88:91], v[16:19], v[132:135]\n" \
"s_waitcnt vmcnt(24) lgkmcnt(0)\n" \
"s_barrier\n" \
"v_mfma_f32_16x16x32_bf16 v[136:139], v[96:99], v[16:19], v[136:139]\n" \
"ds_read_b64_tr_b16 v[80:81], v4 offset:16384\n" \
"ds_read_b64_tr_b16 v[82:83], v4 offset:20480\n" \
"v_mfma_f32_16x16x32_bf16 v[136:139], v[100:103], v[20:23], v[136:139]\n" \
"ds_read_b64_tr_b16 v[84:85], v4 offset:24576\n" \
"ds_read_b64_tr_b16 v[86:87], v4 offset:28672\n" \
"v_mfma_f32_16x16x32_bf16 v[152:155], v[100:103], v[28:31], v[152:155]\n" \
"ds_read_b128 v[48:51], v0 offset:49152\n" \
"ds_read_b128 v[52:55], v0 offset:50176\n" \
"s_mov_b32 s93, 1376256\n" \
"v_mfma_f32_16x16x32_bf16 v[152:155], v[96:99], v[24:27], v[152:155]\n" \
"ds_read_b128 v[56:59], v0 offset:51200\n" \
"ds_read_b128 v[60:63], v0 offset:52224\n" \
"buffer_load_dwordx4 v[248:251], v116, s[84:87], s93 offen offset:0 sc1\n" \
"v_mfma_f32_16x16x32_bf16 v[168:171], v[96:99], v[32:35], v[168:171]\n" \
"ds_read_b128 v[64:67], v2 offset:49152\n" \
"s_add_u32 m0, s70, 32768\n" \
"s_mov_b32 s64, 163840\n" \
"v_mfma_f32_16x16x32_bf16 v[168:171], v[100:103], v[36:39], v[168:171]\n" \
"ds_read_b128 v[68:71], v2 offset:50176\n" \
"buffer_load_dwordx4 v12, s[36:39], s64 offen offset:0 sc1 lds\n" \
"v_mfma_f32_16x16x32_bf16 v[184:187], v[100:103], v[44:47], v[184:187]\n" \
"ds_read_b128 v[72:75], v2 offset:51200\n" \
"v_mfma_f32_16x16x32_bf16 v[184:187], v[96:99], v[40:43], v[184:187]\n" \
"ds_read_b128 v[76:79], v2 offset:52224\n" \
"v_mfma_f32_16x16x32_bf16 v[188:191], v[104:107], v[40:43], v[188:191]\n" \
"ds_read_b64_tr_b16 v[88:89], v5 offset:16384\n" \
"buffer_load_dwordx4 v12, s[36:39], s64 offen offset:1024 sc1 lds\n" \
"v_mfma_f32_16x16x32_bf16 v[188:191], v[108:111], v[44:47], v[188:191]\n" \
"ds_read_b64_tr_b16 v[90:91], v5 offset:20480\n" \
"s_mov_b32 s93, 1376256\n" \
"v_mfma_f32_16x16x32_bf16 v[172:175], v[108:111], v[36:39], v[172:175]\n" \
"ds_read_b64_tr_b16 v[92:93], v5 offset:24576\n" \
"buffer_load_dwordx4 v[252:255], v116, s[84:87], s93 offen offset:1024 sc1\n" \
"v_mfma_f32_16x16x32_bf16 v[172:175], v[104:107], v[32:35], v[172:175]\n" \
"ds_read_b64_tr_b16 v[94:95], v5 offset:28672\n" \
"buffer_load_dwordx4 v12, s[36:39], s64 offen offset:2048 sc1 lds\n" \
"v_mfma_f32_16x16x32_bf16 v[156:159], v[104:107], v[24:27], v[156:159]\n" \
"v_mfma_f32_16x16x32_bf16 v[156:159], v[108:111], v[28:31], v[156:159]\n" \
"v_mfma_f32_16x16x32_bf16 v[140:143], v[108:111], v[20:23], v[140:143]\n" \
"buffer_load_dwordx4 v12, s[36:39], s64 offen offset:3072 sc1 lds\n" \
"v_mfma_f32_16x16x32_bf16 v[140:143], v[104:107], v[16:19], v[140:143]\n" \
"s_waitcnt lgkmcnt(4)\n" \
"v_mfma_f32_16x16x32_bf16 v[128:131], v[80:83], v[48:51], v[128:131]\n" \
"ds_read_b64_tr_b16 v[96:97], v6 offset:16384\n" \
"v_mfma_f32_16x16x32_bf16 v[128:131], v[84:87], v[52:55], v[128:131]\n" \
"ds_read_b64_tr_b16 v[98:99], v6 offset:20480\n" \
"s_add_u32 m0, s70, 0\n" \
"s_mov_b32 s65, 1311744\n" \
"v_mfma_f32_16x16x32_bf16 v[144:147], v[84:87], v[60:63], v[144:147]\n" \
"ds_read_b64_tr_b16 v[100:101], v6 offset:24576\n" \
"buffer_load_dwordx4 v13, s[40:43], s65 offen offset:0 lds\n" \
"v_mfma_f32_16x16x32_bf16 v[144:147], v[80:83], v[56:59], v[144:147]\n" \
"ds_read_b64_tr_b16 v[102:103], v6 offset:28672\n" \
"v_mfma_f32_16x16x32_bf16 v[160:163], v[80:83], v[64:67], v[160:163]\n" \
"v_mfma_f32_16x16x32_bf16 v[160:163], v[84:87], v[68:71], v[160:163]\n" \
"s_mov_b32 s66, 1310848\n" \
"v_mfma_f32_16x16x32_bf16 v[176:179], v[84:87], v[76:79], v[176:179]\n" \
"buffer_load_dwordx4 v13, s[40:43], s66 offen offset:1024 lds\n" \
"v_mfma_f32_16x16x32_bf16 v[176:179], v[80:83], v[72:75], v[176:179]\n" \
"s_waitcnt lgkmcnt(4)\n" \
"v_mfma_f32_16x16x32_bf16 v[180:183], v[88:91], v[72:75], v[180:183]\n" \
"ds_read_b64_tr_b16 v[104:105], v7 offset:16384\n" \
"v_mfma_f32_16x16x32_bf16 v[180:183], v[92:95], v[76:79], v[180:183]\n" \
"ds_read_b64_tr_b16 v[106:107], v7 offset:20480\n" \
"s_mov_b32 s67, 1326080\n" \
"v_mfma_f32_16x16x32_bf16 v[164:167], v[92:95], v[68:71], v[164:167]\n" \
"ds_read_b64_tr_b16 v[108:109], v7 offset:24576\n" \
"buffer_load_dwordx4 v13, s[40:43], s67 offen offset:2048 lds\n" \
"v_mfma_f32_16x16x32_bf16 v[164:167], v[88:91], v[64:67], v[164:167]\n" \
"ds_read_b64_tr_b16 v[110:111], v7 offset:28672\n" \
"v_mfma_f32_16x16x32_bf16 v[148:151], v[88:91], v[56:59], v[148:151]\n" \
"v_mfma_f32_16x16x32_bf16 v[148:151], v[92:95], v[60:63], v[148:151]\n" \
"s_mov_b32 s68, 1325184\n" \
"v_mfma_f32_16x16x32_bf16 v[132:135], v[92:95], v[52:55], v[132:135]\n" \
"buffer_load_dwordx4 v13, s[40:43], s68 offen offset:3072 lds\n" \
"v_mfma_f32_16x16x32_bf16 v[132:135], v[88:91], v[48:51], v[132:135]\n" \
"s_waitcnt vmcnt(22) lgkmcnt(0)\n" \
"s_barrier\n" \
"v_mfma_f32_16x16x32_bf16 v[136:139], v[96:99], v[48:51], v[136:139]\n" \
"ds_read_b64_tr_b16 v[80:81], v8 offset:0\n" \
"ds_read_b64_tr_b16 v[82:83], v8 offset:4096\n" \
"v_mfma_f32_16x16x32_bf16 v[136:139], v[100:103], v[52:55], v[136:139]\n" \
"ds_read_b64_tr_b16 v[84:85], v8 offset:8192\n" \
"ds_read_b64_tr_b16 v[86:87], v8 offset:12288\n" \
"v_mfma_f32_16x16x32_bf16 v[152:155], v[100:103], v[60:63], v[152:155]\n" \
"ds_read_b128 v[16:19], v1 offset:32768\n" \
"ds_read_b128 v[20:23], v1 offset:33792\n" \
"v_mfma_f32_16x16x32_bf16 v[152:155], v[96:99], v[56:59], v[152:155]\n" \
"ds_read_b128 v[24:27], v1 offset:34816\n" \
"ds_read_b128 v[28:31], v1 offset:35840\n" \
"v_mfma_f32_16x16x32_bf16 v[168:171], v[96:99], v[64:67], v[168:171]\n" \
"ds_read_b128 v[32:35], v3 offset:32768\n" \
"s_add_u32 m0, s70, 49152\n" \
"s_mov_b32 s64, 180224\n" \
"v_mfma_f32_16x16x32_bf16 v[168:171], v[100:103], v[68:71], v[168:171]\n" \
"ds_read_b128 v[36:39], v3 offset:33792\n" \
"buffer_load_dwordx4 v12, s[36:39], s64 offen offset:0 sc1 lds\n" \
"v_mfma_f32_16x16x32_bf16 v[184:187], v[100:103], v[76:79], v[184:187]\n" \
"ds_read_b128 v[40:43], v3 offset:34816\n" \
"v_mfma_f32_16x16x32_bf16 v[184:187], v[96:99], v[72:75], v[184:187]\n" \
"ds_read_b128 v[44:47], v3 offset:35840\n" \
"v_mfma_f32_16x16x32_bf16 v[188:191], v[104:107], v[72:75], v[188:191]\n" \
"ds_read_b64_tr_b16 v[88:89], v9 offset:0\n" \
"buffer_load_dwordx4 v12, s[36:39], s64 offen offset:1024 sc1 lds\n" \
"v_mfma_f32_16x16x32_bf16 v[188:191], v[108:111], v[76:79], v[188:191]\n" \
"ds_read_b64_tr_b16 v[90:91], v9 offset:4096\n" \
"v_mfma_f32_16x16x32_bf16 v[172:175], v[108:111], v[68:71], v[172:175]\n" \
"ds_read_b64_tr_b16 v[92:93], v9 offset:8192\n" \
"v_mfma_f32_16x16x32_bf16 v[172:175], v[104:107], v[64:67], v[172:175]\n" \
"ds_read_b64_tr_b16 v[94:95], v9 offset:12288\n" \
"buffer_load_dwordx4 v12, s[36:39], s64 offen offset:2048 sc1 lds\n" \
"v_mfma_f32_16x16x32_bf16 v[156:159], v[104:107], v[56:59], v[156:159]\n" \
"v_mfma_f32_16x16x32_bf16 v[156:159], v[108:111], v[60:63], v[156:159]\n" \
"v_mfma_f32_16x16x32_bf16 v[140:143], v[108:111], v[52:55], v[140:143]\n" \
"buffer_load_dwordx4 v12, s[36:39], s64 offen offset:3072 sc1 lds\n" \
"v_mfma_f32_16x16x32_bf16 v[140:143], v[104:107], v[48:51], v[140:143]\n" \
"s_waitcnt lgkmcnt(4)\n" \
"v_mfma_f32_16x16x32_bf16 v[128:131], v[80:83], v[16:19], v[128:131]\n" \
"ds_read_b64_tr_b16 v[96:97], v10 offset:0\n" \
"v_mfma_f32_16x16x32_bf16 v[128:131], v[84:87], v[20:23], v[128:131]\n" \
"ds_read_b64_tr_b16 v[98:99], v10 offset:4096\n" \
"s_add_u32 m0, s70, 16384\n" \
"s_mov_b32 s65, 1442816\n" \
"v_mfma_f32_16x16x32_bf16 v[144:147], v[84:87], v[28:31], v[144:147]\n" \
"ds_read_b64_tr_b16 v[100:101], v10 offset:8192\n" \
"buffer_load_dwordx4 v13, s[40:43], s65 offen offset:0 lds\n" \
"v_mfma_f32_16x16x32_bf16 v[144:147], v[80:83], v[24:27], v[144:147]\n" \
"ds_read_b64_tr_b16 v[102:103], v10 offset:12288\n" \
"v_mfma_f32_16x16x32_bf16 v[160:163], v[80:83], v[32:35], v[160:163]\n" \
"v_mfma_f32_16x16x32_bf16 v[160:163], v[84:87], v[36:39], v[160:163]\n" \
"s_mov_b32 s66, 1441920\n" \
"v_mfma_f32_16x16x32_bf16 v[176:179], v[84:87], v[44:47], v[176:179]\n" \
"buffer_load_dwordx4 v13, s[40:43], s66 offen offset:1024 lds\n" \
"v_mfma_f32_16x16x32_bf16 v[176:179], v[80:83], v[40:43], v[176:179]\n" \
"s_waitcnt lgkmcnt(4)\n" \
"v_mfma_f32_16x16x32_bf16 v[180:183], v[88:91], v[40:43], v[180:183]\n" \
"ds_read_b64_tr_b16 v[104:105], v11 offset:0\n" \
"v_mfma_f32_16x16x32_bf16 v[180:183], v[92:95], v[44:47], v[180:183]\n" \
"ds_read_b64_tr_b16 v[106:107], v11 offset:4096\n" \
"s_mov_b32 s67, 1457152\n" \
"v_mfma_f32_16x16x32_bf16 v[164:167], v[92:95], v[36:39], v[164:167]\n" \
"ds_read_b64_tr_b16 v[108:109], v11 offset:8192\n" \
"buffer_load_dwordx4 v13, s[40:43], s67 offen offset:2048 lds\n" \
"v_mfma_f32_16x16x32_bf16 v[164:167], v[88:91], v[32:35], v[164:167]\n" \
"ds_read_b64_tr_b16 v[110:111], v11 offset:12288\n" \
"v_mfma_f32_16x16x32_bf16 v[148:151], v[88:91], v[24:27], v[148:151]\n" \
"v_mfma_f32_16x16x32_bf16 v[148:151], v[92:95], v[28:31], v[148:151]\n" \
"s_mov_b32 s68, 1456256\n" \
"v_mfma_f32_16x16x32_bf16 v[132:135], v[92:95], v[20:23], v[132:135]\n" \
"buffer_load_dwordx4 v13, s[40:43], s68 offen offset:3072 lds\n" \
"v_mfma_f32_16x16x32_bf16 v[132:135], v[88:91], v[16:19], v[132:135]\n" \
"s_waitcnt vmcnt(14) lgkmcnt(0)\n" \
"s_barrier\n" \
"v_mfma_f32_16x16x32_bf16 v[136:139], v[96:99], v[16:19], v[136:139]\n" \
"ds_read_b64_tr_b16 v[80:81], v8 offset:16384\n" \
"ds_read_b64_tr_b16 v[82:83], v8 offset:20480\n" \
"v_add_f32 v192, v192, v200\n" \
"v_mfma_f32_16x16x32_bf16 v[136:139], v[100:103], v[20:23], v[136:139]\n" \
"ds_read_b64_tr_b16 v[84:85], v8 offset:24576\n" \
"ds_read_b64_tr_b16 v[86:87], v8 offset:28672\n" \
"v_add_f32 v193, v193, v201\n" \
"v_mfma_f32_16x16x32_bf16 v[152:155], v[100:103], v[28:31], v[152:155]\n" \
"ds_read_b128 v[48:51], v1 offset:49152\n" \
"ds_read_b128 v[52:55], v1 offset:50176\n" \
"v_add_f32 v194, v194, v202\n" \
"v_mfma_f32_16x16x32_bf16 v[152:155], v[96:99], v[24:27], v[152:155]\n" \
"ds_read_b128 v[56:59], v1 offset:51200\n" \
"ds_read_b128 v[60:63], v1 offset:52224\n" \
"v_add_f32 v195, v195, v203\n" \
"v_mfma_f32_16x16x32_bf16 v[168:171], v[96:99], v[32:35], v[168:171]\n" \
"ds_read_b128 v[64:67], v3 offset:49152\n" \
"s_add_u32 m0, s70, 98304\n" \
"s_mov_b32 s64, 196608\n" \
"v_mfma_f32_16x16x32_bf16 v[168:171], v[100:103], v[36:39], v[168:171]\n" \
"ds_read_b128 v[68:71], v3 offset:50176\n" \
"buffer_load_dwordx4 v12, s[36:39], s64 offen offset:0 sc1 lds\n" \
"v_add_f32 v196, v196, v204\n" \
"v_mfma_f32_16x16x32_bf16 v[184:187], v[100:103], v[44:47], v[184:187]\n" \
"ds_read_b128 v[72:75], v3 offset:51200\n" \
"v_add_f32 v197, v197, v205\n" \
"v_add_f32 v198, v198, v206\n" \
"v_mfma_f32_16x16x32_bf16 v[184:187], v[96:99], v[40:43], v[184:187]\n" \
"ds_read_b128 v[76:79], v3 offset:52224\n" \
"v_add_f32 v199, v199, v207\n" \
"v_add_f32 v208, v208, v216\n" \
"v_mfma_f32_16x16x32_bf16 v[188:191], v[104:107], v[40:43], v[188:191]\n" \
"ds_read_b64_tr_b16 v[88:89], v9 offset:16384\n" \
"buffer_load_dwordx4 v12, s[36:39], s64 offen offset:1024 sc1 lds\n" \
"v_add_f32 v209, v209, v217\n" \
"v_mfma_f32_16x16x32_bf16 v[188:191], v[108:111], v[44:47], v[188:191]\n" \
"ds_read_b64_tr_b16 v[90:91], v9 offset:20480\n" \
"v_add_f32 v210, v210, v218\n" \
"v_add_f32 v211, v211, v219\n" \
"v_mfma_f32_16x16x32_bf16 v[172:175], v[108:111], v[36:39], v[172:175]\n" \
"ds_read_b64_tr_b16 v[92:93], v9 offset:24576\n" \
"v_add_f32 v212, v212, v220\n" \
"v_add_f32 v213, v213, v221\n" \
"v_mfma_f32_16x16x32_bf16 v[172:175], v[104:107], v[32:35], v[172:175]\n" \
"ds_read_b64_tr_b16 v[94:95], v9 offset:28672\n" \
"buffer_load_dwordx4 v12, s[36:39], s64 offen offset:2048 sc1 lds\n" \
"v_add_f32 v214, v214, v222\n" \
"v_mfma_f32_16x16x32_bf16 v[156:159], v[104:107], v[24:27], v[156:159]\n" \
"v_add_f32 v215, v215, v223\n" \
"v_add_f32 v224, v224, v232\n" \
"v_add_f32 v225, v225, v233\n" \
"v_mfma_f32_16x16x32_bf16 v[156:159], v[108:111], v[28:31], v[156:159]\n" \
"v_add_f32 v226, v226, v234\n" \
"v_add_f32 v227, v227, v235\n" \
"v_add_f32 v228, v228, v236\n" \
"v_mfma_f32_16x16x32_bf16 v[140:143], v[108:111], v[20:23], v[140:143]\n" \
"buffer_load_dwordx4 v12, s[36:39], s64 offen offset:3072 sc1 lds\n" \
"v_add_f32 v229, v229, v237\n" \
"v_add_f32 v230, v230, v238\n" \
"v_mfma_f32_16x16x32_bf16 v[140:143], v[104:107], v[16:19], v[140:143]\n" \
"v_add_f32 v231, v231, v239\n" \
"v_add_f32 v240, v240, v248\n" \
"v_add_f32 v241, v241, v249\n" \
"s_waitcnt lgkmcnt(4)\n" \
"v_mfma_f32_16x16x32_bf16 v[128:131], v[80:83], v[48:51], v[128:131]\n" \
"ds_read_b64_tr_b16 v[96:97], v10 offset:16384\n" \
"v_add_f32 v242, v242, v250\n" \
"v_add_f32 v243, v243, v251\n" \
"v_mfma_f32_16x16x32_bf16 v[128:131], v[84:87], v[52:55], v[128:131]\n" \
"ds_read_b64_tr_b16 v[98:99], v10 offset:20480\n" \
"s_add_u32 m0, s70, 65536\n" \
"s_mov_b32 s65, 1573888\n" \
"v_mfma_f32_16x16x32_bf16 v[144:147], v[84:87], v[60:63], v[144:147]\n" \
"ds_read_b64_tr_b16 v[100:101], v10 offset:24576\n" \
"buffer_load_dwordx4 v13, s[40:43], s65 offen offset:0 lds\n" \
"v_add_f32 v244, v244, v252\n" \
"v_mfma_f32_16x16x32_bf16 v[144:147], v[80:83], v[56:59], v[144:147]\n" \
"ds_read_b64_tr_b16 v[102:103], v10 offset:28672\n" \
"v_add_f32 v245, v245, v253\n" \
"v_add_f32 v246, v246, v254\n" \
"v_mfma_f32_16x16x32_bf16 v[160:163], v[80:83], v[64:67], v[160:163]\n" \
"v_add_f32 v247, v247, v255\n" \
"v_add_f32 v192, v192, v208\n" \
"v_add_f32 v193, v193, v209\n" \
"v_mfma_f32_16x16x32_bf16 v[160:163], v[84:87], v[68:71], v[160:163]\n" \
"s_mov_b32 s66, 1572992\n" \
"v_add_f32 v194, v194, v210\n" \
"v_add_f32 v195, v195, v211\n" \
"v_mfma_f32_16x16x32_bf16 v[176:179], v[84:87], v[76:79], v[176:179]\n" \
"buffer_load_dwordx4 v13, s[40:43], s66 offen offset:1024 lds\n" \
"v_add_f32 v196, v196, v212\n" \
"v_add_f32 v197, v197, v213\n" \
"v_mfma_f32_16x16x32_bf16 v[176:179], v[80:83], v[72:75], v[176:179]\n" \
"v_add_f32 v198, v198, v214\n" \
"v_add_f32 v199, v199, v215\n" \
"v_add_f32 v224, v224, v240\n" \
"s_waitcnt lgkmcnt(4)\n" \
"v_mfma_f32_16x16x32_bf16 v[180:183], v[88:91], v[72:75], v[180:183]\n" \
"ds_read_b64_tr_b16 v[104:105], v11 offset:16384\n" \
"v_add_f32 v225, v225, v241\n" \
"v_add_f32 v226, v226, v242\n" \
"v_mfma_f32_16x16x32_bf16 v[180:183], v[92:95], v[76:79], v[180:183]\n" \
"ds_read_b64_tr_b16 v[106:107], v11 offset:20480\n" \
"s_mov_b32 s67, 1588224\n" \
"v_add_f32 v227, v227, v243\n" \
"v_mfma_f32_16x16x32_bf16 v[164:167], v[92:95], v[68:71], v[164:167]\n" \
"ds_read_b64_tr_b16 v[108:109], v11 offset:24576\n" \
"buffer_load_dwordx4 v13, s[40:43], s67 offen offset:2048 lds\n" \
"v_add_f32 v228, v228, v244\n" \
"v_mfma_f32_16x16x32_bf16 v[164:167], v[88:91], v[64:67], v[164:167]\n" \
"ds_read_b64_tr_b16 v[110:111], v11 offset:28672\n" \
"v_add_f32 v229, v229, v245\n" \
"v_add_f32 v230, v230, v246\n" \
"v_mfma_f32_16x16x32_bf16 v[148:151], v[88:91], v[56:59], v[148:151]\n" \
"v_add_f32 v231, v231, v247\n" \
"v_add_f32 v192, v192, v224\n" \
"v_add_f32 v193, v193, v225\n" \
"v_mfma_f32_16x16x32_bf16 v[148:151], v[92:95], v[60:63], v[148:151]\n" \
"s_mov_b32 s68, 1587328\n" \
"v_add_f32 v194, v194, v226\n" \
"v_add_f32 v195, v195, v227\n" \
"v_mfma_f32_16x16x32_bf16 v[132:135], v[92:95], v[52:55], v[132:135]\n" \
"buffer_load_dwordx4 v13, s[40:43], s68 offen offset:3072 lds\n" \
"v_add_f32 v196, v196, v228\n" \
"v_add_f32 v197, v197, v229\n" \
"v_mfma_f32_16x16x32_bf16 v[132:135], v[88:91], v[48:51], v[132:135]\n" \
"v_add_f32 v198, v198, v230\n" \
"v_add_f32 v199, v199, v231\n" \
"v_cvt_pk_bf16_f32 v192, v192, v193\n" \
"s_waitcnt vmcnt(16) lgkmcnt(0)\n" \
"s_barrier\n" \
"v_mfma_f32_16x16x32_bf16 v[136:139], v[96:99], v[48:51], v[136:139]\n" \
"ds_read_b64_tr_b16 v[80:81], v4 offset:0\n" \
"ds_read_b64_tr_b16 v[82:83], v4 offset:4096\n" \
"v_cvt_pk_bf16_f32 v193, v194, v195\n" \
"v_mfma_f32_16x16x32_bf16 v[136:139], v[100:103], v[52:55], v[136:139]\n" \
"ds_read_b64_tr_b16 v[84:85], v4 offset:8192\n" \
"ds_read_b64_tr_b16 v[86:87], v4 offset:12288\n" \
"v_cvt_pk_bf16_f32 v194, v196, v197\n" \
"v_mfma_f32_16x16x32_bf16 v[152:155], v[100:103], v[60:63], v[152:155]\n" \
"ds_read_b128 v[16:19], v0 offset:32768\n" \
"ds_read_b128 v[20:23], v0 offset:33792\n" \
"v_cvt_pk_bf16_f32 v195, v198, v199\n" \
"v_mfma_f32_16x16x32_bf16 v[152:155], v[96:99], v[56:59], v[152:155]\n" \
"ds_read_b128 v[24:27], v0 offset:34816\n" \
"ds_read_b128 v[28:31], v0 offset:35840\n" \
"ds_write_b128 v124, v[192:195]\n" \
"v_mfma_f32_16x16x32_bf16 v[168:171], v[96:99], v[64:67], v[168:171]\n" \
"ds_read_b128 v[32:35], v2 offset:32768\n" \
"s_add_u32 m0, s70, 114688\n" \
"s_mov_b32 s64, 212992\n" \
"v_mfma_f32_16x16x32_bf16 v[168:171], v[100:103], v[68:71], v[168:171]\n" \
"ds_read_b128 v[36:39], v2 offset:33792\n" \
"buffer_load_dwordx4 v12, s[36:39], s64 offen offset:0 sc1 lds\n" \
"buffer_load_dwordx4 v[240:243], v15, s[88:91], 0 offen offset:0 sc1\n" \
"v_mfma_f32_16x16x32_bf16 v[184:187], v[100:103], v[76:79], v[184:187]\n" \
"ds_read_b128 v[40:43], v2 offset:34816\n" \
"buffer_load_dwordx4 v[244:247], v15, s[88:91], 0 offen offset:2048 sc1\n" \
"buffer_load_dwordx4 v[248:251], v15, s[88:91], s94 offen offset:0 sc1\n" \
"v_mfma_f32_16x16x32_bf16 v[184:187], v[96:99], v[72:75], v[184:187]\n" \
"ds_read_b128 v[44:47], v2 offset:35840\n" \
"buffer_load_dwordx4 v[252:255], v15, s[88:91], s94 offen offset:2048 sc1\n" \
"v_mfma_f32_16x16x32_bf16 v[188:191], v[104:107], v[72:75], v[188:191]\n" \
"ds_read_b64_tr_b16 v[88:89], v5 offset:0\n" \
"buffer_load_dwordx4 v12, s[36:39], s64 offen offset:1024 sc1 lds\n" \
"v_mfma_f32_16x16x32_bf16 v[188:191], v[108:111], v[76:79], v[188:191]\n" \
"ds_read_b64_tr_b16 v[90:91], v5 offset:4096\n" \
"v_mfma_f32_16x16x32_bf16 v[172:175], v[108:111], v[68:71], v[172:175]\n" \
"ds_read_b64_tr_b16 v[92:93], v5 offset:8192\n" \
"v_mfma_f32_16x16x32_bf16 v[172:175], v[104:107], v[64:67], v[172:175]\n" \
"ds_read_b64_tr_b16 v[94:95], v5 offset:12288\n" \
"buffer_load_dwordx4 v12, s[36:39], s64 offen offset:2048 sc1 lds\n" \
"v_mfma_f32_16x16x32_bf16 v[156:159], v[104:107], v[56:59], v[156:159]\n" \
"v_mfma_f32_16x16x32_bf16 v[156:159], v[108:111], v[60:63], v[156:159]\n" \
"v_mfma_f32_16x16x32_bf16 v[140:143], v[108:111], v[52:55], v[140:143]\n" \
"buffer_load_dwordx4 v12, s[36:39], s64 offen offset:3072 sc1 lds\n" \
"v_mfma_f32_16x16x32_bf16 v[140:143], v[104:107], v[48:51], v[140:143]\n" \
"s_waitcnt lgkmcnt(4)\n" \
"v_mfma_f32_16x16x32_bf16 v[128:131], v[80:83], v[16:19], v[128:131]\n" \
"ds_read_b64_tr_b16 v[96:97], v6 offset:0\n" \
"v_mfma_f32_16x16x32_bf16 v[128:131], v[84:87], v[20:23], v[128:131]\n" \
"ds_read_b64_tr_b16 v[98:99], v6 offset:4096\n" \
"s_add_u32 m0, s70, 81920\n" \
"s_mov_b32 s65, 1704960\n" \
"v_mfma_f32_16x16x32_bf16 v[144:147], v[84:87], v[28:31], v[144:147]\n" \
"ds_read_b64_tr_b16 v[100:101], v6 offset:8192\n" \
"buffer_load_dwordx4 v13, s[40:43], s65 offen offset:0 lds\n" \
"v_mfma_f32_16x16x32_bf16 v[144:147], v[80:83], v[24:27], v[144:147]\n" \
"ds_read_b64_tr_b16 v[102:103], v6 offset:12288\n" \
"v_mfma_f32_16x16x32_bf16 v[160:163], v[80:83], v[32:35], v[160:163]\n" \
"v_mfma_f32_16x16x32_bf16 v[160:163], v[84:87], v[36:39], v[160:163]\n" \
"s_mov_b32 s66, 1704064\n" \
"v_mfma_f32_16x16x32_bf16 v[176:179], v[84:87], v[44:47], v[176:179]\n" \
"buffer_load_dwordx4 v13, s[40:43], s66 offen offset:1024 lds\n" \
"v_mfma_f32_16x16x32_bf16 v[176:179], v[80:83], v[40:43], v[176:179]\n" \
"s_waitcnt lgkmcnt(4)\n" \
"v_mfma_f32_16x16x32_bf16 v[180:183], v[88:91], v[40:43], v[180:183]\n" \
"ds_read_b64_tr_b16 v[104:105], v7 offset:0\n" \
"v_mfma_f32_16x16x32_bf16 v[180:183], v[92:95], v[44:47], v[180:183]\n" \
"ds_read_b64_tr_b16 v[106:107], v7 offset:4096\n" \
"s_mov_b32 s67, 1719296\n" \
"v_mfma_f32_16x16x32_bf16 v[164:167], v[92:95], v[36:39], v[164:167]\n" \
"ds_read_b64_tr_b16 v[108:109], v7 offset:8192\n" \
"buffer_load_dwordx4 v13, s[40:43], s67 offen offset:2048 lds\n" \
"v_mfma_f32_16x16x32_bf16 v[164:167], v[88:91], v[32:35], v[164:167]\n" \
"ds_read_b64_tr_b16 v[110:111], v7 offset:12288\n" \
"v_mfma_f32_16x16x32_bf16 v[148:151], v[88:91], v[24:27], v[148:151]\n" \
"v_mfma_f32_16x16x32_bf16 v[148:151], v[92:95], v[28:31], v[148:151]\n" \
"s_mov_b32 s68, 1718400\n" \
"v_mfma_f32_16x16x32_bf16 v[132:135], v[92:95], v[20:23], v[132:135]\n" \
"buffer_load_dwordx4 v13, s[40:43], s68 offen offset:3072 lds\n" \
"v_mfma_f32_16x16x32_bf16 v[132:135], v[88:91], v[16:19], v[132:135]\n" \
"s_waitcnt vmcnt(20) lgkmcnt(0)\n" \
"s_barrier\n" \
"v_mfma_f32_16x16x32_bf16 v[136:139], v[96:99], v[16:19], v[136:139]\n" \
"ds_read_b64_tr_b16 v[80:81], v4 offset:16384\n" \
"ds_read_b64_tr_b16 v[82:83], v4 offset:20480\n" \
"ds_read_b128 v[224:227], v115 offset:0\n" \
"v_mfma_f32_16x16x32_bf16 v[136:139], v[100:103], v[20:23], v[136:139]\n" \
"ds_read_b64_tr_b16 v[84:85], v4 offset:24576\n" \
"ds_read_b64_tr_b16 v[86:87], v4 offset:28672\n" \
"ds_read_b128 v[228:231], v115 offset:1024\n" \
"v_mfma_f32_16x16x32_bf16 v[152:155], v[100:103], v[28:31], v[152:155]\n" \
"ds_read_b128 v[48:51], v0 offset:49152\n" \
"ds_read_b128 v[52:55], v0 offset:50176\n" \
"ds_read_b128 v[232:235], v115 offset:2048\n" \
"v_mfma_f32_16x16x32_bf16 v[152:155], v[96:99], v[24:27], v[152:155]\n" \
"ds_read_b128 v[56:59], v0 offset:51200\n" \
"ds_read_b128 v[60:63], v0 offset:52224\n" \
"s_nop 0\n" \
"v_mfma_f32_16x16x32_bf16 v[168:171], v[96:99], v[32:35], v[168:171]\n" \
"ds_read_b128 v[64:67], v2 offset:49152\n" \
"s_add_u32 m0, s70, 32768\n" \
"s_mov_b32 s64, 245760\n" \
"v_mfma_f32_16x16x32_bf16 v[168:171], v[100:103], v[36:39], v[168:171]\n" \
"ds_read_b128 v[68:71], v2 offset:50176\n" \
"buffer_load_dwordx4 v12, s[36:39], s64 offen offset:0 sc1 lds\n" \
"s_nop 0\n" \
"v_mfma_f32_16x16x32_bf16 v[184:187], v[100:103], v[44:47], v[184:187]\n" \
"ds_read_b128 v[72:75], v2 offset:51200\n" \
"s_nop 0\n" \
"s_nop 0\n" \
"v_mfma_f32_16x16x32_bf16 v[184:187], v[96:99], v[40:43], v[184:187]\n" \
"ds_read_b128 v[76:79], v2 offset:52224\n" \
"s_nop 0\n" \
"s_nop 0\n" \
"v_mfma_f32_16x16x32_bf16 v[188:191], v[104:107], v[40:43], v[188:191]\n" \
"ds_read_b64_tr_b16 v[88:89], v5 offset:16384\n" \
"buffer_load_dwordx4 v12, s[36:39], s64 offen offset:1024 sc1 lds\n" \
"s_waitcnt lgkmcnt(7)\n" \
"v_mfma_f32_16x16x32_bf16 v[188:191], v[108:111], v[44:47], v[188:191]\n" \
"ds_read_b64_tr_b16 v[90:91], v5 offset:20480\n" \
"s_waitcnt vmcnt(9)\n" \
"s_nop 0\n" \
"v_mfma_f32_16x16x32_bf16 v[172:175], v[108:111], v[36:39], v[172:175]\n" \
"ds_read_b64_tr_b16 v[92:93], v5 offset:24576\n" \
"s_nop 0\n" \
"v_mfma_f32_16x16x32_bf16 v[192:195], v[224:227], v[240:243], 0\n" \
"v_mfma_f32_16x16x32_bf16 v[172:175], v[104:107], v[32:35], v[172:175]\n" \
"ds_read_b64_tr_b16 v[94:95], v5 offset:28672\n" \
"buffer_load_dwordx4 v12, s[36:39], s64 offen offset:2048 sc1 lds\n" \
"v_mfma_f32_16x16x32_bf16 v[196:199], v[228:231], v[240:243], 0\n" \
"v_mfma_f32_16x16x32_bf16 v[156:159], v[104:107], v[24:27], v[156:159]\n" \
"v_mfma_f32_16x16x32_bf16 v[200:203], v[232:235], v[240:243], 0\n" \
"v_mfma_f32_16x16x32_bf16 v[204:207], v[224:227], v[244:247], 0\n" \
"v_mfma_f32_16x16x32_bf16 v[208:211], v[228:231], v[244:247], 0\n" \
"v_mfma_f32_16x16x32_bf16 v[156:159], v[108:111], v[28:31], v[156:159]\n" \
"v_mfma_f32_16x16x32_bf16 v[212:215], v[232:235], v[244:247], 0\n" \
"s_nop 0\n" \
"s_nop 0\n" \
"v_mfma_f32_16x16x32_bf16 v[140:143], v[108:111], v[20:23], v[140:143]\n" \
"buffer_load_dwordx4 v12, s[36:39], s64 offen offset:3072 sc1 lds\n" \
"s_nop 0\n" \
"s_nop 0\n" \
"v_mfma_f32_16x16x32_bf16 v[140:143], v[104:107], v[16:19], v[140:143]\n" \
"s_nop 0\n" \
"s_nop 0\n" \
"v_add_f32 v200, 0x358637bd, v200\n" \
"s_waitcnt lgkmcnt(4)\n" \
"v_mfma_f32_16x16x32_bf16 v[128:131], v[80:83], v[48:51], v[128:131]\n" \
"ds_read_b64_tr_b16 v[96:97], v6 offset:16384\n" \
"v_rcp_f32 v200, v200\n" \
"s_nop 0\n" \
"v_mfma_f32_16x16x32_bf16 v[128:131], v[84:87], v[52:55], v[128:131]\n" \
"ds_read_b64_tr_b16 v[98:99], v6 offset:20480\n" \
"s_add_u32 m0, s70, 0\n" \
"s_mov_b32 s65, 1967104\n" \
"v_mfma_f32_16x16x32_bf16 v[144:147], v[84:87], v[60:63], v[144:147]\n" \
"ds_read_b64_tr_b16 v[100:101], v6 offset:24576\n" \
"buffer_load_dwordx4 v13, s[40:43], s65 offen offset:0 lds\n" \
"v_mul_f32 v192, v192, v200\n" \
"v_mfma_f32_16x16x32_bf16 v[144:147], v[80:83], v[56:59], v[144:147]\n" \
"ds_read_b64_tr_b16 v[102:103], v6 offset:28672\n" \
"v_mul_f32 v193, v193, v200\n" \
"v_mul_f32 v194, v194, v200\n" \
"v_mfma_f32_16x16x32_bf16 v[160:163], v[80:83], v[64:67], v[160:163]\n" \
"v_mul_f32 v195, v195, v200\n" \
"v_mul_f32 v196, v196, v200\n" \
"v_mul_f32 v197, v197, v200\n" \
"v_mfma_f32_16x16x32_bf16 v[160:163], v[84:87], v[68:71], v[160:163]\n" \
"s_mov_b32 s66, 1966208\n" \
"v_mul_f32 v198, v198, v200\n" \
"v_mul_f32 v199, v199, v200\n" \
"v_mfma_f32_16x16x32_bf16 v[176:179], v[84:87], v[76:79], v[176:179]\n" \
"buffer_load_dwordx4 v13, s[40:43], s66 offen offset:1024 lds\n" \
"v_cvt_pk_bf16_f32 v216, v192, v193\n" \
"v_cvt_pk_bf16_f32 v217, v194, v195\n" \
"v_mfma_f32_16x16x32_bf16 v[176:179], v[80:83], v[72:75], v[176:179]\n" \
"v_cvt_pk_bf16_f32 v218, v196, v197\n" \
"v_cvt_pk_bf16_f32 v219, v198, v199\n" \
"buffer_store_dwordx4 v[216:219], v15, s[96:99], 0 offen offset:0 sc1\n" \
"s_waitcnt lgkmcnt(4)\n" \
"v_mfma_f32_16x16x32_bf16 v[180:183], v[88:91], v[72:75], v[180:183]\n" \
"ds_read_b64_tr_b16 v[104:105], v7 offset:16384\n" \
"v_mfma_f32_16x16x32_bf16 v[192:195], v[224:227], v[248:251], 0\n" \
"v_mfma_f32_16x16x32_bf16 v[196:199], v[228:231], v[248:251], 0\n" \
"v_mfma_f32_16x16x32_bf16 v[180:183], v[92:95], v[76:79], v[180:183]\n" \
"ds_read_b64_tr_b16 v[106:107], v7 offset:20480\n" \
"s_mov_b32 s67, 1981440\n" \
"v_mfma_f32_16x16x32_bf16 v[200:203], v[232:235], v[248:251], 0\n" \
"v_mfma_f32_16x16x32_bf16 v[164:167], v[92:95], v[68:71], v[164:167]\n" \
"ds_read_b64_tr_b16 v[108:109], v7 offset:24576\n" \
"buffer_load_dwordx4 v13, s[40:43], s67 offen offset:2048 lds\n" \
"v_add_f32 v212, 0x358637bd, v212\n" \
"v_mfma_f32_16x16x32_bf16 v[164:167], v[88:91], v[64:67], v[164:167]\n" \
"ds_read_b64_tr_b16 v[110:111], v7 offset:28672\n" \
"v_rcp_f32 v212, v212\n" \
"s_nop 0\n" \
"v_mfma_f32_16x16x32_bf16 v[148:151], v[88:91], v[56:59], v[148:151]\n" \
"v_mul_f32 v204, v204, v212\n" \
"v_mul_f32 v205, v205, v212\n" \
"v_mul_f32 v206, v206, v212\n" \
"v_mfma_f32_16x16x32_bf16 v[148:151], v[92:95], v[60:63], v[148:151]\n" \
"s_mov_b32 s68, 1980544\n" \
"v_mul_f32 v207, v207, v212\n" \
"v_mul_f32 v208, v208, v212\n" \
"v_mfma_f32_16x16x32_bf16 v[132:135], v[92:95], v[52:55], v[132:135]\n" \
"buffer_load_dwordx4 v13, s[40:43], s68 offen offset:3072 lds\n" \
"v_mul_f32 v209, v209, v212\n" \
"v_mul_f32 v210, v210, v212\n" \
"v_mfma_f32_16x16x32_bf16 v[132:135], v[88:91], v[48:51], v[132:135]\n" \
"v_mul_f32 v211, v211, v212\n" \
"v_cvt_pk_bf16_f32 v220, v204, v205\n" \
"v_cvt_pk_bf16_f32 v221, v206, v207\n" \
"s_waitcnt vmcnt(21) lgkmcnt(0)\n" \
"s_barrier\n" \
"v_mfma_f32_16x16x32_bf16 v[136:139], v[96:99], v[48:51], v[136:139]\n" \
"ds_read_b64_tr_b16 v[80:81], v8 offset:0\n" \
"ds_read_b64_tr_b16 v[82:83], v8 offset:4096\n" \
"v_cvt_pk_bf16_f32 v222, v208, v209\n" \
"v_mfma_f32_16x16x32_bf16 v[136:139], v[100:103], v[52:55], v[136:139]\n" \
"ds_read_b64_tr_b16 v[84:85], v8 offset:8192\n" \
"ds_read_b64_tr_b16 v[86:87], v8 offset:12288\n" \
"v_cvt_pk_bf16_f32 v223, v210, v211\n" \
"v_mfma_f32_16x16x32_bf16 v[152:155], v[100:103], v[60:63], v[152:155]\n" \
"ds_read_b128 v[16:19], v1 offset:32768\n" \
"ds_read_b128 v[20:23], v1 offset:33792\n" \
"buffer_store_dwordx4 v[220:223], v15, s[96:99], 0 offen offset:2048 sc1\n" \
"v_mfma_f32_16x16x32_bf16 v[152:155], v[96:99], v[56:59], v[152:155]\n" \
"ds_read_b128 v[24:27], v1 offset:34816\n" \
"ds_read_b128 v[28:31], v1 offset:35840\n" \
"v_mfma_f32_16x16x32_bf16 v[204:207], v[224:227], v[252:255], 0\n" \
"v_mfma_f32_16x16x32_bf16 v[168:171], v[96:99], v[64:67], v[168:171]\n" \
"ds_read_b128 v[32:35], v3 offset:32768\n" \
"s_add_u32 m0, s70, 49152\n" \
"s_mov_b32 s64, 262144\n" \
"v_mfma_f32_16x16x32_bf16 v[168:171], v[100:103], v[68:71], v[168:171]\n" \
"ds_read_b128 v[36:39], v3 offset:33792\n" \
"buffer_load_dwordx4 v12, s[36:39], s64 offen offset:0 sc1 lds\n" \
"v_mfma_f32_16x16x32_bf16 v[208:211], v[228:231], v[252:255], 0\n" \
"v_mfma_f32_16x16x32_bf16 v[184:187], v[100:103], v[76:79], v[184:187]\n" \
"ds_read_b128 v[40:43], v3 offset:34816\n" \
"v_mfma_f32_16x16x32_bf16 v[212:215], v[232:235], v[252:255], 0\n" \
"v_add_f32 v200, 0x358637bd, v200\n" \
"v_mfma_f32_16x16x32_bf16 v[184:187], v[96:99], v[72:75], v[184:187]\n" \
"ds_read_b128 v[44:47], v3 offset:35840\n" \
"v_rcp_f32 v200, v200\n" \
"s_nop 0\n" \
"v_mfma_f32_16x16x32_bf16 v[188:191], v[104:107], v[72:75], v[188:191]\n" \
"ds_read_b64_tr_b16 v[88:89], v9 offset:0\n" \
"buffer_load_dwordx4 v12, s[36:39], s64 offen offset:1024 sc1 lds\n" \
"v_mul_f32 v192, v192, v200\n" \
"v_mfma_f32_16x16x32_bf16 v[188:191], v[108:111], v[76:79], v[188:191]\n" \
"ds_read_b64_tr_b16 v[90:91], v9 offset:4096\n" \
"v_mul_f32 v193, v193, v200\n" \
"v_mul_f32 v194, v194, v200\n" \
"v_mfma_f32_16x16x32_bf16 v[172:175], v[108:111], v[68:71], v[172:175]\n" \
"ds_read_b64_tr_b16 v[92:93], v9 offset:8192\n" \
"v_mul_f32 v195, v195, v200\n" \
"v_mul_f32 v196, v196, v200\n" \
"v_mfma_f32_16x16x32_bf16 v[172:175], v[104:107], v[64:67], v[172:175]\n" \
"ds_read_b64_tr_b16 v[94:95], v9 offset:12288\n" \
"buffer_load_dwordx4 v12, s[36:39], s64 offen offset:2048 sc1 lds\n" \
"v_mul_f32 v197, v197, v200\n" \
"v_mfma_f32_16x16x32_bf16 v[156:159], v[104:107], v[56:59], v[156:159]\n" \
"v_mul_f32 v198, v198, v200\n" \
"v_mul_f32 v199, v199, v200\n" \
"v_cvt_pk_bf16_f32 v216, v192, v193\n" \
"v_mfma_f32_16x16x32_bf16 v[156:159], v[108:111], v[60:63], v[156:159]\n" \
"v_cvt_pk_bf16_f32 v217, v194, v195\n" \
"v_cvt_pk_bf16_f32 v218, v196, v197\n" \
"v_cvt_pk_bf16_f32 v219, v198, v199\n" \
"v_mfma_f32_16x16x32_bf16 v[140:143], v[108:111], v[52:55], v[140:143]\n" \
"buffer_load_dwordx4 v12, s[36:39], s64 offen offset:3072 sc1 lds\n" \
"buffer_store_dwordx4 v[216:219], v15, s[96:99], s94 offen offset:0 sc1\n" \
"v_add_f32 v212, 0x358637bd, v212\n" \
"v_mfma_f32_16x16x32_bf16 v[140:143], v[104:107], v[48:51], v[140:143]\n" \
"v_rcp_f32 v212, v212\n" \
"s_nop 0\n" \
"v_mul_f32 v204, v204, v212\n" \
"s_waitcnt lgkmcnt(4)\n" \
"v_mfma_f32_16x16x32_bf16 v[128:131], v[80:83], v[16:19], v[128:131]\n" \
"ds_read_b64_tr_b16 v[96:97], v10 offset:0\n" \
"v_mul_f32 v205, v205, v212\n" \
"v_mul_f32 v206, v206, v212\n" \
"v_mfma_f32_16x16x32_bf16 v[128:131], v[84:87], v[20:23], v[128:131]\n" \
"ds_read_b64_tr_b16 v[98:99], v10 offset:4096\n" \
"s_add_u32 m0, s70, 16384\n" \
"s_mov_b32 s65, 2098176\n" \
"v_mfma_f32_16x16x32_bf16 v[144:147], v[84:87], v[28:31], v[144:147]\n" \
"ds_read_b64_tr_b16 v[100:101], v10 offset:8192\n" \
"buffer_load_dwordx4 v13, s[40:43], s65 offen offset:0 lds\n" \
"v_mul_f32 v207, v207, v212\n" \
"v_mfma_f32_16x16x32_bf16 v[144:147], v[80:83], v[24:27], v[144:147]\n" \
"ds_read_b64_tr_b16 v[102:103], v10 offset:12288\n" \
"v_mul_f32 v208, v208, v212\n" \
"v_mul_f32 v209, v209, v212\n" \
"v_mfma_f32_16x16x32_bf16 v[160:163], v[80:83], v[32:35], v[160:163]\n" \
"v_mul_f32 v210, v210, v212\n" \
"v_mul_f32 v211, v211, v212\n" \
"v_cvt_pk_bf16_f32 v220, v204, v205\n" \
"v_mfma_f32_16x16x32_bf16 v[160:163], v[84:87], v[36:39], v[160:163]\n" \
"s_mov_b32 s66, 2097280\n" \
"v_cvt_pk_bf16_f32 v221, v206, v207\n" \
"v_cvt_pk_bf16_f32 v222, v208, v209\n" \
"v_mfma_f32_16x16x32_bf16 v[176:179], v[84:87], v[44:47], v[176:179]\n" \
"buffer_load_dwordx4 v13, s[40:43], s66 offen offset:1024 lds\n" \
"v_cvt_pk_bf16_f32 v223, v210, v211\n" \
"buffer_store_dwordx4 v[220:223], v15, s[96:99], s94 offen offset:2048 sc1\n" \
"v_mfma_f32_16x16x32_bf16 v[176:179], v[80:83], v[40:43], v[176:179]\n" \
"s_waitcnt lgkmcnt(4)\n" \
"v_mfma_f32_16x16x32_bf16 v[180:183], v[88:91], v[40:43], v[180:183]\n" \
"ds_read_b64_tr_b16 v[104:105], v11 offset:0\n" \
"v_mfma_f32_16x16x32_bf16 v[180:183], v[92:95], v[44:47], v[180:183]\n" \
"ds_read_b64_tr_b16 v[106:107], v11 offset:4096\n" \
"s_mov_b32 s67, 2112512\n" \
"v_mfma_f32_16x16x32_bf16 v[164:167], v[92:95], v[36:39], v[164:167]\n" \
"ds_read_b64_tr_b16 v[108:109], v11 offset:8192\n" \
"buffer_load_dwordx4 v13, s[40:43], s67 offen offset:2048 lds\n" \
"v_mfma_f32_16x16x32_bf16 v[164:167], v[88:91], v[32:35], v[164:167]\n" \
"ds_read_b64_tr_b16 v[110:111], v11 offset:12288\n" \
"v_mfma_f32_16x16x32_bf16 v[148:151], v[88:91], v[24:27], v[148:151]\n" \
"v_mfma_f32_16x16x32_bf16 v[148:151], v[92:95], v[28:31], v[148:151]\n" \
"s_mov_b32 s68, 2111616\n" \
"v_mfma_f32_16x16x32_bf16 v[132:135], v[92:95], v[20:23], v[132:135]\n" \
"buffer_load_dwordx4 v13, s[40:43], s68 offen offset:3072 lds\n" \
"v_mfma_f32_16x16x32_bf16 v[132:135], v[88:91], v[16:19], v[132:135]\n" \
"s_waitcnt vmcnt(20) lgkmcnt(0)\n" \
"s_barrier\n" \
"v_mfma_f32_16x16x32_bf16 v[136:139], v[96:99], v[16:19], v[136:139]\n" \
"ds_read_b64_tr_b16 v[80:81], v8 offset:16384\n" \
"ds_read_b64_tr_b16 v[82:83], v8 offset:20480\n" \
"v_mfma_f32_16x16x32_bf16 v[136:139], v[100:103], v[20:23], v[136:139]\n" \
"ds_read_b64_tr_b16 v[84:85], v8 offset:24576\n" \
"ds_read_b64_tr_b16 v[86:87], v8 offset:28672\n" \
"v_mfma_f32_16x16x32_bf16 v[152:155], v[100:103], v[28:31], v[152:155]\n" \
"ds_read_b128 v[48:51], v1 offset:49152\n" \
"ds_read_b128 v[52:55], v1 offset:50176\n" \
"v_mfma_f32_16x16x32_bf16 v[152:155], v[96:99], v[24:27], v[152:155]\n" \
"ds_read_b128 v[56:59], v1 offset:51200\n" \
"ds_read_b128 v[60:63], v1 offset:52224\n" \
"v_mfma_f32_16x16x32_bf16 v[168:171], v[96:99], v[32:35], v[168:171]\n" \
"ds_read_b128 v[64:67], v3 offset:49152\n" \
"s_add_u32 m0, s70, 98304\n" \
"s_mov_b32 s64, 278528\n" \
"v_mfma_f32_16x16x32_bf16 v[168:171], v[100:103], v[36:39], v[168:171]\n" \
"ds_read_b128 v[68:71], v3 offset:50176\n" \
"buffer_load_dwordx4 v12, s[36:39], s64 offen offset:0 sc1 lds\n" \
"v_mfma_f32_16x16x32_bf16 v[184:187], v[100:103], v[44:47], v[184:187]\n" \
"ds_read_b128 v[72:75], v3 offset:51200\n" \
"v_mfma_f32_16x16x32_bf16 v[184:187], v[96:99], v[40:43], v[184:187]\n" \
"ds_read_b128 v[76:79], v3 offset:52224\n" \
"v_mfma_f32_16x16x32_bf16 v[188:191], v[104:107], v[40:43], v[188:191]\n" \
"ds_read_b64_tr_b16 v[88:89], v9 offset:16384\n" \
"buffer_load_dwordx4 v12, s[36:39], s64 offen offset:1024 sc1 lds\n" \
"v_mfma_f32_16x16x32_bf16 v[188:191], v[108:111], v[44:47], v[188:191]\n" \
"ds_read_b64_tr_b16 v[90:91], v9 offset:20480\n" \
"v_mfma_f32_16x16x32_bf16 v[172:175], v[108:111], v[36:39], v[172:175]\n" \
"ds_read_b64_tr_b16 v[92:93], v9 offset:24576\n" \
"v_mfma_f32_16x16x32_bf16 v[172:175], v[104:107], v[32:35], v[172:175]\n" \
"ds_read_b64_tr_b16 v[94:95], v9 offset:28672\n" \
"buffer_load_dwordx4 v12, s[36:39], s64 offen offset:2048 sc1 lds\n" \
"v_mfma_f32_16x16x32_bf16 v[156:159], v[104:107], v[24:27], v[156:159]\n" \
"v_mfma_f32_16x16x32_bf16 v[156:159], v[108:111], v[28:31], v[156:159]\n" \
"v_mfma_f32_16x16x32_bf16 v[140:143], v[108:111], v[20:23], v[140:143]\n" \
"buffer_load_dwordx4 v12, s[36:39], s64 offen offset:3072 sc1 lds\n" \
"v_mfma_f32_16x16x32_bf16 v[140:143], v[104:107], v[16:19], v[140:143]\n" \
"s_waitcnt lgkmcnt(4)\n" \
"v_mfma_f32_16x16x32_bf16 v[128:131], v[80:83], v[48:51], v[128:131]\n" \
"ds_read_b64_tr_b16 v[96:97], v10 offset:16384\n" \
"v_mfma_f32_16x16x32_bf16 v[128:131], v[84:87], v[52:55], v[128:131]\n" \
"ds_read_b64_tr_b16 v[98:99], v10 offset:20480\n" \
"s_add_u32 m0, s70, 65536\n" \
"s_mov_b32 s65, 2229248\n" \
"v_mfma_f32_16x16x32_bf16 v[144:147], v[84:87], v[60:63], v[144:147]\n" \
"ds_read_b64_tr_b16 v[100:101], v10 offset:24576\n" \
"buffer_load_dwordx4 v13, s[40:43], s65 offen offset:0 lds\n" \
"v_mfma_f32_16x16x32_bf16 v[144:147], v[80:83], v[56:59], v[144:147]\n" \
"ds_read_b64_tr_b16 v[102:103], v10 offset:28672\n" \
"v_mfma_f32_16x16x32_bf16 v[160:163], v[80:83], v[64:67], v[160:163]\n" \
"v_mfma_f32_16x16x32_bf16 v[160:163], v[84:87], v[68:71], v[160:163]\n" \
"s_mov_b32 s66, 2228352\n" \
"v_mfma_f32_16x16x32_bf16 v[176:179], v[84:87], v[76:79], v[176:179]\n" \
"buffer_load_dwordx4 v13, s[40:43], s66 offen offset:1024 lds\n" \
"v_mfma_f32_16x16x32_bf16 v[176:179], v[80:83], v[72:75], v[176:179]\n" \
"s_waitcnt lgkmcnt(4)\n" \
"v_mfma_f32_16x16x32_bf16 v[180:183], v[88:91], v[72:75], v[180:183]\n" \
"ds_read_b64_tr_b16 v[104:105], v11 offset:16384\n" \
"v_mfma_f32_16x16x32_bf16 v[180:183], v[92:95], v[76:79], v[180:183]\n" \
"ds_read_b64_tr_b16 v[106:107], v11 offset:20480\n" \
"s_mov_b32 s67, 2243584\n" \
"v_mfma_f32_16x16x32_bf16 v[164:167], v[92:95], v[68:71], v[164:167]\n" \
"ds_read_b64_tr_b16 v[108:109], v11 offset:24576\n" \
"buffer_load_dwordx4 v13, s[40:43], s67 offen offset:2048 lds\n" \
"v_mfma_f32_16x16x32_bf16 v[164:167], v[88:91], v[64:67], v[164:167]\n" \
"ds_read_b64_tr_b16 v[110:111], v11 offset:28672\n" \
"v_mfma_f32_16x16x32_bf16 v[148:151], v[88:91], v[56:59], v[148:151]\n" \
"v_mfma_f32_16x16x32_bf16 v[148:151], v[92:95], v[60:63], v[148:151]\n" \
"s_mov_b32 s68, 2242688\n" \
"v_mfma_f32_16x16x32_bf16 v[132:135], v[92:95], v[52:55], v[132:135]\n" \
"buffer_load_dwordx4 v13, s[40:43], s68 offen offset:3072 lds\n" \
"v_mfma_f32_16x16x32_bf16 v[132:135], v[88:91], v[48:51], v[132:135]\n" \
"s_waitcnt vmcnt(19) lgkmcnt(0)\n" \
"s_barrier\n" \
"v_mfma_f32_16x16x32_bf16 v[136:139], v[96:99], v[48:51], v[136:139]\n" \
"ds_read_b64_tr_b16 v[80:81], v4 offset:0\n" \
"ds_read_b64_tr_b16 v[82:83], v4 offset:4096\n" \
"v_mfma_f32_16x16x32_bf16 v[136:139], v[100:103], v[52:55], v[136:139]\n" \
"ds_read_b64_tr_b16 v[84:85], v4 offset:8192\n" \
"ds_read_b64_tr_b16 v[86:87], v4 offset:12288\n" \
"v_mfma_f32_16x16x32_bf16 v[152:155], v[100:103], v[60:63], v[152:155]\n" \
"ds_read_b128 v[16:19], v0 offset:32768\n" \
"ds_read_b128 v[20:23], v0 offset:33792\n" \
"v_mfma_f32_16x16x32_bf16 v[152:155], v[96:99], v[56:59], v[152:155]\n" \
"ds_read_b128 v[24:27], v0 offset:34816\n" \
"ds_read_b128 v[28:31], v0 offset:35840\n" \
"v_mfma_f32_16x16x32_bf16 v[168:171], v[96:99], v[64:67], v[168:171]\n" \
"ds_read_b128 v[32:35], v2 offset:32768\n" \
"s_add_u32 m0, s70, 114688\n" \
"s_mov_b32 s64, 294912\n" \
"v_mfma_f32_16x16x32_bf16 v[168:171], v[100:103], v[68:71], v[168:171]\n" \
"ds_read_b128 v[36:39], v2 offset:33792\n" \
"buffer_load_dwordx4 v12, s[36:39], s64 offen offset:0 sc1 lds\n" \
"v_mfma_f32_16x16x32_bf16 v[184:187], v[100:103], v[76:79], v[184:187]\n" \
"ds_read_b128 v[40:43], v2 offset:34816\n" \
"v_mfma_f32_16x16x32_bf16 v[184:187], v[96:99], v[72:75], v[184:187]\n" \
"ds_read_b128 v[44:47], v2 offset:35840\n" \
"v_mfma_f32_16x16x32_bf16 v[188:191], v[104:107], v[72:75], v[188:191]\n" \
"ds_read_b64_tr_b16 v[88:89], v5 offset:0\n" \
"buffer_load_dwordx4 v12, s[36:39], s64 offen offset:1024 sc1 lds\n" \
"v_mfma_f32_16x16x32_bf16 v[188:191], v[108:111], v[76:79], v[188:191]\n" \
"ds_read_b64_tr_b16 v[90:91], v5 offset:4096\n" \
"v_mfma_f32_16x16x32_bf16 v[172:175], v[108:111], v[68:71], v[172:175]\n" \
"ds_read_b64_tr_b16 v[92:93], v5 offset:8192\n" \
"v_mfma_f32_16x16x32_bf16 v[172:175], v[104:107], v[64:67], v[172:175]\n" \
"ds_read_b64_tr_b16 v[94:95], v5 offset:12288\n" \
"buffer_load_dwordx4 v12, s[36:39], s64 offen offset:2048 sc1 lds\n" \
"v_mfma_f32_16x16x32_bf16 v[156:159], v[104:107], v[56:59], v[156:159]\n" \
"v_mfma_f32_16x16x32_bf16 v[156:159], v[108:111], v[60:63], v[156:159]\n" \
"v_mfma_f32_16x16x32_bf16 v[140:143], v[108:111], v[52:55], v[140:143]\n" \
"buffer_load_dwordx4 v12, s[36:39], s64 offen offset:3072 sc1 lds\n" \
"v_mfma_f32_16x16x32_bf16 v[140:143], v[104:107], v[48:51], v[140:143]\n" \
"s_waitcnt lgkmcnt(4)\n" \
"v_mfma_f32_16x16x32_bf16 v[128:131], v[80:83], v[16:19], v[128:131]\n" \
"ds_read_b64_tr_b16 v[96:97], v6 offset:0\n" \
"v_mfma_f32_16x16x32_bf16 v[128:131], v[84:87], v[20:23], v[128:131]\n" \
"ds_read_b64_tr_b16 v[98:99], v6 offset:4096\n" \
"s_add_u32 m0, s70, 81920\n" \
"s_mov_b32 s65, 2360320\n" \
"v_mfma_f32_16x16x32_bf16 v[144:147], v[84:87], v[28:31], v[144:147]\n" \
"ds_read_b64_tr_b16 v[100:101], v6 offset:8192\n" \
"buffer_load_dwordx4 v13, s[40:43], s65 offen offset:0 lds\n" \
"v_mfma_f32_16x16x32_bf16 v[144:147], v[80:83], v[24:27], v[144:147]\n" \
"ds_read_b64_tr_b16 v[102:103], v6 offset:12288\n" \
"v_mfma_f32_16x16x32_bf16 v[160:163], v[80:83], v[32:35], v[160:163]\n" \
"v_mfma_f32_16x16x32_bf16 v[160:163], v[84:87], v[36:39], v[160:163]\n" \
"s_mov_b32 s66, 2359424\n" \
"v_mfma_f32_16x16x32_bf16 v[176:179], v[84:87], v[44:47], v[176:179]\n" \
"buffer_load_dwordx4 v13, s[40:43], s66 offen offset:1024 lds\n" \
"v_mfma_f32_16x16x32_bf16 v[176:179], v[80:83], v[40:43], v[176:179]\n" \
"s_waitcnt lgkmcnt(4)\n" \
"v_mfma_f32_16x16x32_bf16 v[180:183], v[88:91], v[40:43], v[180:183]\n" \
"ds_read_b64_tr_b16 v[104:105], v7 offset:0\n" \
"v_mfma_f32_16x16x32_bf16 v[180:183], v[92:95], v[44:47], v[180:183]\n" \
"ds_read_b64_tr_b16 v[106:107], v7 offset:4096\n" \
"s_mov_b32 s67, 2374656\n" \
"v_mfma_f32_16x16x32_bf16 v[164:167], v[92:95], v[36:39], v[164:167]\n" \
"ds_read_b64_tr_b16 v[108:109], v7 offset:8192\n" \
"buffer_load_dwordx4 v13, s[40:43], s67 offen offset:2048 lds\n" \
"v_mfma_f32_16x16x32_bf16 v[164:167], v[88:91], v[32:35], v[164:167]\n" \
"ds_read_b64_tr_b16 v[110:111], v7 offset:12288\n" \
"v_mfma_f32_16x16x32_bf16 v[148:151], v[88:91], v[24:27], v[148:151]\n" \
"v_mfma_f32_16x16x32_bf16 v[148:151], v[92:95], v[28:31], v[148:151]\n" \
"s_mov_b32 s68, 2373760\n" \
"v_mfma_f32_16x16x32_bf16 v[132:135], v[92:95], v[20:23], v[132:135]\n" \
"buffer_load_dwordx4 v13, s[40:43], s68 offen offset:3072 lds\n" \
"v_mfma_f32_16x16x32_bf16 v[132:135], v[88:91], v[16:19], v[132:135]\n" \
"s_waitcnt vmcnt(16) lgkmcnt(0)\n" \
"s_barrier\n" \
"v_mfma_f32_16x16x32_bf16 v[136:139], v[96:99], v[16:19], v[136:139]\n" \
"ds_read_b64_tr_b16 v[80:81], v4 offset:16384\n" \
"ds_read_b64_tr_b16 v[82:83], v4 offset:20480\n" \
"v_mfma_f32_16x16x32_bf16 v[136:139], v[100:103], v[20:23], v[136:139]\n" \
"ds_read_b64_tr_b16 v[84:85], v4 offset:24576\n" \
"ds_read_b64_tr_b16 v[86:87], v4 offset:28672\n" \
"v_mfma_f32_16x16x32_bf16 v[152:155], v[100:103], v[28:31], v[152:155]\n" \
"ds_read_b128 v[48:51], v0 offset:49152\n" \
"ds_read_b128 v[52:55], v0 offset:50176\n" \
"v_mfma_f32_16x16x32_bf16 v[152:155], v[96:99], v[24:27], v[152:155]\n" \
"ds_read_b128 v[56:59], v0 offset:51200\n" \
"ds_read_b128 v[60:63], v0 offset:52224\n" \
"v_mfma_f32_16x16x32_bf16 v[168:171], v[96:99], v[32:35], v[168:171]\n" \
"ds_read_b128 v[64:67], v2 offset:49152\n" \
"s_add_u32 m0, s70, 32768\n" \
"s_sub_u32 s64, 655360, s73\n" \
"v_mfma_f32_16x16x32_bf16 v[168:171], v[100:103], v[36:39], v[168:171]\n" \
"ds_read_b128 v[68:71], v2 offset:50176\n" \
"buffer_load_dwordx4 v12, s[36:39], s64 offen offset:0 sc1 lds\n" \
"v_mfma_f32_16x16x32_bf16 v[184:187], v[100:103], v[44:47], v[184:187]\n" \
"ds_read_b128 v[72:75], v2 offset:51200\n" \
"v_mfma_f32_16x16x32_bf16 v[184:187], v[96:99], v[40:43], v[184:187]\n" \
"ds_read_b128 v[76:79], v2 offset:52224\n" \
"v_mfma_f32_16x16x32_bf16 v[188:191], v[104:107], v[40:43], v[188:191]\n" \
"ds_read_b64_tr_b16 v[88:89], v5 offset:16384\n" \
"buffer_load_dwordx4 v12, s[36:39], s64 offen offset:1024 sc1 lds\n" \
"v_mfma_f32_16x16x32_bf16 v[188:191], v[108:111], v[44:47], v[188:191]\n" \
"ds_read_b64_tr_b16 v[90:91], v5 offset:20480\n" \
"v_mfma_f32_16x16x32_bf16 v[172:175], v[108:111], v[36:39], v[172:175]\n" \
"ds_read_b64_tr_b16 v[92:93], v5 offset:24576\n" \
"v_mfma_f32_16x16x32_bf16 v[172:175], v[104:107], v[32:35], v[172:175]\n" \
"ds_read_b64_tr_b16 v[94:95], v5 offset:28672\n" \
"buffer_load_dwordx4 v12, s[36:39], s64 offen offset:2048 sc1 lds\n" \
"v_mfma_f32_16x16x32_bf16 v[156:159], v[104:107], v[24:27], v[156:159]\n" \
"v_mfma_f32_16x16x32_bf16 v[156:159], v[108:111], v[28:31], v[156:159]\n" \
"v_mfma_f32_16x16x32_bf16 v[140:143], v[108:111], v[20:23], v[140:143]\n" \
"buffer_load_dwordx4 v12, s[36:39], s64 offen offset:3072 sc1 lds\n" \
"v_mfma_f32_16x16x32_bf16 v[140:143], v[104:107], v[16:19], v[140:143]\n" \
"s_waitcnt lgkmcnt(4)\n" \
"v_mfma_f32_16x16x32_bf16 v[128:131], v[80:83], v[48:51], v[128:131]\n" \
"ds_read_b64_tr_b16 v[96:97], v6 offset:16384\n" \
"v_mfma_f32_16x16x32_bf16 v[128:131], v[84:87], v[52:55], v[128:131]\n" \
"ds_read_b64_tr_b16 v[98:99], v6 offset:20480\n" \
"s_add_u32 m0, s70, 0\n" \
"s_sub_u32 s65, 5243904, s75\n" \
"v_mfma_f32_16x16x32_bf16 v[144:147], v[84:87], v[60:63], v[144:147]\n" \
"ds_read_b64_tr_b16 v[100:101], v6 offset:24576\n" \
"buffer_load_dwordx4 v13, s[40:43], s65 offen offset:0 lds\n" \
"v_mfma_f32_16x16x32_bf16 v[144:147], v[80:83], v[56:59], v[144:147]\n" \
"ds_read_b64_tr_b16 v[102:103], v6 offset:28672\n" \
"v_mfma_f32_16x16x32_bf16 v[160:163], v[80:83], v[64:67], v[160:163]\n" \
"v_mfma_f32_16x16x32_bf16 v[160:163], v[84:87], v[68:71], v[160:163]\n" \
"s_sub_u32 s66, 5243008, s75\n" \
"v_mfma_f32_16x16x32_bf16 v[176:179], v[84:87], v[76:79], v[176:179]\n" \
"buffer_load_dwordx4 v13, s[40:43], s66 offen offset:1024 lds\n" \
"v_mfma_f32_16x16x32_bf16 v[176:179], v[80:83], v[72:75], v[176:179]\n" \
"s_waitcnt lgkmcnt(4)\n" \
"v_mfma_f32_16x16x32_bf16 v[180:183], v[88:91], v[72:75], v[180:183]\n" \
"ds_read_b64_tr_b16 v[104:105], v7 offset:16384\n" \
"v_mfma_f32_16x16x32_bf16 v[180:183], v[92:95], v[76:79], v[180:183]\n" \
"ds_read_b64_tr_b16 v[106:107], v7 offset:20480\n" \
"s_sub_u32 s67, 5258240, s75\n" \
"v_mfma_f32_16x16x32_bf16 v[164:167], v[92:95], v[68:71], v[164:167]\n" \
"ds_read_b64_tr_b16 v[108:109], v7 offset:24576\n" \
"buffer_load_dwordx4 v13, s[40:43], s67 offen offset:2048 lds\n" \
"v_mfma_f32_16x16x32_bf16 v[164:167], v[88:91], v[64:67], v[164:167]\n" \
"ds_read_b64_tr_b16 v[110:111], v7 offset:28672\n" \
"v_mfma_f32_16x16x32_bf16 v[148:151], v[88:91], v[56:59], v[148:151]\n" \
"v_mfma_f32_16x16x32_bf16 v[148:151], v[92:95], v[60:63], v[148:151]\n" \
"s_sub_u32 s68, 5257344, s75\n" \
"v_mfma_f32_16x16x32_bf16 v[132:135], v[92:95], v[52:55], v[132:135]\n" \
"buffer_load_dwordx4 v13, s[40:43], s68 offen offset:3072 lds\n" \
"v_mfma_f32_16x16x32_bf16 v[132:135], v[88:91], v[48:51], v[132:135]\n" \
"s_waitcnt vmcnt(16) lgkmcnt(0)\n" \
"s_barrier\n" \
"buffer_store_dwordx2 v[120:121], v118, s[80:83], 0 offen sc1\n" \
"v_mfma_f32_16x16x32_bf16 v[136:139], v[96:99], v[48:51], v[136:139]\n" \
"ds_read_b64_tr_b16 v[80:81], v8 offset:0\n" \
"ds_read_b64_tr_b16 v[82:83], v8 offset:4096\n" \
"v_mfma_f32_16x16x32_bf16 v[136:139], v[100:103], v[52:55], v[136:139]\n" \
"ds_read_b64_tr_b16 v[84:85], v8 offset:8192\n" \
"ds_read_b64_tr_b16 v[86:87], v8 offset:12288\n" \
"v_mfma_f32_16x16x32_bf16 v[152:155], v[100:103], v[60:63], v[152:155]\n" \
"ds_read_b128 v[16:19], v1 offset:32768\n" \
"ds_read_b128 v[20:23], v1 offset:33792\n" \
"v_mfma_f32_16x16x32_bf16 v[152:155], v[96:99], v[56:59], v[152:155]\n" \
"ds_read_b128 v[24:27], v1 offset:34816\n" \
"ds_read_b128 v[28:31], v1 offset:35840\n" \
"v_mfma_f32_16x16x32_bf16 v[168:171], v[96:99], v[64:67], v[168:171]\n" \
"ds_read_b128 v[32:35], v3 offset:32768\n" \
"s_add_u32 m0, s70, 49152\n" \
"s_sub_u32 s64, 671744, s73\n" \
"v_mfma_f32_16x16x32_bf16 v[168:171], v[100:103], v[68:71], v[168:171]\n" \
"ds_read_b128 v[36:39], v3 offset:33792\n" \
"buffer_load_dwordx4 v12, s[36:39], s64 offen offset:0 sc1 lds\n" \
"v_mfma_f32_16x16x32_bf16 v[184:187], v[100:103], v[76:79], v[184:187]\n" \
"ds_read_b128 v[40:43], v3 offset:34816\n" \
"v_mfma_f32_16x16x32_bf16 v[184:187], v[96:99], v[72:75], v[184:187]\n" \
"ds_read_b128 v[44:47], v3 offset:35840\n" \
"v_mfma_f32_16x16x32_bf16 v[188:191], v[104:107], v[72:75], v[188:191]\n" \
"ds_read_b64_tr_b16 v[88:89], v9 offset:0\n" \
"buffer_load_dwordx4 v12, s[36:39], s64 offen offset:1024 sc1 lds\n" \
"v_mfma_f32_16x16x32_bf16 v[188:191], v[108:111], v[76:79], v[188:191]\n" \
"ds_read_b64_tr_b16 v[90:91], v9 offset:4096\n" \
"v_mfma_f32_16x16x32_bf16 v[172:175], v[108:111], v[68:71], v[172:175]\n" \
"ds_read_b64_tr_b16 v[92:93], v9 offset:8192\n" \
"v_mfma_f32_16x16x32_bf16 v[172:175], v[104:107], v[64:67], v[172:175]\n" \
"ds_read_b64_tr_b16 v[94:95], v9 offset:12288\n" \
"buffer_load_dwordx4 v12, s[36:39], s64 offen offset:2048 sc1 lds\n" \
"v_mfma_f32_16x16x32_bf16 v[156:159], v[104:107], v[56:59], v[156:159]\n" \
"v_mfma_f32_16x16x32_bf16 v[156:159], v[108:111], v[60:63], v[156:159]\n" \
"v_mfma_f32_16x16x32_bf16 v[140:143], v[108:111], v[52:55], v[140:143]\n" \
"buffer_load_dwordx4 v12, s[36:39], s64 offen offset:3072 sc1 lds\n" \
"v_mfma_f32_16x16x32_bf16 v[140:143], v[104:107], v[48:51], v[140:143]\n" \
"s_waitcnt lgkmcnt(4)\n" \
"v_mfma_f32_16x16x32_bf16 v[128:131], v[80:83], v[16:19], v[128:131]\n" \
"ds_read_b64_tr_b16 v[96:97], v10 offset:0\n" \
"v_mfma_f32_16x16x32_bf16 v[128:131], v[84:87], v[20:23], v[128:131]\n" \
"ds_read_b64_tr_b16 v[98:99], v10 offset:4096\n" \
"s_add_u32 m0, s70, 16384\n" \
"s_sub_u32 s65, 5374976, s75\n" \
"v_mfma_f32_16x16x32_bf16 v[144:147], v[84:87], v[28:31], v[144:147]\n" \
"ds_read_b64_tr_b16 v[100:101], v10 offset:8192\n" \
"buffer_load_dwordx4 v13, s[40:43], s65 offen offset:0 lds\n" \
"v_mfma_f32_16x16x32_bf16 v[144:147], v[80:83], v[24:27], v[144:147]\n" \
"ds_read_b64_tr_b16 v[102:103], v10 offset:12288\n" \
"v_mfma_f32_16x16x32_bf16 v[160:163], v[80:83], v[32:35], v[160:163]\n" \
"v_mfma_f32_16x16x32_bf16 v[160:163], v[84:87], v[36:39], v[160:163]\n" \
"s_sub_u32 s66, 5374080, s75\n" \
"v_mfma_f32_16x16x32_bf16 v[176:179], v[84:87], v[44:47], v[176:179]\n" \
"buffer_load_dwordx4 v13, s[40:43], s66 offen offset:1024 lds\n" \
"v_mfma_f32_16x16x32_bf16 v[176:179], v[80:83], v[40:43], v[176:179]\n" \
"s_waitcnt lgkmcnt(4)\n" \
"v_mfma_f32_16x16x32_bf16 v[180:183], v[88:91], v[40:43], v[180:183]\n" \
"ds_read_b64_tr_b16 v[104:105], v11 offset:0\n" \
"v_mfma_f32_16x16x32_bf16 v[180:183], v[92:95], v[44:47], v[180:183]\n" \
"ds_read_b64_tr_b16 v[106:107], v11 offset:4096\n" \
"s_sub_u32 s67, 5389312, s75\n" \
"v_mfma_f32_16x16x32_bf16 v[164:167], v[92:95], v[36:39], v[164:167]\n" \
"ds_read_b64_tr_b16 v[108:109], v11 offset:8192\n" \
"buffer_load_dwordx4 v13, s[40:43], s67 offen offset:2048 lds\n" \
"v_mfma_f32_16x16x32_bf16 v[164:167], v[88:91], v[32:35], v[164:167]\n" \
"ds_read_b64_tr_b16 v[110:111], v11 offset:12288\n" \
"v_mfma_f32_16x16x32_bf16 v[148:151], v[88:91], v[24:27], v[148:151]\n" \
"v_mfma_f32_16x16x32_bf16 v[148:151], v[92:95], v[28:31], v[148:151]\n" \
"s_sub_u32 s68, 5388416, s75\n" \
"v_mfma_f32_16x16x32_bf16 v[132:135], v[92:95], v[20:23], v[132:135]\n" \
"buffer_load_dwordx4 v13, s[40:43], s68 offen offset:3072 lds\n" \
"v_mfma_f32_16x16x32_bf16 v[132:135], v[88:91], v[16:19], v[132:135]\n" \
"s_waitcnt vmcnt(17) lgkmcnt(0)\n" \
"s_barrier\n" \
"v_mfma_f32_16x16x32_bf16 v[136:139], v[96:99], v[16:19], v[136:139]\n" \
"ds_read_b64_tr_b16 v[80:81], v8 offset:16384\n" \
"ds_read_b64_tr_b16 v[82:83], v8 offset:20480\n" \
"v_mfma_f32_16x16x32_bf16 v[136:139], v[100:103], v[20:23], v[136:139]\n" \
"ds_read_b64_tr_b16 v[84:85], v8 offset:24576\n" \
"ds_read_b64_tr_b16 v[86:87], v8 offset:28672\n" \
"v_mfma_f32_16x16x32_bf16 v[152:155], v[100:103], v[28:31], v[152:155]\n" \
"ds_read_b128 v[48:51], v1 offset:49152\n" \
"ds_read_b128 v[52:55], v1 offset:50176\n" \
"v_mfma_f32_16x16x32_bf16 v[152:155], v[96:99], v[24:27], v[152:155]\n" \
"ds_read_b128 v[56:59], v1 offset:51200\n" \
"ds_read_b128 v[60:63], v1 offset:52224\n" \
"v_mfma_f32_16x16x32_bf16 v[168:171], v[96:99], v[32:35], v[168:171]\n" \
"ds_read_b128 v[64:67], v3 offset:49152\n" \
"s_add_u32 m0, s70, 98304\n" \
"s_sub_u32 s64, 688128, s73\n" \
"v_mfma_f32_16x16x32_bf16 v[168:171], v[100:103], v[36:39], v[168:171]\n" \
"ds_read_b128 v[68:71], v3 offset:50176\n" \
"buffer_load_dwordx4 v12, s[36:39], s64 offen offset:0 sc1 lds\n" \
"v_mfma_f32_16x16x32_bf16 v[184:187], v[100:103], v[44:47], v[184:187]\n" \
"ds_read_b128 v[72:75], v3 offset:51200\n" \
"v_mfma_f32_16x16x32_bf16 v[184:187], v[96:99], v[40:43], v[184:187]\n" \
"ds_read_b128 v[76:79], v3 offset:52224\n" \
"v_mfma_f32_16x16x32_bf16 v[188:191], v[104:107], v[40:43], v[188:191]\n" \
"ds_read_b64_tr_b16 v[88:89], v9 offset:16384\n" \
"buffer_load_dwordx4 v12, s[36:39], s64 offen offset:1024 sc1 lds\n" \
"v_mfma_f32_16x16x32_bf16 v[188:191], v[108:111], v[44:47], v[188:191]\n" \
"ds_read_b64_tr_b16 v[90:91], v9 offset:20480\n" \
"v_mfma_f32_16x16x32_bf16 v[172:175], v[108:111], v[36:39], v[172:175]\n" \
"ds_read_b64_tr_b16 v[92:93], v9 offset:24576\n" \
"v_mfma_f32_16x16x32_bf16 v[172:175], v[104:107], v[32:35], v[172:175]\n" \
"ds_read_b64_tr_b16 v[94:95], v9 offset:28672\n" \
"buffer_load_dwordx4 v12, s[36:39], s64 offen offset:2048 sc1 lds\n" \
"v_mfma_f32_16x16x32_bf16 v[156:159], v[104:107], v[24:27], v[156:159]\n" \
"v_mfma_f32_16x16x32_bf16 v[156:159], v[108:111], v[28:31], v[156:159]\n" \
"v_mfma_f32_16x16x32_bf16 v[140:143], v[108:111], v[20:23], v[140:143]\n" \
"buffer_load_dwordx4 v12, s[36:39], s64 offen offset:3072 sc1 lds\n" \
"v_mfma_f32_16x16x32_bf16 v[140:143], v[104:107], v[16:19], v[140:143]\n" \
"s_waitcnt lgkmcnt(4)\n" \
"v_mfma_f32_16x16x32_bf16 v[128:131], v[80:83], v[48:51], v[128:131]\n" \
"ds_read_b64_tr_b16 v[96:97], v10 offset:16384\n" \
"v_mfma_f32_16x16x32_bf16 v[128:131], v[84:87], v[52:55], v[128:131]\n" \
"ds_read_b64_tr_b16 v[98:99], v10 offset:20480\n" \
"s_add_u32 m0, s70, 65536\n" \
"s_sub_u32 s65, 5506048, s75\n" \
"v_mfma_f32_16x16x32_bf16 v[144:147], v[84:87], v[60:63], v[144:147]\n" \
"ds_read_b64_tr_b16 v[100:101], v10 offset:24576\n" \
"buffer_load_dwordx4 v13, s[40:43], s65 offen offset:0 lds\n" \
"v_mfma_f32_16x16x32_bf16 v[144:147], v[80:83], v[56:59], v[144:147]\n" \
"ds_read_b64_tr_b16 v[102:103], v10 offset:28672\n" \
"v_mfma_f32_16x16x32_bf16 v[160:163], v[80:83], v[64:67], v[160:163]\n" \
"v_mfma_f32_16x16x32_bf16 v[160:163], v[84:87], v[68:71], v[160:163]\n" \
"s_sub_u32 s66, 5505152, s75\n" \
"v_mfma_f32_16x16x32_bf16 v[176:179], v[84:87], v[76:79], v[176:179]\n" \
"buffer_load_dwordx4 v13, s[40:43], s66 offen offset:1024 lds\n" \
"v_mfma_f32_16x16x32_bf16 v[176:179], v[80:83], v[72:75], v[176:179]\n" \
"s_waitcnt lgkmcnt(4)\n" \
"v_mfma_f32_16x16x32_bf16 v[180:183], v[88:91], v[72:75], v[180:183]\n" \
"ds_read_b64_tr_b16 v[104:105], v11 offset:16384\n" \
"v_mfma_f32_16x16x32_bf16 v[180:183], v[92:95], v[76:79], v[180:183]\n" \
"ds_read_b64_tr_b16 v[106:107], v11 offset:20480\n" \
"s_sub_u32 s67, 5520384, s75\n" \
"v_mfma_f32_16x16x32_bf16 v[164:167], v[92:95], v[68:71], v[164:167]\n" \
"ds_read_b64_tr_b16 v[108:109], v11 offset:24576\n" \
"buffer_load_dwordx4 v13, s[40:43], s67 offen offset:2048 lds\n" \
"v_mfma_f32_16x16x32_bf16 v[164:167], v[88:91], v[64:67], v[164:167]\n" \
"ds_read_b64_tr_b16 v[110:111], v11 offset:28672\n" \
"v_mfma_f32_16x16x32_bf16 v[148:151], v[88:91], v[56:59], v[148:151]\n" \
"v_mfma_f32_16x16x32_bf16 v[148:151], v[92:95], v[60:63], v[148:151]\n" \
"s_sub_u32 s68, 5519488, s75\n" \
"v_mfma_f32_16x16x32_bf16 v[132:135], v[92:95], v[52:55], v[132:135]\n" \
"buffer_load_dwordx4 v13, s[40:43], s68 offen offset:3072 lds\n" \
"v_mfma_f32_16x16x32_bf16 v[132:135], v[88:91], v[48:51], v[132:135]\n" \
"s_waitcnt vmcnt(17) lgkmcnt(0)\n" \
"s_barrier\n" \
"v_mfma_f32_16x16x32_bf16 v[136:139], v[96:99], v[48:51], v[136:139]\n" \
"ds_read_b64_tr_b16 v[80:81], v4 offset:0\n" \
"ds_read_b64_tr_b16 v[82:83], v4 offset:4096\n" \
"v_mfma_f32_16x16x32_bf16 v[136:139], v[100:103], v[52:55], v[136:139]\n" \
"ds_read_b64_tr_b16 v[84:85], v4 offset:8192\n" \
"ds_read_b64_tr_b16 v[86:87], v4 offset:12288\n" \
"v_mfma_f32_16x16x32_bf16 v[152:155], v[100:103], v[60:63], v[152:155]\n" \
"ds_read_b128 v[16:19], v0 offset:32768\n" \
"ds_read_b128 v[20:23], v0 offset:33792\n" \
"v_mfma_f32_16x16x32_bf16 v[152:155], v[96:99], v[56:59], v[152:155]\n" \
"ds_read_b128 v[24:27], v0 offset:34816\n" \
"ds_read_b128 v[28:31], v0 offset:35840\n" \
"v_mfma_f32_16x16x32_bf16 v[168:171], v[96:99], v[64:67], v[168:171]\n" \
"ds_read_b128 v[32:35], v2 offset:32768\n" \
"s_add_u32 m0, s70, 114688\n" \
"s_sub_u32 s64, 704512, s73\n" \
"v_mfma_f32_16x16x32_bf16 v[168:171], v[100:103], v[68:71], v[168:171]\n" \
"ds_read_b128 v[36:39], v2 offset:33792\n" \
"buffer_load_dwordx4 v12, s[36:39], s64 offen offset:0 sc1 lds\n" \
"v_mfma_f32_16x16x32_bf16 v[184:187], v[100:103], v[76:79], v[184:187]\n" \
"ds_read_b128 v[40:43], v2 offset:34816\n" \
"v_mfma_f32_16x16x32_bf16 v[184:187], v[96:99], v[72:75], v[184:187]\n" \
"ds_read_b128 v[44:47], v2 offset:35840\n" \
"v_mfma_f32_16x16x32_bf16 v[188:191], v[104:107], v[72:75], v[188:191]\n" \
"ds_read_b64_tr_b16 v[88:89], v5 offset:0\n" \
"buffer_load_dwordx4 v12, s[36:39], s64 offen offset:1024 sc1 lds\n" \
"v_mfma_f32_16x16x32_bf16 v[188:191], v[108:111], v[76:79], v[188:191]\n" \
"ds_read_b64_tr_b16 v[90:91], v5 offset:4096\n" \
"v_mfma_f32_16x16x32_bf16 v[172:175], v[108:111], v[68:71], v[172:175]\n" \
"ds_read_b64_tr_b16 v[92:93], v5 offset:8192\n" \
"v_mfma_f32_16x16x32_bf16 v[172:175], v[104:107], v[64:67], v[172:175]\n" \
"ds_read_b64_tr_b16 v[94:95], v5 offset:12288\n" \
"buffer_load_dwordx4 v12, s[36:39], s64 offen offset:2048 sc1 lds\n" \
"v_mfma_f32_16x16x32_bf16 v[156:159], v[104:107], v[56:59], v[156:159]\n" \
"v_mfma_f32_16x16x32_bf16 v[156:159], v[108:111], v[60:63], v[156:159]\n" \
"v_mfma_f32_16x16x32_bf16 v[140:143], v[108:111], v[52:55], v[140:143]\n" \
"buffer_load_dwordx4 v12, s[36:39], s64 offen offset:3072 sc1 lds\n" \
"v_mfma_f32_16x16x32_bf16 v[140:143], v[104:107], v[48:51], v[140:143]\n" \
"s_waitcnt lgkmcnt(4)\n" \
"v_mfma_f32_16x16x32_bf16 v[128:131], v[80:83], v[16:19], v[128:131]\n" \
"ds_read_b64_tr_b16 v[96:97], v6 offset:0\n" \
"v_mfma_f32_16x16x32_bf16 v[128:131], v[84:87], v[20:23], v[128:131]\n" \
"ds_read_b64_tr_b16 v[98:99], v6 offset:4096\n" \
"s_add_u32 m0, s70, 81920\n" \
"s_sub_u32 s65, 5637120, s75\n" \
"v_mfma_f32_16x16x32_bf16 v[144:147], v[84:87], v[28:31], v[144:147]\n" \
"ds_read_b64_tr_b16 v[100:101], v6 offset:8192\n" \
"buffer_load_dwordx4 v13, s[40:43], s65 offen offset:0 lds\n" \
"v_mfma_f32_16x16x32_bf16 v[144:147], v[80:83], v[24:27], v[144:147]\n" \
"ds_read_b64_tr_b16 v[102:103], v6 offset:12288\n" \
"v_mfma_f32_16x16x32_bf16 v[160:163], v[80:83], v[32:35], v[160:163]\n" \
"v_mfma_f32_16x16x32_bf16 v[160:163], v[84:87], v[36:39], v[160:163]\n" \
"s_sub_u32 s66, 5636224, s75\n" \
"v_mfma_f32_16x16x32_bf16 v[176:179], v[84:87], v[44:47], v[176:179]\n" \
"buffer_load_dwordx4 v13, s[40:43], s66 offen offset:1024 lds\n" \
"v_mfma_f32_16x16x32_bf16 v[176:179], v[80:83], v[40:43], v[176:179]\n" \
"s_waitcnt lgkmcnt(4)\n" \
"v_mfma_f32_16x16x32_bf16 v[180:183], v[88:91], v[40:43], v[180:183]\n" \
"ds_read_b64_tr_b16 v[104:105], v7 offset:0\n" \
"v_mfma_f32_16x16x32_bf16 v[180:183], v[92:95], v[44:47], v[180:183]\n" \
"ds_read_b64_tr_b16 v[106:107], v7 offset:4096\n" \
"s_sub_u32 s67, 5651456, s75\n" \
"v_mfma_f32_16x16x32_bf16 v[164:167], v[92:95], v[36:39], v[164:167]\n" \
"ds_read_b64_tr_b16 v[108:109], v7 offset:8192\n" \
"buffer_load_dwordx4 v13, s[40:43], s67 offen offset:2048 lds\n" \
"v_mfma_f32_16x16x32_bf16 v[164:167], v[88:91], v[32:35], v[164:167]\n" \
"ds_read_b64_tr_b16 v[110:111], v7 offset:12288\n" \
"v_mfma_f32_16x16x32_bf16 v[148:151], v[88:91], v[24:27], v[148:151]\n" \
"v_mfma_f32_16x16x32_bf16 v[148:151], v[92:95], v[28:31], v[148:151]\n" \
"s_sub_u32 s68, 5650560, s75\n" \
"v_mfma_f32_16x16x32_bf16 v[132:135], v[92:95], v[20:23], v[132:135]\n" \
"buffer_load_dwordx4 v13, s[40:43], s68 offen offset:3072 lds\n" \
"v_mfma_f32_16x16x32_bf16 v[132:135], v[88:91], v[16:19], v[132:135]\n" \
"s_waitcnt vmcnt(16) lgkmcnt(0)\n" \
"s_barrier\n" \
"v_mfma_f32_16x16x32_bf16 v[136:139], v[96:99], v[16:19], v[136:139]\n" \
"ds_read_b64_tr_b16 v[80:81], v4 offset:16384\n" \
"ds_read_b64_tr_b16 v[82:83], v4 offset:20480\n" \
"v_mfma_f32_16x16x32_bf16 v[136:139], v[100:103], v[20:23], v[136:139]\n" \
"ds_read_b64_tr_b16 v[84:85], v4 offset:24576\n" \
"ds_read_b64_tr_b16 v[86:87], v4 offset:28672\n" \
"v_mfma_f32_16x16x32_bf16 v[152:155], v[100:103], v[28:31], v[152:155]\n" \
"ds_read_b128 v[48:51], v0 offset:49152\n" \
"ds_read_b128 v[52:55], v0 offset:50176\n" \
"v_mfma_f32_16x16x32_bf16 v[152:155], v[96:99], v[24:27], v[152:155]\n" \
"ds_read_b128 v[56:59], v0 offset:51200\n" \
"ds_read_b128 v[60:63], v0 offset:52224\n" \
"v_mfma_f32_16x16x32_bf16 v[168:171], v[96:99], v[32:35], v[168:171]\n" \
"ds_read_b128 v[64:67], v2 offset:49152\n" \
"s_add_u32 m0, s70, 32768\n" \
"s_sub_u32 s64, 720896, s73\n" \
"v_mfma_f32_16x16x32_bf16 v[168:171], v[100:103], v[36:39], v[168:171]\n" \
"ds_read_b128 v[68:71], v2 offset:50176\n" \
"buffer_load_dwordx4 v12, s[36:39], s64 offen offset:0 sc1 lds\n" \
"v_mfma_f32_16x16x32_bf16 v[184:187], v[100:103], v[44:47], v[184:187]\n" \
"ds_read_b128 v[72:75], v2 offset:51200\n" \
"v_mfma_f32_16x16x32_bf16 v[184:187], v[96:99], v[40:43], v[184:187]\n" \
"ds_read_b128 v[76:79], v2 offset:52224\n" \
"v_mfma_f32_16x16x32_bf16 v[188:191], v[104:107], v[40:43], v[188:191]\n" \
"ds_read_b64_tr_b16 v[88:89], v5 offset:16384\n" \
"buffer_load_dwordx4 v12, s[36:39], s64 offen offset:1024 sc1 lds\n" \
"v_mfma_f32_16x16x32_bf16 v[188:191], v[108:111], v[44:47], v[188:191]\n" \
"ds_read_b64_tr_b16 v[90:91], v5 offset:20480\n" \
"v_mfma_f32_16x16x32_bf16 v[172:175], v[108:111], v[36:39], v[172:175]\n" \
"ds_read_b64_tr_b16 v[92:93], v5 offset:24576\n" \
"v_mfma_f32_16x16x32_bf16 v[172:175], v[104:107], v[32:35], v[172:175]\n" \
"ds_read_b64_tr_b16 v[94:95], v5 offset:28672\n" \
"buffer_load_dwordx4 v12, s[36:39], s64 offen offset:2048 sc1 lds\n" \
"v_mfma_f32_16x16x32_bf16 v[156:159], v[104:107], v[24:27], v[156:159]\n" \
"v_mfma_f32_16x16x32_bf16 v[156:159], v[108:111], v[28:31], v[156:159]\n" \
"v_mfma_f32_16x16x32_bf16 v[140:143], v[108:111], v[20:23], v[140:143]\n" \
"buffer_load_dwordx4 v12, s[36:39], s64 offen offset:3072 sc1 lds\n" \
"v_mfma_f32_16x16x32_bf16 v[140:143], v[104:107], v[16:19], v[140:143]\n" \
"s_cmp_lg_u32 s62, 0\n" \
"s_cbranch_scc1 L_w0po_%=\n" \
"buffer_load_dwordx2 v[112:113], v117, s[80:83], s60 offen offset:768 sc1\n" \
"L_w0po_%=:\n" \
"s_waitcnt lgkmcnt(4)\n" \
"v_mfma_f32_16x16x32_bf16 v[128:131], v[80:83], v[48:51], v[128:131]\n" \
"ds_read_b64_tr_b16 v[96:97], v6 offset:16384\n" \
"v_mfma_f32_16x16x32_bf16 v[128:131], v[84:87], v[52:55], v[128:131]\n" \
"ds_read_b64_tr_b16 v[98:99], v6 offset:20480\n" \
"s_add_u32 m0, s70, 0\n" \
"s_sub_u32 s65, 5768192, s75\n" \
"v_mfma_f32_16x16x32_bf16 v[144:147], v[84:87], v[60:63], v[144:147]\n" \
"ds_read_b64_tr_b16 v[100:101], v6 offset:24576\n" \
"buffer_load_dwordx4 v13, s[40:43], s65 offen offset:0 lds\n" \
"v_mfma_f32_16x16x32_bf16 v[144:147], v[80:83], v[56:59], v[144:147]\n" \
"ds_read_b64_tr_b16 v[102:103], v6 offset:28672\n" \
"v_mfma_f32_16x16x32_bf16 v[160:163], v[80:83], v[64:67], v[160:163]\n" \
"v_mfma_f32_16x16x32_bf16 v[160:163], v[84:87], v[68:71], v[160:163]\n" \
"s_sub_u32 s66, 5767296, s75\n" \
"v_mfma_f32_16x16x32_bf16 v[176:179], v[84:87], v[76:79], v[176:179]\n" \
"buffer_load_dwordx4 v13, s[40:43], s66 offen offset:1024 lds\n" \
"v_mfma_f32_16x16x32_bf16 v[176:179], v[80:83], v[72:75], v[176:179]\n" \
"s_waitcnt lgkmcnt(4)\n" \
"v_mfma_f32_16x16x32_bf16 v[180:183], v[88:91], v[72:75], v[180:183]\n" \
"ds_read_b64_tr_b16 v[104:105], v7 offset:16384\n" \
"v_mfma_f32_16x16x32_bf16 v[180:183], v[92:95], v[76:79], v[180:183]\n" \
"ds_read_b64_tr_b16 v[106:107], v7 offset:20480\n" \
"s_sub_u32 s67, 5782528, s75\n" \
"v_mfma_f32_16x16x32_bf16 v[164:167], v[92:95], v[68:71], v[164:167]\n" \
"ds_read_b64_tr_b16 v[108:109], v7 offset:24576\n" \
"buffer_load_dwordx4 v13, s[40:43], s67 offen offset:2048 lds\n" \
"v_mfma_f32_16x16x32_bf16 v[164:167], v[88:91], v[64:67], v[164:167]\n" \
"ds_read_b64_tr_b16 v[110:111], v7 offset:28672\n" \
"v_mfma_f32_16x16x32_bf16 v[148:151], v[88:91], v[56:59], v[148:151]\n" \
"v_mfma_f32_16x16x32_bf16 v[148:151], v[92:95], v[60:63], v[148:151]\n" \
"s_sub_u32 s68, 5781632, s75\n" \
"v_mfma_f32_16x16x32_bf16 v[132:135], v[92:95], v[52:55], v[132:135]\n" \
"buffer_load_dwordx4 v13, s[40:43], s68 offen offset:3072 lds\n" \
"v_mfma_f32_16x16x32_bf16 v[132:135], v[88:91], v[48:51], v[132:135]\n" \
"s_waitcnt vmcnt(16) lgkmcnt(0)\n" \
"s_barrier\n" \
"v_mfma_f32_16x16x32_bf16 v[136:139], v[96:99], v[48:51], v[136:139]\n" \
"ds_read_b64_tr_b16 v[80:81], v8 offset:0\n" \
"ds_read_b64_tr_b16 v[82:83], v8 offset:4096\n" \
"v_mfma_f32_16x16x32_bf16 v[136:139], v[100:103], v[52:55], v[136:139]\n" \
"ds_read_b64_tr_b16 v[84:85], v8 offset:8192\n" \
"ds_read_b64_tr_b16 v[86:87], v8 offset:12288\n" \
"v_mfma_f32_16x16x32_bf16 v[152:155], v[100:103], v[60:63], v[152:155]\n" \
"ds_read_b128 v[16:19], v1 offset:32768\n" \
"ds_read_b128 v[20:23], v1 offset:33792\n" \
"v_mfma_f32_16x16x32_bf16 v[152:155], v[96:99], v[56:59], v[152:155]\n" \
"ds_read_b128 v[24:27], v1 offset:34816\n" \
"ds_read_b128 v[28:31], v1 offset:35840\n" \
"v_mfma_f32_16x16x32_bf16 v[168:171], v[96:99], v[64:67], v[168:171]\n" \
"ds_read_b128 v[32:35], v3 offset:32768\n" \
"s_add_u32 m0, s70, 49152\n" \
"s_sub_u32 s64, 737280, s73\n" \
"v_mfma_f32_16x16x32_bf16 v[168:171], v[100:103], v[68:71], v[168:171]\n" \
"ds_read_b128 v[36:39], v3 offset:33792\n" \
"buffer_load_dwordx4 v12, s[36:39], s64 offen offset:0 sc1 lds\n" \
"v_mfma_f32_16x16x32_bf16 v[184:187], v[100:103], v[76:79], v[184:187]\n" \
"ds_read_b128 v[40:43], v3 offset:34816\n" \
"v_mfma_f32_16x16x32_bf16 v[184:187], v[96:99], v[72:75], v[184:187]\n" \
"ds_read_b128 v[44:47], v3 offset:35840\n" \
"v_mfma_f32_16x16x32_bf16 v[188:191], v[104:107], v[72:75], v[188:191]\n" \
"ds_read_b64_tr_b16 v[88:89], v9 offset:0\n" \
"buffer_load_dwordx4 v12, s[36:39], s64 offen offset:1024 sc1 lds\n" \
"v_mfma_f32_16x16x32_bf16 v[188:191], v[108:111], v[76:79], v[188:191]\n" \
"ds_read_b64_tr_b16 v[90:91], v9 offset:4096\n" \
"v_mfma_f32_16x16x32_bf16 v[172:175], v[108:111], v[68:71], v[172:175]\n" \
"ds_read_b64_tr_b16 v[92:93], v9 offset:8192\n" \
"v_mfma_f32_16x16x32_bf16 v[172:175], v[104:107], v[64:67], v[172:175]\n" \
"ds_read_b64_tr_b16 v[94:95], v9 offset:12288\n" \
"buffer_load_dwordx4 v12, s[36:39], s64 offen offset:2048 sc1 lds\n" \
"v_mfma_f32_16x16x32_bf16 v[156:159], v[104:107], v[56:59], v[156:159]\n" \
"v_mfma_f32_16x16x32_bf16 v[156:159], v[108:111], v[60:63], v[156:159]\n" \
"v_mfma_f32_16x16x32_bf16 v[140:143], v[108:111], v[52:55], v[140:143]\n" \
"buffer_load_dwordx4 v12, s[36:39], s64 offen offset:3072 sc1 lds\n" \
"v_mfma_f32_16x16x32_bf16 v[140:143], v[104:107], v[48:51], v[140:143]\n" \
"s_waitcnt lgkmcnt(4)\n" \
"v_mfma_f32_16x16x32_bf16 v[128:131], v[80:83], v[16:19], v[128:131]\n" \
"ds_read_b64_tr_b16 v[96:97], v10 offset:0\n" \
"v_mfma_f32_16x16x32_bf16 v[128:131], v[84:87], v[20:23], v[128:131]\n" \
"ds_read_b64_tr_b16 v[98:99], v10 offset:4096\n" \
"s_add_u32 m0, s70, 16384\n" \
"s_sub_u32 s65, 5899264, s75\n" \
"v_mfma_f32_16x16x32_bf16 v[144:147], v[84:87], v[28:31], v[144:147]\n" \
"ds_read_b64_tr_b16 v[100:101], v10 offset:8192\n" \
"buffer_load_dwordx4 v13, s[40:43], s65 offen offset:0 lds\n" \
"v_mfma_f32_16x16x32_bf16 v[144:147], v[80:83], v[24:27], v[144:147]\n" \
"ds_read_b64_tr_b16 v[102:103], v10 offset:12288\n" \
"v_mfma_f32_16x16x32_bf16 v[160:163], v[80:83], v[32:35], v[160:163]\n" \
"v_mfma_f32_16x16x32_bf16 v[160:163], v[84:87], v[36:39], v[160:163]\n" \
"s_sub_u32 s66, 5898368, s75\n" \
"v_mfma_f32_16x16x32_bf16 v[176:179], v[84:87], v[44:47], v[176:179]\n" \
"buffer_load_dwordx4 v13, s[40:43], s66 offen offset:1024 lds\n" \
"v_mfma_f32_16x16x32_bf16 v[176:179], v[80:83], v[40:43], v[176:179]\n" \
"s_waitcnt lgkmcnt(4)\n" \
"v_mfma_f32_16x16x32_bf16 v[180:183], v[88:91], v[40:43], v[180:183]\n" \
"ds_read_b64_tr_b16 v[104:105], v11 offset:0\n" \
"v_mfma_f32_16x16x32_bf16 v[180:183], v[92:95], v[44:47], v[180:183]\n" \
"ds_read_b64_tr_b16 v[106:107], v11 offset:4096\n" \
"s_sub_u32 s67, 5913600, s75\n" \
"v_mfma_f32_16x16x32_bf16 v[164:167], v[92:95], v[36:39], v[164:167]\n" \
"ds_read_b64_tr_b16 v[108:109], v11 offset:8192\n" \
"buffer_load_dwordx4 v13, s[40:43], s67 offen offset:2048 lds\n" \
"v_mfma_f32_16x16x32_bf16 v[164:167], v[88:91], v[32:35], v[164:167]\n" \
"ds_read_b64_tr_b16 v[110:111], v11 offset:12288\n" \
"v_mfma_f32_16x16x32_bf16 v[148:151], v[88:91], v[24:27], v[148:151]\n" \
"v_mfma_f32_16x16x32_bf16 v[148:151], v[92:95], v[28:31], v[148:151]\n" \
"s_sub_u32 s68, 5912704, s75\n" \
"v_mfma_f32_16x16x32_bf16 v[132:135], v[92:95], v[20:23], v[132:135]\n" \
"buffer_load_dwordx4 v13, s[40:43], s68 offen offset:3072 lds\n" \
"v_mfma_f32_16x16x32_bf16 v[132:135], v[88:91], v[16:19], v[132:135]\n" \
"s_waitcnt vmcnt(16) lgkmcnt(0)\n" \
"s_barrier\n" \
"v_mfma_f32_16x16x32_bf16 v[136:139], v[96:99], v[16:19], v[136:139]\n" \
"ds_read_b64_tr_b16 v[80:81], v8 offset:16384\n" \
"ds_read_b64_tr_b16 v[82:83], v8 offset:20480\n" \
"v_mfma_f32_16x16x32_bf16 v[136:139], v[100:103], v[20:23], v[136:139]\n" \
"ds_read_b64_tr_b16 v[84:85], v8 offset:24576\n" \
"ds_read_b64_tr_b16 v[86:87], v8 offset:28672\n" \
"v_mfma_f32_16x16x32_bf16 v[152:155], v[100:103], v[28:31], v[152:155]\n" \
"ds_read_b128 v[48:51], v1 offset:49152\n" \
"ds_read_b128 v[52:55], v1 offset:50176\n" \
"v_mfma_f32_16x16x32_bf16 v[152:155], v[96:99], v[24:27], v[152:155]\n" \
"ds_read_b128 v[56:59], v1 offset:51200\n" \
"ds_read_b128 v[60:63], v1 offset:52224\n" \
"v_mfma_f32_16x16x32_bf16 v[168:171], v[96:99], v[32:35], v[168:171]\n" \
"ds_read_b128 v[64:67], v3 offset:49152\n" \
"s_add_u32 m0, s70, 98304\n" \
"s_sub_u32 s64, 753664, s73\n" \
"v_mfma_f32_16x16x32_bf16 v[168:171], v[100:103], v[36:39], v[168:171]\n" \
"ds_read_b128 v[68:71], v3 offset:50176\n" \
"buffer_load_dwordx4 v12, s[36:39], s64 offen offset:0 sc1 lds\n" \
"v_mfma_f32_16x16x32_bf16 v[184:187], v[100:103], v[44:47], v[184:187]\n" \
"ds_read_b128 v[72:75], v3 offset:51200\n" \
"v_mfma_f32_16x16x32_bf16 v[184:187], v[96:99], v[40:43], v[184:187]\n" \
"ds_read_b128 v[76:79], v3 offset:52224\n" \
"v_mfma_f32_16x16x32_bf16 v[188:191], v[104:107], v[40:43], v[188:191]\n" \
"ds_read_b64_tr_b16 v[88:89], v9 offset:16384\n" \
"buffer_load_dwordx4 v12, s[36:39], s64 offen offset:1024 sc1 lds\n" \
"v_mfma_f32_16x16x32_bf16 v[188:191], v[108:111], v[44:47], v[188:191]\n" \
"ds_read_b64_tr_b16 v[90:91], v9 offset:20480\n" \
"v_mfma_f32_16x16x32_bf16 v[172:175], v[108:111], v[36:39], v[172:175]\n" \
"ds_read_b64_tr_b16 v[92:93], v9 offset:24576\n" \
"v_mfma_f32_16x16x32_bf16 v[172:175], v[104:107], v[32:35], v[172:175]\n" \
"ds_read_b64_tr_b16 v[94:95], v9 offset:28672\n" \
"buffer_load_dwordx4 v12, s[36:39], s64 offen offset:2048 sc1 lds\n" \
"v_mfma_f32_16x16x32_bf16 v[156:159], v[104:107], v[24:27], v[156:159]\n" \
"v_mfma_f32_16x16x32_bf16 v[156:159], v[108:111], v[28:31], v[156:159]\n" \
"v_mfma_f32_16x16x32_bf16 v[140:143], v[108:111], v[20:23], v[140:143]\n" \
"buffer_load_dwordx4 v12, s[36:39], s64 offen offset:3072 sc1 lds\n" \
"v_mfma_f32_16x16x32_bf16 v[140:143], v[104:107], v[16:19], v[140:143]\n" \
"s_waitcnt lgkmcnt(4)\n" \
"v_mfma_f32_16x16x32_bf16 v[128:131], v[80:83], v[48:51], v[128:131]\n" \
"ds_read_b64_tr_b16 v[96:97], v10 offset:16384\n" \
"v_mfma_f32_16x16x32_bf16 v[128:131], v[84:87], v[52:55], v[128:131]\n" \
"ds_read_b64_tr_b16 v[98:99], v10 offset:20480\n" \
"s_add_u32 m0, s70, 65536\n" \
"s_sub_u32 s65, 6030336, s75\n" \
"v_mfma_f32_16x16x32_bf16 v[144:147], v[84:87], v[60:63], v[144:147]\n" \
"ds_read_b64_tr_b16 v[100:101], v10 offset:24576\n" \
"buffer_load_dwordx4 v13, s[40:43], s65 offen offset:0 lds\n" \
"v_mfma_f32_16x16x32_bf16 v[144:147], v[80:83], v[56:59], v[144:147]\n" \
"ds_read_b64_tr_b16 v[102:103], v10 offset:28672\n" \
"v_mfma_f32_16x16x32_bf16 v[160:163], v[80:83], v[64:67], v[160:163]\n" \
"v_mfma_f32_16x16x32_bf16 v[160:163], v[84:87], v[68:71], v[160:163]\n" \
"s_sub_u32 s66, 6029440, s75\n" \
"v_mfma_f32_16x16x32_bf16 v[176:179], v[84:87], v[76:79], v[176:179]\n" \
"buffer_load_dwordx4 v13, s[40:43], s66 offen offset:1024 lds\n" \
"v_mfma_f32_16x16x32_bf16 v[176:179], v[80:83], v[72:75], v[176:179]\n" \
"s_waitcnt lgkmcnt(4)\n" \
"v_mfma_f32_16x16x32_bf16 v[180:183], v[88:91], v[72:75], v[180:183]\n" \
"ds_read_b64_tr_b16 v[104:105], v11 offset:16384\n" \
"v_mfma_f32_16x16x32_bf16 v[180:183], v[92:95], v[76:79], v[180:183]\n" \
"ds_read_b64_tr_b16 v[106:107], v11 offset:20480\n" \
"s_sub_u32 s67, 6044672, s75\n" \
"v_mfma_f32_16x16x32_bf16 v[164:167], v[92:95], v[68:71], v[164:167]\n" \
"ds_read_b64_tr_b16 v[108:109], v11 offset:24576\n" \
"buffer_load_dwordx4 v13, s[40:43], s67 offen offset:2048 lds\n" \
"v_mfma_f32_16x16x32_bf16 v[164:167], v[88:91], v[64:67], v[164:167]\n" \
"ds_read_b64_tr_b16 v[110:111], v11 offset:28672\n" \
"v_mfma_f32_16x16x32_bf16 v[148:151], v[88:91], v[56:59], v[148:151]\n" \
"v_mfma_f32_16x16x32_bf16 v[148:151], v[92:95], v[60:63], v[148:151]\n" \
"s_sub_u32 s68, 6043776, s75\n" \
"v_mfma_f32_16x16x32_bf16 v[132:135], v[92:95], v[52:55], v[132:135]\n" \
"buffer_load_dwordx4 v13, s[40:43], s68 offen offset:3072 lds\n" \
"v_mfma_f32_16x16x32_bf16 v[132:135], v[88:91], v[48:51], v[132:135]\n" \
"s_waitcnt vmcnt(16) lgkmcnt(0)\n" \
"s_barrier\n" \
"v_mfma_f32_16x16x32_bf16 v[136:139], v[96:99], v[48:51], v[136:139]\n" \
"ds_read_b64_tr_b16 v[80:81], v4 offset:0\n" \
"ds_read_b64_tr_b16 v[82:83], v4 offset:4096\n" \
"v_mfma_f32_16x16x32_bf16 v[136:139], v[100:103], v[52:55], v[136:139]\n" \
"ds_read_b64_tr_b16 v[84:85], v4 offset:8192\n" \
"ds_read_b64_tr_b16 v[86:87], v4 offset:12288\n" \
"v_mfma_f32_16x16x32_bf16 v[152:155], v[100:103], v[60:63], v[152:155]\n" \
"ds_read_b128 v[16:19], v0 offset:32768\n" \
"ds_read_b128 v[20:23], v0 offset:33792\n" \
"v_mfma_f32_16x16x32_bf16 v[152:155], v[96:99], v[56:59], v[152:155]\n" \
"ds_read_b128 v[24:27], v0 offset:34816\n" \
"ds_read_b128 v[28:31], v0 offset:35840\n" \
"v_mfma_f32_16x16x32_bf16 v[168:171], v[96:99], v[64:67], v[168:171]\n" \
"ds_read_b128 v[32:35], v2 offset:32768\n" \
"s_add_u32 m0, s70, 114688\n" \
"s_sub_u32 s64, 770048, s73\n" \
"v_mfma_f32_16x16x32_bf16 v[168:171], v[100:103], v[68:71], v[168:171]\n" \
"ds_read_b128 v[36:39], v2 offset:33792\n" \
"buffer_load_dwordx4 v12, s[36:39], s64 offen offset:0 sc1 lds\n" \
"v_mfma_f32_16x16x32_bf16 v[184:187], v[100:103], v[76:79], v[184:187]\n" \
"ds_read_b128 v[40:43], v2 offset:34816\n" \
"v_mfma_f32_16x16x32_bf16 v[184:187], v[96:99], v[72:75], v[184:187]\n" \
"ds_read_b128 v[44:47], v2 offset:35840\n" \
"v_mfma_f32_16x16x32_bf16 v[188:191], v[104:107], v[72:75], v[188:191]\n" \
"ds_read_b64_tr_b16 v[88:89], v5 offset:0\n" \
"buffer_load_dwordx4 v12, s[36:39], s64 offen offset:1024 sc1 lds\n" \
"v_mfma_f32_16x16x32_bf16 v[188:191], v[108:111], v[76:79], v[188:191]\n" \
"ds_read_b64_tr_b16 v[90:91], v5 offset:4096\n" \
"v_mfma_f32_16x16x32_bf16 v[172:175], v[108:111], v[68:71], v[172:175]\n" \
"ds_read_b64_tr_b16 v[92:93], v5 offset:8192\n" \
"v_mfma_f32_16x16x32_bf16 v[172:175], v[104:107], v[64:67], v[172:175]\n" \
"ds_read_b64_tr_b16 v[94:95], v5 offset:12288\n" \
"buffer_load_dwordx4 v12, s[36:39], s64 offen offset:2048 sc1 lds\n" \
"v_mfma_f32_16x16x32_bf16 v[156:159], v[104:107], v[56:59], v[156:159]\n" \
"v_mfma_f32_16x16x32_bf16 v[156:159], v[108:111], v[60:63], v[156:159]\n" \
"v_mfma_f32_16x16x32_bf16 v[140:143], v[108:111], v[52:55], v[140:143]\n" \
"buffer_load_dwordx4 v12, s[36:39], s64 offen offset:3072 sc1 lds\n" \
"v_mfma_f32_16x16x32_bf16 v[140:143], v[104:107], v[48:51], v[140:143]\n" \
"s_waitcnt lgkmcnt(4)\n" \
"v_mfma_f32_16x16x32_bf16 v[128:131], v[80:83], v[16:19], v[128:131]\n" \
"ds_read_b64_tr_b16 v[96:97], v6 offset:0\n" \
"v_mfma_f32_16x16x32_bf16 v[128:131], v[84:87], v[20:23], v[128:131]\n" \
"ds_read_b64_tr_b16 v[98:99], v6 offset:4096\n" \
"s_add_u32 m0, s70, 81920\n" \
"s_sub_u32 s65, 6161408, s75\n" \
"v_mfma_f32_16x16x32_bf16 v[144:147], v[84:87], v[28:31], v[144:147]\n" \
"ds_read_b64_tr_b16 v[100:101], v6 offset:8192\n" \
"buffer_load_dwordx4 v13, s[40:43], s65 offen offset:0 lds\n" \
"v_mfma_f32_16x16x32_bf16 v[144:147], v[80:83], v[24:27], v[144:147]\n" \
"ds_read_b64_tr_b16 v[102:103], v6 offset:12288\n" \
"v_mfma_f32_16x16x32_bf16 v[160:163], v[80:83], v[32:35], v[160:163]\n" \
"v_mfma_f32_16x16x32_bf16 v[160:163], v[84:87], v[36:39], v[160:163]\n" \
"s_sub_u32 s66, 6160512, s75\n" \
"v_mfma_f32_16x16x32_bf16 v[176:179], v[84:87], v[44:47], v[176:179]\n" \
"buffer_load_dwordx4 v13, s[40:43], s66 offen offset:1024 lds\n" \
"v_mfma_f32_16x16x32_bf16 v[176:179], v[80:83], v[40:43], v[176:179]\n" \
"s_waitcnt lgkmcnt(4)\n" \
"v_mfma_f32_16x16x32_bf16 v[180:183], v[88:91], v[40:43], v[180:183]\n" \
"ds_read_b64_tr_b16 v[104:105], v7 offset:0\n" \
"v_mfma_f32_16x16x32_bf16 v[180:183], v[92:95], v[44:47], v[180:183]\n" \
"ds_read_b64_tr_b16 v[106:107], v7 offset:4096\n" \
"s_sub_u32 s67, 6175744, s75\n" \
"v_mfma_f32_16x16x32_bf16 v[164:167], v[92:95], v[36:39], v[164:167]\n" \
"ds_read_b64_tr_b16 v[108:109], v7 offset:8192\n" \
"buffer_load_dwordx4 v13, s[40:43], s67 offen offset:2048 lds\n" \
"v_mfma_f32_16x16x32_bf16 v[164:167], v[88:91], v[32:35], v[164:167]\n" \
"ds_read_b64_tr_b16 v[110:111], v7 offset:12288\n" \
"v_mfma_f32_16x16x32_bf16 v[148:151], v[88:91], v[24:27], v[148:151]\n" \
"v_mfma_f32_16x16x32_bf16 v[148:151], v[92:95], v[28:31], v[148:151]\n" \
"s_sub_u32 s68, 6174848, s75\n" \
"v_mfma_f32_16x16x32_bf16 v[132:135], v[92:95], v[20:23], v[132:135]\n" \
"buffer_load_dwordx4 v13, s[40:43], s68 offen offset:3072 lds\n" \
"v_mfma_f32_16x16x32_bf16 v[132:135], v[88:91], v[16:19], v[132:135]\n" \
"s_waitcnt vmcnt(16) lgkmcnt(0)\n" \
"s_cmp_lg_u32 s62, 0\n" \
"s_cbranch_scc1 L_w0so_%=\n" \
"v_cmp_eq_u64 vcc, s[28:29], v[112:113]\n" \
"s_nop 1\n" \
"s_cmp_eq_u64 vcc, -1\n" \
"s_cbranch_scc1 L_oko_%=\n" \
"s_mov_b32 s76, 0x40000\n" \
"L_spo_%=:\n" \
"buffer_load_dwordx2 v[112:113], v117, s[80:83], s60 offen offset:768 sc1\n" \
"s_waitcnt vmcnt(0)\n" \
"v_cmp_eq_u64 vcc, s[28:29], v[112:113]\n" \
"s_nop 1\n" \
"s_cmp_eq_u64 vcc, -1\n" \
"s_cbranch_scc1 L_oko_%=\n" \
"s_sleep 1\n" \
"s_sub_u32 s76, s76, 1\n" \
"s_cmp_lg_u32 s76, 0\n" \
"s_cbranch_scc1 L_spo_%=\n" \
"s_mov_b32 s77, 1\n" \
"L_oko_%=:\n" \
"buffer_store_dwordx2 v[122:123], v117, s[80:83], s60 offen offset:768 sc1\n" \
"L_w0so_%=:\n" \
"s_barrier\n" \
"v_mfma_f32_16x16x32_bf16 v[136:139], v[96:99], v[16:19], v[136:139]\n" \
"ds_read_b64_tr_b16 v[80:81], v4 offset:16384\n" \
"ds_read_b64_tr_b16 v[82:83], v4 offset:20480\n" \
"v_mfma_f32_16x16x32_bf16 v[136:139], v[100:103], v[20:23], v[136:139]\n" \
"ds_read_b64_tr_b16 v[84:85], v4 offset:24576\n" \
"ds_read_b64_tr_b16 v[86:87], v4 offset:28672\n" \
"v_mfma_f32_16x16x32_bf16 v[152:155], v[100:103], v[28:31], v[152:155]\n" \
"ds_read_b128 v[48:51], v0 offset:49152\n" \
"ds_read_b128 v[52:55], v0 offset:50176\n" \
"v_mfma_f32_16x16x32_bf16 v[152:155], v[96:99], v[24:27], v[152:155]\n" \
"ds_read_b128 v[56:59], v0 offset:51200\n" \
"ds_read_b128 v[60:63], v0 offset:52224\n" \
"v_mfma_f32_16x16x32_bf16 v[168:171], v[96:99], v[32:35], v[168:171]\n" \
"ds_read_b128 v[64:67], v2 offset:49152\n" \
"s_add_u32 m0, s72, 32768\n" \
"s_mov_b32 s64, s69\n" \
"v_mfma_f32_16x16x32_bf16 v[168:171], v[100:103], v[36:39], v[168:171]\n" \
"ds_read_b128 v[68:71], v2 offset:50176\n" \
"buffer_load_dwordx4 v14, s[36:39], s64 offen offset:0 sc1 lds\n" \
"v_mfma_f32_16x16x32_bf16 v[184:187], v[100:103], v[44:47], v[184:187]\n" \
"ds_read_b128 v[72:75], v2 offset:51200\n" \
"v_mfma_f32_16x16x32_bf16 v[184:187], v[96:99], v[40:43], v[184:187]\n" \
"ds_read_b128 v[76:79], v2 offset:52224\n" \
"v_mfma_f32_16x16x32_bf16 v[188:191], v[104:107], v[40:43], v[188:191]\n" \
"ds_read_b64_tr_b16 v[88:89], v5 offset:16384\n" \
"buffer_load_dwordx4 v14, s[36:39], s64 offen offset:1024 sc1 lds\n" \
"s_add_u32 s64, s64, 16384\n" \
"v_mfma_f32_16x16x32_bf16 v[188:191], v[108:111], v[44:47], v[188:191]\n" \
"ds_read_b64_tr_b16 v[90:91], v5 offset:20480\n" \
"v_mfma_f32_16x16x32_bf16 v[172:175], v[108:111], v[36:39], v[172:175]\n" \
"ds_read_b64_tr_b16 v[92:93], v5 offset:24576\n" \
"s_add_u32 m0, s72, 40960\n" \
"v_mfma_f32_16x16x32_bf16 v[172:175], v[104:107], v[32:35], v[172:175]\n" \
"ds_read_b64_tr_b16 v[94:95], v5 offset:28672\n" \
"buffer_load_dwordx4 v14, s[36:39], s64 offen offset:0 sc1 lds\n" \
"v_mfma_f32_16x16x32_bf16 v[156:159], v[104:107], v[24:27], v[156:159]\n" \
"v_mfma_f32_16x16x32_bf16 v[156:159], v[108:111], v[28:31], v[156:159]\n" \
"v_mfma_f32_16x16x32_bf16 v[140:143], v[108:111], v[20:23], v[140:143]\n" \
"buffer_load_dwordx4 v14, s[36:39], s64 offen offset:1024 sc1 lds\n" \
"s_add_u32 s64, s64, 16384\n" \
"v_mfma_f32_16x16x32_bf16 v[140:143], v[104:107], v[16:19], v[140:143]\n" \
"s_waitcnt lgkmcnt(4)\n" \
"v_mfma_f32_16x16x32_bf16 v[128:131], v[80:83], v[48:51], v[128:131]\n" \
"ds_read_b64_tr_b16 v[96:97], v6 offset:16384\n" \
"v_mfma_f32_16x16x32_bf16 v[128:131], v[84:87], v[52:55], v[128:131]\n" \
"ds_read_b64_tr_b16 v[98:99], v6 offset:20480\n" \
"s_add_u32 m0, s70, 0\n" \
"s_mov_b32 s65, 1024\n" \
"v_mfma_f32_16x16x32_bf16 v[144:147], v[84:87], v[60:63], v[144:147]\n" \
"ds_read_b64_tr_b16 v[100:101], v6 offset:24576\n" \
"buffer_load_dwordx4 v13, s[44:47], s65 offen offset:0 lds\n" \
"v_mfma_f32_16x16x32_bf16 v[144:147], v[80:83], v[56:59], v[144:147]\n" \
"ds_read_b64_tr_b16 v[102:103], v6 offset:28672\n" \
"v_mfma_f32_16x16x32_bf16 v[160:163], v[80:83], v[64:67], v[160:163]\n" \
"v_mfma_f32_16x16x32_bf16 v[160:163], v[84:87], v[68:71], v[160:163]\n" \
"s_mov_b32 s66, 128\n" \
"v_mfma_f32_16x16x32_bf16 v[176:179], v[84:87], v[76:79], v[176:179]\n" \
"buffer_load_dwordx4 v13, s[44:47], s66 offen offset:1024 lds\n" \
"v_mfma_f32_16x16x32_bf16 v[176:179], v[80:83], v[72:75], v[176:179]\n" \
"s_waitcnt lgkmcnt(4)\n" \
"v_mfma_f32_16x16x32_bf16 v[180:183], v[88:91], v[72:75], v[180:183]\n" \
"ds_read_b64_tr_b16 v[104:105], v7 offset:16384\n" \
"v_mfma_f32_16x16x32_bf16 v[180:183], v[92:95], v[76:79], v[180:183]\n" \
"ds_read_b64_tr_b16 v[106:107], v7 offset:20480\n" \
"s_mov_b32 s67, 15360\n" \
"v_mfma_f32_16x16x32_bf16 v[164:167], v[92:95], v[68:71], v[164:167]\n" \
"ds_read_b64_tr_b16 v[108:109], v7 offset:24576\n" \
"buffer_load_dwordx4 v13, s[44:47], s67 offen offset:2048 lds\n" \
"v_mfma_f32_16x16x32_bf16 v[164:167], v[88:91], v[64:67], v[164:167]\n" \
"ds_read_b64_tr_b16 v[110:111], v7 offset:28672\n" \
"v_mfma_f32_16x16x32_bf16 v[148:151], v[88:91], v[56:59], v[148:151]\n" \
"v_mfma_f32_16x16x32_bf16 v[148:151], v[92:95], v[60:63], v[148:151]\n" \
"s_mov_b32 s68, 14464\n" \
"v_mfma_f32_16x16x32_bf16 v[132:135], v[92:95], v[52:55], v[132:135]\n" \
"buffer_load_dwordx4 v13, s[44:47], s68 offen offset:3072 lds\n" \
"v_mfma_f32_16x16x32_bf16 v[132:135], v[88:91], v[48:51], v[132:135]\n" \
"s_waitcnt vmcnt(16) lgkmcnt(0)\n" \
"s_barrier\n" \
"v_mfma_f32_16x16x32_bf16 v[136:139], v[96:99], v[48:51], v[136:139]\n" \
"ds_read_b64_tr_b16 v[80:81], v8 offset:0\n" \
"ds_read_b64_tr_b16 v[82:83], v8 offset:4096\n" \
"v_mfma_f32_16x16x32_bf16 v[136:139], v[100:103], v[52:55], v[136:139]\n" \
"ds_read_b64_tr_b16 v[84:85], v8 offset:8192\n" \
"ds_read_b64_tr_b16 v[86:87], v8 offset:12288\n" \
"v_mfma_f32_16x16x32_bf16 v[152:155], v[100:103], v[60:63], v[152:155]\n" \
"ds_read_b128 v[16:19], v1 offset:32768\n" \
"ds_read_b128 v[20:23], v1 offset:33792\n" \
"v_mfma_f32_16x16x32_bf16 v[152:155], v[96:99], v[56:59], v[152:155]\n" \
"ds_read_b128 v[24:27], v1 offset:34816\n" \
"ds_read_b128 v[28:31], v1 offset:35840\n" \
"v_mfma_f32_16x16x32_bf16 v[168:171], v[96:99], v[64:67], v[168:171]\n" \
"ds_read_b128 v[32:35], v3 offset:32768\n" \
"s_add_u32 m0, s72, 49152\n" \
"v_mfma_f32_16x16x32_bf16 v[168:171], v[100:103], v[68:71], v[168:171]\n" \
"ds_read_b128 v[36:39], v3 offset:33792\n" \
"buffer_load_dwordx4 v14, s[36:39], s64 offen offset:0 sc1 lds\n" \
"v_mfma_f32_16x16x32_bf16 v[184:187], v[100:103], v[76:79], v[184:187]\n" \
"ds_read_b128 v[40:43], v3 offset:34816\n" \
"v_mfma_f32_16x16x32_bf16 v[184:187], v[96:99], v[72:75], v[184:187]\n" \
"ds_read_b128 v[44:47], v3 offset:35840\n" \
"v_mfma_f32_16x16x32_bf16 v[188:191], v[104:107], v[72:75], v[188:191]\n" \
"ds_read_b64_tr_b16 v[88:89], v9 offset:0\n" \
"buffer_load_dwordx4 v14, s[36:39], s64 offen offset:1024 sc1 lds\n" \
"s_add_u32 s64, s64, 16384\n" \
"v_mfma_f32_16x16x32_bf16 v[188:191], v[108:111], v[76:79], v[188:191]\n" \
"ds_read_b64_tr_b16 v[90:91], v9 offset:4096\n" \
"v_mfma_f32_16x16x32_bf16 v[172:175], v[108:111], v[68:71], v[172:175]\n" \
"ds_read_b64_tr_b16 v[92:93], v9 offset:8192\n" \
"s_add_u32 m0, s72, 57344\n" \
"v_mfma_f32_16x16x32_bf16 v[172:175], v[104:107], v[64:67], v[172:175]\n" \
"ds_read_b64_tr_b16 v[94:95], v9 offset:12288\n" \
"buffer_load_dwordx4 v14, s[36:39], s64 offen offset:0 sc1 lds\n" \
"v_mfma_f32_16x16x32_bf16 v[156:159], v[104:107], v[56:59], v[156:159]\n" \
"v_mfma_f32_16x16x32_bf16 v[156:159], v[108:111], v[60:63], v[156:159]\n" \
"v_mfma_f32_16x16x32_bf16 v[140:143], v[108:111], v[52:55], v[140:143]\n" \
"buffer_load_dwordx4 v14, s[36:39], s64 offen offset:1024 sc1 lds\n" \
"s_add_u32 s64, s64, 16384\n" \
"v_mfma_f32_16x16x32_bf16 v[140:143], v[104:107], v[48:51], v[140:143]\n" \
"s_waitcnt lgkmcnt(4)\n" \
"v_mfma_f32_16x16x32_bf16 v[128:131], v[80:83], v[16:19], v[128:131]\n" \
"ds_read_b64_tr_b16 v[96:97], v10 offset:0\n" \
"v_mfma_f32_16x16x32_bf16 v[128:131], v[84:87], v[20:23], v[128:131]\n" \
"ds_read_b64_tr_b16 v[98:99], v10 offset:4096\n" \
"s_add_u32 m0, s70, 16384\n" \
"s_mov_b32 s65, 263168\n" \
"v_mfma_f32_16x16x32_bf16 v[144:147], v[84:87], v[28:31], v[144:147]\n" \
"ds_read_b64_tr_b16 v[100:101], v10 offset:8192\n" \
"buffer_load_dwordx4 v13, s[44:47], s65 offen offset:0 lds\n" \
"v_mfma_f32_16x16x32_bf16 v[144:147], v[80:83], v[24:27], v[144:147]\n" \
"ds_read_b64_tr_b16 v[102:103], v10 offset:12288\n" \
"v_mfma_f32_16x16x32_bf16 v[160:163], v[80:83], v[32:35], v[160:163]\n" \
"v_mfma_f32_16x16x32_bf16 v[160:163], v[84:87], v[36:39], v[160:163]\n" \
"s_mov_b32 s66, 262272\n" \
"v_mfma_f32_16x16x32_bf16 v[176:179], v[84:87], v[44:47], v[176:179]\n" \
"buffer_load_dwordx4 v13, s[44:47], s66 offen offset:1024 lds\n" \
"v_mfma_f32_16x16x32_bf16 v[176:179], v[80:83], v[40:43], v[176:179]\n" \
"s_waitcnt lgkmcnt(4)\n" \
"v_mfma_f32_16x16x32_bf16 v[180:183], v[88:91], v[40:43], v[180:183]\n" \
"ds_read_b64_tr_b16 v[104:105], v11 offset:0\n" \
"v_mfma_f32_16x16x32_bf16 v[180:183], v[92:95], v[44:47], v[180:183]\n" \
"ds_read_b64_tr_b16 v[106:107], v11 offset:4096\n" \
"s_mov_b32 s67, 277504\n" \
"v_mfma_f32_16x16x32_bf16 v[164:167], v[92:95], v[36:39], v[164:167]\n" \
"ds_read_b64_tr_b16 v[108:109], v11 offset:8192\n" \
"buffer_load_dwordx4 v13, s[44:47], s67 offen offset:2048 lds\n" \
"v_mfma_f32_16x16x32_bf16 v[164:167], v[88:91], v[32:35], v[164:167]\n" \
"ds_read_b64_tr_b16 v[110:111], v11 offset:12288\n" \
"v_mfma_f32_16x16x32_bf16 v[148:151], v[88:91], v[24:27], v[148:151]\n" \
"v_mfma_f32_16x16x32_bf16 v[148:151], v[92:95], v[28:31], v[148:151]\n" \
"s_mov_b32 s68, 276608\n" \
"v_mfma_f32_16x16x32_bf16 v[132:135], v[92:95], v[20:23], v[132:135]\n" \
"buffer_load_dwordx4 v13, s[44:47], s68 offen offset:3072 lds\n" \
"v_mfma_f32_16x16x32_bf16 v[132:135], v[88:91], v[16:19], v[132:135]\n" \
"s_waitcnt vmcnt(16) lgkmcnt(0)\n" \
"s_barrier\n" \
"v_mfma_f32_16x16x32_bf16 v[136:139], v[96:99], v[16:19], v[136:139]\n" \
"ds_read_b64_tr_b16 v[80:81], v8 offset:16384\n" \
"ds_read_b64_tr_b16 v[82:83], v8 offset:20480\n" \
"v_mfma_f32_16x16x32_bf16 v[136:139], v[100:103], v[20:23], v[136:139]\n" \
"ds_read_b64_tr_b16 v[84:85], v8 offset:24576\n" \
"ds_read_b64_tr_b16 v[86:87], v8 offset:28672\n" \
"v_mfma_f32_16x16x32_bf16 v[152:155], v[100:103], v[28:31], v[152:155]\n" \
"ds_read_b128 v[48:51], v1 offset:49152\n" \
"ds_read_b128 v[52:55], v1 offset:50176\n" \
"v_mfma_f32_16x16x32_bf16 v[152:155], v[96:99], v[24:27], v[152:155]\n" \
"ds_read_b128 v[56:59], v1 offset:51200\n" \
"ds_read_b128 v[60:63], v1 offset:52224\n" \
"v_mfma_f32_16x16x32_bf16 v[168:171], v[96:99], v[32:35], v[168:171]\n" \
"ds_read_b128 v[64:67], v3 offset:49152\n" \
"s_add_u32 m0, s72, 98304\n" \
"v_mfma_f32_16x16x32_bf16 v[168:171], v[100:103], v[36:39], v[168:171]\n" \
"ds_read_b128 v[68:71], v3 offset:50176\n" \
"buffer_load_dwordx4 v14, s[36:39], s64 offen offset:0 sc1 lds\n" \
"v_mfma_f32_16x16x32_bf16 v[184:187], v[100:103], v[44:47], v[184:187]\n" \
"ds_read_b128 v[72:75], v3 offset:51200\n" \
"v_mfma_f32_16x16x32_bf16 v[184:187], v[96:99], v[40:43], v[184:187]\n" \
"ds_read_b128 v[76:79], v3 offset:52224\n" \
"v_mfma_f32_16x16x32_bf16 v[188:191], v[104:107], v[40:43], v[188:191]\n" \
"ds_read_b64_tr_b16 v[88:89], v9 offset:16384\n" \
"buffer_load_dwordx4 v14, s[36:39], s64 offen offset:1024 sc1 lds\n" \
"s_add_u32 s64, s64, 16384\n" \
"v_mfma_f32_16x16x32_bf16 v[188:191], v[108:111], v[44:47], v[188:191]\n" \
"ds_read_b64_tr_b16 v[90:91], v9 offset:20480\n" \
"v_mfma_f32_16x16x32_bf16 v[172:175], v[108:111], v[36:39], v[172:175]\n" \
"ds_read_b64_tr_b16 v[92:93], v9 offset:24576\n" \
"s_add_u32 m0, s72, 106496\n" \
"v_mfma_f32_16x16x32_bf16 v[172:175], v[104:107], v[32:35], v[172:175]\n" \
"ds_read_b64_tr_b16 v[94:95], v9 offset:28672\n" \
"buffer_load_dwordx4 v14, s[36:39], s64 offen offset:0 sc1 lds\n" \
"v_mfma_f32_16x16x32_bf16 v[156:159], v[104:107], v[24:27], v[156:159]\n" \
"v_mfma_f32_16x16x32_bf16 v[156:159], v[108:111], v[28:31], v[156:159]\n" \
"v_mfma_f32_16x16x32_bf16 v[140:143], v[108:111], v[20:23], v[140:143]\n" \
"buffer_load_dwordx4 v14, s[36:39], s64 offen offset:1024 sc1 lds\n" \
"s_add_u32 s64, s64, 16384\n" \
"v_mfma_f32_16x16x32_bf16 v[140:143], v[104:107], v[16:19], v[140:143]\n" \
"s_waitcnt lgkmcnt(4)\n" \
"v_mfma_f32_16x16x32_bf16 v[128:131], v[80:83], v[48:51], v[128:131]\n" \
"ds_read_b64_tr_b16 v[96:97], v10 offset:16384\n" \
"v_mfma_f32_16x16x32_bf16 v[128:131], v[84:87], v[52:55], v[128:131]\n" \
"ds_read_b64_tr_b16 v[98:99], v10 offset:20480\n" \
"s_add_u32 m0, s70, 65536\n" \
"s_mov_b32 s65, 394240\n" \
"v_mfma_f32_16x16x32_bf16 v[144:147], v[84:87], v[60:63], v[144:147]\n" \
"ds_read_b64_tr_b16 v[100:101], v10 offset:24576\n" \
"buffer_load_dwordx4 v13, s[44:47], s65 offen offset:0 lds\n" \
"v_mfma_f32_16x16x32_bf16 v[144:147], v[80:83], v[56:59], v[144:147]\n" \
"ds_read_b64_tr_b16 v[102:103], v10 offset:28672\n" \
"v_mfma_f32_16x16x32_bf16 v[160:163], v[80:83], v[64:67], v[160:163]\n" \
"v_mfma_f32_16x16x32_bf16 v[160:163], v[84:87], v[68:71], v[160:163]\n" \
"s_mov_b32 s66, 393344\n" \
"v_mfma_f32_16x16x32_bf16 v[176:179], v[84:87], v[76:79], v[176:179]\n" \
"buffer_load_dwordx4 v13, s[44:47], s66 offen offset:1024 lds\n" \
"v_mfma_f32_16x16x32_bf16 v[176:179], v[80:83], v[72:75], v[176:179]\n" \
"s_waitcnt lgkmcnt(4)\n" \
"v_mfma_f32_16x16x32_bf16 v[180:183], v[88:91], v[72:75], v[180:183]\n" \
"ds_read_b64_tr_b16 v[104:105], v11 offset:16384\n" \
"v_mfma_f32_16x16x32_bf16 v[180:183], v[92:95], v[76:79], v[180:183]\n" \
"ds_read_b64_tr_b16 v[106:107], v11 offset:20480\n" \
"s_mov_b32 s67, 408576\n" \
"v_mfma_f32_16x16x32_bf16 v[164:167], v[92:95], v[68:71], v[164:167]\n" \
"ds_read_b64_tr_b16 v[108:109], v11 offset:24576\n" \
"buffer_load_dwordx4 v13, s[44:47], s67 offen offset:2048 lds\n" \
"v_mfma_f32_16x16x32_bf16 v[164:167], v[88:91], v[64:67], v[164:167]\n" \
"ds_read_b64_tr_b16 v[110:111], v11 offset:28672\n" \
"v_mfma_f32_16x16x32_bf16 v[148:151], v[88:91], v[56:59], v[148:151]\n" \
"v_mfma_f32_16x16x32_bf16 v[148:151], v[92:95], v[60:63], v[148:151]\n" \
"s_mov_b32 s68, 407680\n" \
"v_mfma_f32_16x16x32_bf16 v[132:135], v[92:95], v[52:55], v[132:135]\n" \
"buffer_load_dwordx4 v13, s[44:47], s68 offen offset:3072 lds\n" \
"v_mfma_f32_16x16x32_bf16 v[132:135], v[88:91], v[48:51], v[132:135]\n" \
"s_waitcnt vmcnt(16) lgkmcnt(0)\n" \
"s_barrier\n" \
"v_mfma_f32_16x16x32_bf16 v[136:139], v[96:99], v[48:51], v[136:139]\n" \
"ds_read_b64_tr_b16 v[80:81], v4 offset:0\n" \
"ds_read_b64_tr_b16 v[82:83], v4 offset:4096\n" \
"v_mfma_f32_16x16x32_bf16 v[136:139], v[100:103], v[52:55], v[136:139]\n" \
"ds_read_b64_tr_b16 v[84:85], v4 offset:8192\n" \
"ds_read_b64_tr_b16 v[86:87], v4 offset:12288\n" \
"v_mfma_f32_16x16x32_bf16 v[152:155], v[100:103], v[60:63], v[152:155]\n" \
"ds_read_b128 v[16:19], v126 offset:32768\n" \
"ds_read_b128 v[20:23], v126 offset:33792\n" \
"v_mfma_f32_16x16x32_bf16 v[152:155], v[96:99], v[56:59], v[152:155]\n" \
"ds_read_b128 v[24:27], v126 offset:34816\n" \
"ds_read_b128 v[28:31], v126 offset:35840\n" \
"v_mfma_f32_16x16x32_bf16 v[168:171], v[96:99], v[64:67], v[168:171]\n" \
"s_add_u32 m0, s72, 114688\n" \
"v_mfma_f32_16x16x32_bf16 v[168:171], v[100:103], v[68:71], v[168:171]\n" \
"buffer_load_dwordx4 v14, s[36:39], s64 offen offset:0 sc1 lds\n" \
"v_mfma_f32_16x16x32_bf16 v[184:187], v[100:103], v[76:79], v[184:187]\n" \
"v_mfma_f32_16x16x32_bf16 v[184:187], v[96:99], v[72:75], v[184:187]\n" \
"v_mfma_f32_16x16x32_bf16 v[188:191], v[104:107], v[72:75], v[188:191]\n" \
"ds_read_b64_tr_b16 v[88:89], v5 offset:0\n" \
"buffer_load_dwordx4 v14, s[36:39], s64 offen offset:1024 sc1 lds\n" \
"s_add_u32 s64, s64, 16384\n" \
"v_mfma_f32_16x16x32_bf16 v[188:191], v[108:111], v[76:79], v[188:191]\n" \
"ds_read_b64_tr_b16 v[90:91], v5 offset:4096\n" \
"v_mfma_f32_16x16x32_bf16 v[172:175], v[108:111], v[68:71], v[172:175]\n" \
"ds_read_b64_tr_b16 v[92:93], v5 offset:8192\n" \
"s_add_u32 m0, s72, 122880\n" \
"v_mfma_f32_16x16x32_bf16 v[172:175], v[104:107], v[64:67], v[172:175]\n" \
"ds_read_b64_tr_b16 v[94:95], v5 offset:12288\n" \
"buffer_load_dwordx4 v14, s[36:39], s64 offen offset:0 sc1 lds\n" \
"v_mfma_f32_16x16x32_bf16 v[156:159], v[104:107], v[56:59], v[156:159]\n" \
"v_mfma_f32_16x16x32_bf16 v[156:159], v[108:111], v[60:63], v[156:159]\n" \
"v_mfma_f32_16x16x32_bf16 v[140:143], v[108:111], v[52:55], v[140:143]\n" \
"buffer_load_dwordx4 v14, s[36:39], s64 offen offset:1024 sc1 lds\n" \
"s_add_u32 s64, s64, 16384\n" \
"v_mfma_f32_16x16x32_bf16 v[140:143], v[104:107], v[48:51], v[140:143]\n" \
"s_waitcnt lgkmcnt(4)\n" \
"v_mfma_f32_16x16x32_bf16 v[192:195], v[80:83], v[16:19], 0\n" \
"ds_read_b64_tr_b16 v[96:97], v6 offset:0\n" \
"s_add_u32 m0, s70, 81920\n" \
"s_mov_b32 s65, 525312\n" \
"v_mfma_f32_16x16x32_bf16 v[192:195], v[84:87], v[20:23], v[192:195]\n" \
"ds_read_b64_tr_b16 v[98:99], v6 offset:4096\n" \
"buffer_load_dwordx4 v13, s[44:47], s65 offen offset:0 lds\n" \
"s_nop 0\n" \
"v_mfma_f32_16x16x32_bf16 v[208:211], v[84:87], v[28:31], 0\n" \
"ds_read_b64_tr_b16 v[100:101], v6 offset:8192\n" \
"s_mov_b32 s66, 524416\n" \
"s_nop 0\n" \
"v_mfma_f32_16x16x32_bf16 v[208:211], v[80:83], v[24:27], v[208:211]\n" \
"ds_read_b64_tr_b16 v[102:103], v6 offset:12288\n" \
"buffer_load_dwordx4 v13, s[44:47], s66 offen offset:1024 lds\n" \
"v_cvt_pk_bf16_f32 v160, v160, v161\n" \
"s_waitcnt lgkmcnt(4)\n" \
"v_mfma_f32_16x16x32_bf16 v[212:215], v[88:91], v[24:27], 0\n" \
"ds_read_b64_tr_b16 v[104:105], v7 offset:0\n" \
"s_mov_b32 s67, 539648\n" \
"v_cvt_pk_bf16_f32 v161, v162, v163\n" \
"v_mfma_f32_16x16x32_bf16 v[212:215], v[92:95], v[28:31], v[212:215]\n" \
"ds_read_b64_tr_b16 v[106:107], v7 offset:4096\n" \
"buffer_load_dwordx4 v13, s[44:47], s67 offen offset:2048 lds\n" \
"v_cvt_pk_bf16_f32 v162, v164, v165\n" \
"v_mfma_f32_16x16x32_bf16 v[196:199], v[92:95], v[20:23], 0\n" \
"ds_read_b64_tr_b16 v[108:109], v7 offset:8192\n" \
"s_mov_b32 s68, 538752\n" \
"v_cvt_pk_bf16_f32 v163, v166, v167\n" \
"v_mfma_f32_16x16x32_bf16 v[196:199], v[88:91], v[16:19], v[196:199]\n" \
"ds_read_b64_tr_b16 v[110:111], v7 offset:12288\n" \
"buffer_load_dwordx4 v13, s[44:47], s68 offen offset:3072 lds\n" \
"buffer_store_dwordx4 v[160:163], v15, s[48:51], 0 offen offset:0 sc1\n" \
"s_waitcnt vmcnt(29) lgkmcnt(0)\n" \
"s_barrier\n" \
"v_mfma_f32_16x16x32_bf16 v[200:203], v[96:99], v[16:19], 0\n" \
"ds_read_b128 v[48:51], v126 offset:40960\n" \
"ds_read_b128 v[52:55], v126 offset:41984\n" \
"s_add_u32 m0, s72, 32768\n" \
"v_mfma_f32_16x16x32_bf16 v[200:203], v[100:103], v[20:23], v[200:203]\n" \
"ds_read_b128 v[56:59], v126 offset:43008\n" \
"ds_read_b128 v[60:63], v126 offset:44032\n" \
"buffer_load_dwordx4 v14, s[36:39], s64 offen offset:0 sc1 lds\n" \
"v_mfma_f32_16x16x32_bf16 v[216:219], v[100:103], v[28:31], 0\n" \
"v_cvt_pk_bf16_f32 v168, v168, v169\n" \
"v_cvt_pk_bf16_f32 v169, v170, v171\n" \
"v_cvt_pk_bf16_f32 v170, v172, v173\n" \
"v_mfma_f32_16x16x32_bf16 v[216:219], v[96:99], v[24:27], v[216:219]\n" \
"v_cvt_pk_bf16_f32 v171, v174, v175\n" \
"buffer_store_dwordx4 v[168:171], v15, s[48:51], 0 offen offset:1024 sc1\n" \
"v_cvt_pk_bf16_f32 v176, v176, v177\n" \
"v_mfma_f32_16x16x32_bf16 v[220:223], v[104:107], v[24:27], 0\n" \
"v_cvt_pk_bf16_f32 v177, v178, v179\n" \
"v_cvt_pk_bf16_f32 v178, v180, v181\n" \
"v_cvt_pk_bf16_f32 v179, v182, v183\n" \
"v_mfma_f32_16x16x32_bf16 v[220:223], v[108:111], v[28:31], v[220:223]\n" \
"buffer_load_dwordx4 v14, s[36:39], s64 offen offset:1024 sc1 lds\n" \
"s_add_u32 s64, s64, 16384\n" \
"buffer_store_dwordx4 v[176:179], v15, s[48:51], 0 offen offset:2048 sc1\n" \
"v_mfma_f32_16x16x32_bf16 v[204:207], v[108:111], v[20:23], 0\n" \
"v_cvt_pk_bf16_f32 v184, v184, v185\n" \
"v_cvt_pk_bf16_f32 v185, v186, v187\n" \
"v_cvt_pk_bf16_f32 v186, v188, v189\n" \
"v_mfma_f32_16x16x32_bf16 v[204:207], v[104:107], v[16:19], v[204:207]\n" \
"v_cvt_pk_bf16_f32 v187, v190, v191\n" \
"buffer_store_dwordx4 v[184:187], v15, s[48:51], 0 offen offset:3072 sc1\n" \
"s_waitcnt lgkmcnt(0)\n" \
"v_mfma_f32_16x16x32_bf16 v[192:195], a[0:3], v[48:51], v[192:195]\n" \
"s_add_u32 m0, s70, 0\n" \
"s_mov_b32 s65, 787456\n" \
"v_mfma_f32_16x16x32_bf16 v[192:195], a[4:7], v[52:55], v[192:195]\n" \
"buffer_load_dwordx4 v13, s[44:47], s65 offen offset:0 lds\n" \
"v_mfma_f32_16x16x32_bf16 v[208:211], a[4:7], v[60:63], v[208:211]\n" \
"s_mov_b32 s66, 786560\n" \
"v_mfma_f32_16x16x32_bf16 v[208:211], a[0:3], v[56:59], v[208:211]\n" \
"buffer_load_dwordx4 v13, s[44:47], s66 offen offset:1024 lds\n" \
"v_mfma_f32_16x16x32_bf16 v[212:215], a[8:11], v[56:59], v[212:215]\n" \
"s_mov_b32 s67, 801792\n" \
"v_mfma_f32_16x16x32_bf16 v[212:215], a[12:15], v[60:63], v[212:215]\n" \
"buffer_load_dwordx4 v13, s[44:47], s67 offen offset:2048 lds\n" \
"v_mfma_f32_16x16x32_bf16 v[196:199], a[12:15], v[52:55], v[196:199]\n" \
"s_mov_b32 s68, 800896\n" \
"v_mfma_f32_16x16x32_bf16 v[196:199], a[8:11], v[48:51], v[196:199]\n" \
"buffer_load_dwordx4 v13, s[44:47], s68 offen offset:3072 lds\n" \
"s_waitcnt vmcnt(26) lgkmcnt(0)\n" \
"s_barrier\n" \
"v_mfma_f32_16x16x32_bf16 v[200:203], a[16:19], v[48:51], v[200:203]\n" \
"ds_read_b64_tr_b16 v[80:81], v4 offset:16384\n" \
"ds_read_b64_tr_b16 v[82:83], v4 offset:20480\n" \
"s_add_u32 m0, s72, 40960\n" \
"v_mfma_f32_16x16x32_bf16 v[200:203], a[20:23], v[52:55], v[200:203]\n" \
"ds_read_b64_tr_b16 v[84:85], v4 offset:24576\n" \
"ds_read_b64_tr_b16 v[86:87], v4 offset:28672\n" \
"buffer_load_dwordx4 v14, s[36:39], s64 offen offset:0 sc1 lds\n" \
"v_mfma_f32_16x16x32_bf16 v[216:219], a[20:23], v[60:63], v[216:219]\n" \
"ds_read_b128 v[16:19], v126 offset:49152\n" \
"ds_read_b128 v[20:23], v126 offset:50176\n" \
"v_mfma_f32_16x16x32_bf16 v[216:219], a[16:19], v[56:59], v[216:219]\n" \
"ds_read_b128 v[24:27], v126 offset:51200\n" \
"ds_read_b128 v[28:31], v126 offset:52224\n" \
"v_mfma_f32_16x16x32_bf16 v[220:223], a[24:27], v[56:59], v[220:223]\n" \
"ds_read_b64_tr_b16 v[88:89], v5 offset:16384\n" \
"v_mfma_f32_16x16x32_bf16 v[220:223], a[28:31], v[60:63], v[220:223]\n" \
"ds_read_b64_tr_b16 v[90:91], v5 offset:20480\n" \
"buffer_load_dwordx4 v14, s[36:39], s64 offen offset:1024 sc1 lds\n" \
"s_add_u32 s64, s64, 16384\n" \
"v_mfma_f32_16x16x32_bf16 v[204:207], a[28:31], v[52:55], v[204:207]\n" \
"ds_read_b64_tr_b16 v[92:93], v5 offset:24576\n" \
"v_mfma_f32_16x16x32_bf16 v[204:207], a[24:27], v[48:51], v[204:207]\n" \
"ds_read_b64_tr_b16 v[94:95], v5 offset:28672\n" \
"s_waitcnt lgkmcnt(4)\n" \
"v_mfma_f32_16x16x32_bf16 v[192:195], v[80:83], v[16:19], v[192:195]\n" \
"ds_read_b64_tr_b16 v[96:97], v6 offset:16384\n" \
"v_mfma_f32_16x16x32_bf16 v[192:195], v[84:87], v[20:23], v[192:195]\n" \
"ds_read_b64_tr_b16 v[98:99], v6 offset:20480\n" \
"v_mfma_f32_16x16x32_bf16 v[208:211], v[84:87], v[28:31], v[208:211]\n" \
"ds_read_b64_tr_b16 v[100:101], v6 offset:24576\n" \
"v_mfma_f32_16x16x32_bf16 v[208:211], v[80:83], v[24:27], v[208:211]\n" \
"ds_read_b64_tr_b16 v[102:103], v6 offset:28672\n" \
"s_waitcnt lgkmcnt(4)\n" \
"v_mfma_f32_16x16x32_bf16 v[212:215], v[88:91], v[24:27], v[212:215]\n" \
"ds_read_b64_tr_b16 v[104:105], v7 offset:16384\n" \
"v_mfma_f32_16x16x32_bf16 v[212:215], v[92:95], v[28:31], v[212:215]\n" \
"ds_read_b64_tr_b16 v[106:107], v7 offset:20480\n" \
"v_mfma_f32_16x16x32_bf16 v[196:199], v[92:95], v[20:23], v[196:199]\n" \
"ds_read_b64_tr_b16 v[108:109], v7 offset:24576\n" \
"v_mfma_f32_16x16x32_bf16 v[196:199], v[88:91], v[16:19], v[196:199]\n" \
"ds_read_b64_tr_b16 v[110:111], v7 offset:28672\n" \
"s_waitcnt vmcnt(20) lgkmcnt(0)\n" \
"s_barrier\n" \
"v_mfma_f32_16x16x32_bf16 v[200:203], v[96:99], v[16:19], v[200:203]\n" \
"ds_read_b64_tr_b16 v[80:81], v8 offset:0\n" \
"ds_read_b64_tr_b16 v[82:83], v8 offset:4096\n" \
"s_add_u32 m0, s72, 49152\n" \
"v_mfma_f32_16x16x32_bf16 v[200:203], v[100:103], v[20:23], v[200:203]\n" \
"ds_read_b64_tr_b16 v[84:85], v8 offset:8192\n" \
"ds_read_b64_tr_b16 v[86:87], v8 offset:12288\n" \
"buffer_load_dwordx4 v14, s[36:39], s64 offen offset:0 sc1 lds\n" \
"v_mfma_f32_16x16x32_bf16 v[216:219], v[100:103], v[28:31], v[216:219]\n" \
"ds_read_b128 v[48:51], v126 offset:57344\n" \
"ds_read_b128 v[52:55], v126 offset:58368\n" \
"v_mfma_f32_16x16x32_bf16 v[216:219], v[96:99], v[24:27], v[216:219]\n" \
"ds_read_b128 v[56:59], v126 offset:59392\n" \
"ds_read_b128 v[60:63], v126 offset:60416\n" \
"v_mfma_f32_16x16x32_bf16 v[220:223], v[104:107], v[24:27], v[220:223]\n" \
"ds_read_b64_tr_b16 v[88:89], v9 offset:0\n" \
"v_mfma_f32_16x16x32_bf16 v[220:223], v[108:111], v[28:31], v[220:223]\n" \
"ds_read_b64_tr_b16 v[90:91], v9 offset:4096\n" \
"buffer_load_dwordx4 v14, s[36:39], s64 offen offset:1024 sc1 lds\n" \
"s_add_u32 s64, s64, 16384\n" \
"v_mfma_f32_16x16x32_bf16 v[204:207], v[108:111], v[20:23], v[204:207]\n" \
"ds_read_b64_tr_b16 v[92:93], v9 offset:8192\n" \
"v_mfma_f32_16x16x32_bf16 v[204:207], v[104:107], v[16:19], v[204:207]\n" \
"ds_read_b64_tr_b16 v[94:95], v9 offset:12288\n" \
"s_waitcnt lgkmcnt(4)\n" \
"v_mfma_f32_16x16x32_bf16 v[192:195], v[80:83], v[48:51], v[192:195]\n" \
"ds_read_b64_tr_b16 v[96:97], v10 offset:0\n" \
"s_add_u32 m0, s70, 16384\n" \
"s_mov_b32 s65, 918528\n" \
"v_mfma_f32_16x16x32_bf16 v[192:195], v[84:87], v[52:55], v[192:195]\n" \
"ds_read_b64_tr_b16 v[98:99], v10 offset:4096\n" \
"buffer_load_dwordx4 v13, s[44:47], s65 offen offset:0 lds\n" \
"v_mfma_f32_16x16x32_bf16 v[208:211], v[84:87], v[60:63], v[208:211]\n" \
"ds_read_b64_tr_b16 v[100:101], v10 offset:8192\n" \
"s_mov_b32 s66, 917632\n" \
"v_mfma_f32_16x16x32_bf16 v[208:211], v[80:83], v[56:59], v[208:211]\n" \
"ds_read_b64_tr_b16 v[102:103], v10 offset:12288\n" \
"buffer_load_dwordx4 v13, s[44:47], s66 offen offset:1024 lds\n" \
"s_waitcnt lgkmcnt(4)\n" \
"v_mfma_f32_16x16x32_bf16 v[212:215], v[88:91], v[56:59], v[212:215]\n" \
"ds_read_b64_tr_b16 v[104:105], v11 offset:0\n" \
"s_mov_b32 s67, 932864\n" \
"v_mfma_f32_16x16x32_bf16 v[212:215], v[92:95], v[60:63], v[212:215]\n" \
"ds_read_b64_tr_b16 v[106:107], v11 offset:4096\n" \
"buffer_load_dwordx4 v13, s[44:47], s67 offen offset:2048 lds\n" \
"v_mfma_f32_16x16x32_bf16 v[196:199], v[92:95], v[52:55], v[196:199]\n" \
"ds_read_b64_tr_b16 v[108:109], v11 offset:8192\n" \
"s_mov_b32 s68, 931968\n" \
"v_mfma_f32_16x16x32_bf16 v[196:199], v[88:91], v[48:51], v[196:199]\n" \
"ds_read_b64_tr_b16 v[110:111], v11 offset:12288\n" \
"buffer_load_dwordx4 v13, s[44:47], s68 offen offset:3072 lds\n" \
"s_waitcnt vmcnt(18) lgkmcnt(0)\n" \
"s_barrier\n" \
"v_mfma_f32_16x16x32_bf16 v[200:203], v[96:99], v[48:51], v[200:203]\n" \
"ds_read_b64_tr_b16 v[80:81], v8 offset:16384\n" \
"ds_read_b64_tr_b16 v[82:83], v8 offset:20480\n" \
"s_add_u32 m0, s72, 57344\n" \
"v_mfma_f32_16x16x32_bf16 v[200:203], v[100:103], v[52:55], v[200:203]\n" \
"ds_read_b64_tr_b16 v[84:85], v8 offset:24576\n" \
"ds_read_b64_tr_b16 v[86:87], v8 offset:28672\n" \
"buffer_load_dwordx4 v14, s[36:39], s64 offen offset:0 sc1 lds\n" \
"v_mfma_f32_16x16x32_bf16 v[216:219], v[100:103], v[60:63], v[216:219]\n" \
"ds_read_b128 v[16:19], v127 offset:32768\n" \
"ds_read_b128 v[20:23], v127 offset:33792\n" \
"v_mfma_f32_16x16x32_bf16 v[216:219], v[96:99], v[56:59], v[216:219]\n" \
"ds_read_b128 v[24:27], v127 offset:34816\n" \
"ds_read_b128 v[28:31], v127 offset:35840\n" \
"v_mfma_f32_16x16x32_bf16 v[220:223], v[104:107], v[56:59], v[220:223]\n" \
"ds_read_b64_tr_b16 v[88:89], v9 offset:16384\n" \
"v_mfma_f32_16x16x32_bf16 v[220:223], v[108:111], v[60:63], v[220:223]\n" \
"ds_read_b64_tr_b16 v[90:91], v9 offset:20480\n" \
"buffer_load_dwordx4 v14, s[36:39], s64 offen offset:1024 sc1 lds\n" \
"s_add_u32 s64, s64, 16384\n" \
"v_mfma_f32_16x16x32_bf16 v[204:207], v[108:111], v[52:55], v[204:207]\n" \
"ds_read_b64_tr_b16 v[92:93], v9 offset:24576\n" \
"v_mfma_f32_16x16x32_bf16 v[204:207], v[104:107], v[48:51], v[204:207]\n" \
"ds_read_b64_tr_b16 v[94:95], v9 offset:28672\n" \
"s_waitcnt lgkmcnt(4)\n" \
"v_mfma_f32_16x16x32_bf16 v[192:195], v[80:83], v[16:19], v[192:195]\n" \
"ds_read_b64_tr_b16 v[96:97], v10 offset:16384\n" \
"s_add_u32 m0, s70, 65536\n" \
"s_mov_b32 s65, 1049600\n" \
"v_mfma_f32_16x16x32_bf16 v[192:195], v[84:87], v[20:23], v[192:195]\n" \
"ds_read_b64_tr_b16 v[98:99], v10 offset:20480\n" \
"buffer_load_dwordx4 v13, s[44:47], s65 offen offset:0 lds\n" \
"v_mfma_f32_16x16x32_bf16 v[208:211], v[84:87], v[28:31], v[208:211]\n" \
"ds_read_b64_tr_b16 v[100:101], v10 offset:24576\n" \
"s_mov_b32 s66, 1048704\n" \
"v_mfma_f32_16x16x32_bf16 v[208:211], v[80:83], v[24:27], v[208:211]\n" \
"ds_read_b64_tr_b16 v[102:103], v10 offset:28672\n" \
"buffer_load_dwordx4 v13, s[44:47], s66 offen offset:1024 lds\n" \
"s_waitcnt lgkmcnt(4)\n" \
"v_mfma_f32_16x16x32_bf16 v[212:215], v[88:91], v[24:27], v[212:215]\n" \
"ds_read_b64_tr_b16 v[104:105], v11 offset:16384\n" \
"s_mov_b32 s67, 1063936\n" \
"v_mfma_f32_16x16x32_bf16 v[212:215], v[92:95], v[28:31], v[212:215]\n" \
"ds_read_b64_tr_b16 v[106:107], v11 offset:20480\n" \
"buffer_load_dwordx4 v13, s[44:47], s67 offen offset:2048 lds\n" \
"v_mfma_f32_16x16x32_bf16 v[196:199], v[92:95], v[20:23], v[196:199]\n" \
"ds_read_b64_tr_b16 v[108:109], v11 offset:24576\n" \
"s_mov_b32 s68, 1063040\n" \
"v_mfma_f32_16x16x32_bf16 v[196:199], v[88:91], v[16:19], v[196:199]\n" \
"ds_read_b64_tr_b16 v[110:111], v11 offset:28672\n" \
"buffer_load_dwordx4 v13, s[44:47], s68 offen offset:3072 lds\n" \
"s_waitcnt vmcnt(18) lgkmcnt(0)\n" \
"s_barrier\n" \
"buffer_store_dwordx2 v[120:121], v119, s[80:83], 0 offen sc1\n" \
"v_mfma_f32_16x16x32_bf16 v[200:203], v[96:99], v[16:19], v[200:203]\n" \
"ds_read_b128 v[48:51], v127 offset:40960\n" \
"ds_read_b128 v[52:55], v127 offset:41984\n" \
"s_add_u32 m0, s72, 98304\n" \
"v_mfma_f32_16x16x32_bf16 v[200:203], v[100:103], v[20:23], v[200:203]\n" \
"ds_read_b128 v[56:59], v127 offset:43008\n" \
"ds_read_b128 v[60:63], v127 offset:44032\n" \
"buffer_load_dwordx4 v14, s[36:39], s64 offen offset:0 sc1 lds\n" \
"v_mfma_f32_16x16x32_bf16 v[216:219], v[100:103], v[28:31], v[216:219]\n" \
"v_mfma_f32_16x16x32_bf16 v[216:219], v[96:99], v[24:27], v[216:219]\n" \
"v_mfma_f32_16x16x32_bf16 v[220:223], v[104:107], v[24:27], v[220:223]\n" \
"v_mfma_f32_16x16x32_bf16 v[220:223], v[108:111], v[28:31], v[220:223]\n" \
"buffer_load_dwordx4 v14, s[36:39], s64 offen offset:1024 sc1 lds\n" \
"s_add_u32 s64, s64, 16384\n" \
"v_mfma_f32_16x16x32_bf16 v[204:207], v[108:111], v[20:23], v[204:207]\n" \
"v_mfma_f32_16x16x32_bf16 v[204:207], v[104:107], v[16:19], v[204:207]\n" \
"s_waitcnt lgkmcnt(0)\n" \
"v_mfma_f32_16x16x32_bf16 v[192:195], a[32:35], v[48:51], v[192:195]\n" \
"s_add_u32 m0, s70, 81920\n" \
"s_mov_b32 s65, 1311744\n" \
"v_mfma_f32_16x16x32_bf16 v[192:195], a[36:39], v[52:55], v[192:195]\n" \
"buffer_load_dwordx4 v13, s[44:47], s65 offen offset:0 lds\n" \
"v_mfma_f32_16x16x32_bf16 v[208:211], a[36:39], v[60:63], v[208:211]\n" \
"s_mov_b32 s66, 1310848\n" \
"v_mfma_f32_16x16x32_bf16 v[208:211], a[32:35], v[56:59], v[208:211]\n" \
"buffer_load_dwordx4 v13, s[44:47], s66 offen offset:1024 lds\n" \
"v_mfma_f32_16x16x32_bf16 v[212:215], a[40:43], v[56:59], v[212:215]\n" \
"s_mov_b32 s67, 1326080\n" \
"v_mfma_f32_16x16x32_bf16 v[212:215], a[44:47], v[60:63], v[212:215]\n" \
"buffer_load_dwordx4 v13, s[44:47], s67 offen offset:2048 lds\n" \
"v_mfma_f32_16x16x32_bf16 v[196:199], a[44:47], v[52:55], v[196:199]\n" \
"s_mov_b32 s68, 1325184\n" \
"v_mfma_f32_16x16x32_bf16 v[196:199], a[40:43], v[48:51], v[196:199]\n" \
"buffer_load_dwordx4 v13, s[44:47], s68 offen offset:3072 lds\n" \
"s_waitcnt vmcnt(21) lgkmcnt(0)\n" \
"s_barrier\n" \
"v_mfma_f32_16x16x32_bf16 v[200:203], a[48:51], v[48:51], v[200:203]\n" \
"ds_read_b64_tr_b16 v[80:81], v4 offset:0\n" \
"ds_read_b64_tr_b16 v[82:83], v4 offset:4096\n" \
"s_add_u32 m0, s72, 106496\n" \
"v_mfma_f32_16x16x32_bf16 v[200:203], a[52:55], v[52:55], v[200:203]\n" \
"ds_read_b64_tr_b16 v[84:85], v4 offset:8192\n" \
"ds_read_b64_tr_b16 v[86:87], v4 offset:12288\n" \
"buffer_load_dwordx4 v14, s[36:39], s64 offen offset:0 sc1 lds\n" \
"v_mfma_f32_16x16x32_bf16 v[216:219], a[52:55], v[60:63], v[216:219]\n" \
"ds_read_b128 v[16:19], v127 offset:49152\n" \
"ds_read_b128 v[20:23], v127 offset:50176\n" \
"v_mfma_f32_16x16x32_bf16 v[216:219], a[48:51], v[56:59], v[216:219]\n" \
"ds_read_b128 v[24:27], v127 offset:51200\n" \
"ds_read_b128 v[28:31], v127 offset:52224\n" \
"v_mfma_f32_16x16x32_bf16 v[220:223], a[56:59], v[56:59], v[220:223]\n" \
"ds_read_b64_tr_b16 v[88:89], v5 offset:0\n" \
"v_mfma_f32_16x16x32_bf16 v[220:223], a[60:63], v[60:63], v[220:223]\n" \
"ds_read_b64_tr_b16 v[90:91], v5 offset:4096\n" \
"buffer_load_dwordx4 v14, s[36:39], s64 offen offset:1024 sc1 lds\n" \
"s_add_u32 s64, s64, 16384\n" \
"v_mfma_f32_16x16x32_bf16 v[204:207], a[60:63], v[52:55], v[204:207]\n" \
"ds_read_b64_tr_b16 v[92:93], v5 offset:8192\n" \
"v_mfma_f32_16x16x32_bf16 v[204:207], a[56:59], v[48:51], v[204:207]\n" \
"ds_read_b64_tr_b16 v[94:95], v5 offset:12288\n" \
"s_waitcnt lgkmcnt(4)\n" \
"v_mfma_f32_16x16x32_bf16 v[192:195], v[80:83], v[16:19], v[192:195]\n" \
"ds_read_b64_tr_b16 v[96:97], v6 offset:0\n" \
"v_mfma_f32_16x16x32_bf16 v[192:195], v[84:87], v[20:23], v[192:195]\n" \
"ds_read_b64_tr_b16 v[98:99], v6 offset:4096\n" \
"v_mfma_f32_16x16x32_bf16 v[208:211], v[84:87], v[28:31], v[208:211]\n" \
"ds_read_b64_tr_b16 v[100:101], v6 offset:8192\n" \
"v_mfma_f32_16x16x32_bf16 v[208:211], v[80:83], v[24:27], v[208:211]\n" \
"ds_read_b64_tr_b16 v[102:103], v6 offset:12288\n" \
"s_waitcnt lgkmcnt(4)\n" \
"v_mfma_f32_16x16x32_bf16 v[212:215], v[88:91], v[24:27], v[212:215]\n" \
"ds_read_b64_tr_b16 v[104:105], v7 offset:0\n" \
"v_mfma_f32_16x16x32_bf16 v[212:215], v[92:95], v[28:31], v[212:215]\n" \
"ds_read_b64_tr_b16 v[106:107], v7 offset:4096\n" \
"v_mfma_f32_16x16x32_bf16 v[196:199], v[92:95], v[20:23], v[196:199]\n" \
"ds_read_b64_tr_b16 v[108:109], v7 offset:8192\n" \
"v_mfma_f32_16x16x32_bf16 v[196:199], v[88:91], v[16:19], v[196:199]\n" \
"ds_read_b64_tr_b16 v[110:111], v7 offset:12288\n" \
"s_waitcnt vmcnt(15) lgkmcnt(0)\n" \
"s_barrier\n" \
"v_mfma_f32_16x16x32_bf16 v[200:203], v[96:99], v[16:19], v[200:203]\n" \
"ds_read_b64_tr_b16 v[80:81], v4 offset:16384\n" \
"ds_read_b64_tr_b16 v[82:83], v4 offset:20480\n" \
"s_add_u32 m0, s72, 114688\n" \
"v_mfma_f32_16x16x32_bf16 v[200:203], v[100:103], v[20:23], v[200:203]\n" \
"ds_read_b64_tr_b16 v[84:85], v4 offset:24576\n" \
"ds_read_b64_tr_b16 v[86:87], v4 offset:28672\n" \
"buffer_load_dwordx4 v14, s[36:39], s64 offen offset:0 sc1 lds\n" \
"v_mfma_f32_16x16x32_bf16 v[216:219], v[100:103], v[28:31], v[216:219]\n" \
"ds_read_b128 v[48:51], v127 offset:57344\n" \
"ds_read_b128 v[52:55], v127 offset:58368\n" \
"v_mfma_f32_16x16x32_bf16 v[216:219], v[96:99], v[24:27], v[216:219]\n" \
"ds_read_b128 v[56:59], v127 offset:59392\n" \
"ds_read_b128 v[60:63], v127 offset:60416\n" \
"v_mfma_f32_16x16x32_bf16 v[220:223], v[104:107], v[24:27], v[220:223]\n" \
"ds_read_b64_tr_b16 v[88:89], v5 offset:16384\n" \
"v_mfma_f32_16x16x32_bf16 v[220:223], v[108:111], v[28:31], v[220:223]\n" \
"ds_read_b64_tr_b16 v[90:91], v5 offset:20480\n" \
"buffer_load_dwordx4 v14, s[36:39], s64 offen offset:1024 sc1 lds\n" \
"s_add_u32 s64, s64, 16384\n" \
"v_mfma_f32_16x16x32_bf16 v[204:207], v[108:111], v[20:23], v[204:207]\n" \
"ds_read_b64_tr_b16 v[92:93], v5 offset:24576\n" \
"v_mfma_f32_16x16x32_bf16 v[204:207], v[104:107], v[16:19], v[204:207]\n" \
"ds_read_b64_tr_b16 v[94:95], v5 offset:28672\n" \
"s_waitcnt lgkmcnt(4)\n" \
"v_mfma_f32_16x16x32_bf16 v[192:195], v[80:83], v[48:51], v[192:195]\n" \
"ds_read_b64_tr_b16 v[96:97], v6 offset:16384\n" \
"s_add_u32 m0, s70, 0\n" \
"s_mov_b32 s65, 1442816\n" \
"v_mfma_f32_16x16x32_bf16 v[192:195], v[84:87], v[52:55], v[192:195]\n" \
"ds_read_b64_tr_b16 v[98:99], v6 offset:20480\n" \
"buffer_load_dwordx4 v13, s[44:47], s65 offen offset:0 lds\n" \
"v_mfma_f32_16x16x32_bf16 v[208:211], v[84:87], v[60:63], v[208:211]\n" \
"ds_read_b64_tr_b16 v[100:101], v6 offset:24576\n" \
"s_mov_b32 s66, 1441920\n" \
"v_mfma_f32_16x16x32_bf16 v[208:211], v[80:83], v[56:59], v[208:211]\n" \
"ds_read_b64_tr_b16 v[102:103], v6 offset:28672\n" \
"buffer_load_dwordx4 v13, s[44:47], s66 offen offset:1024 lds\n" \
"s_waitcnt lgkmcnt(4)\n" \
"v_mfma_f32_16x16x32_bf16 v[212:215], v[88:91], v[56:59], v[212:215]\n" \
"ds_read_b64_tr_b16 v[104:105], v7 offset:16384\n" \
"s_mov_b32 s67, 1457152\n" \
"v_mfma_f32_16x16x32_bf16 v[212:215], v[92:95], v[60:63], v[212:215]\n" \
"ds_read_b64_tr_b16 v[106:107], v7 offset:20480\n" \
"buffer_load_dwordx4 v13, s[44:47], s67 offen offset:2048 lds\n" \
"v_mfma_f32_16x16x32_bf16 v[196:199], v[92:95], v[52:55], v[196:199]\n" \
"ds_read_b64_tr_b16 v[108:109], v7 offset:24576\n" \
"s_mov_b32 s68, 1456256\n" \
"v_mfma_f32_16x16x32_bf16 v[196:199], v[88:91], v[48:51], v[196:199]\n" \
"ds_read_b64_tr_b16 v[110:111], v7 offset:28672\n" \
"buffer_load_dwordx4 v13, s[44:47], s68 offen offset:3072 lds\n" \
"s_waitcnt vmcnt(15) lgkmcnt(0)\n" \
"s_barrier\n" \
"v_mfma_f32_16x16x32_bf16 v[200:203], v[96:99], v[48:51], v[200:203]\n" \
"ds_read_b64_tr_b16 v[80:81], v8 offset:0\n" \
"ds_read_b64_tr_b16 v[82:83], v8 offset:4096\n" \
"s_add_u32 m0, s72, 122880\n" \
"v_mfma_f32_16x16x32_bf16 v[200:203], v[100:103], v[52:55], v[200:203]\n" \
"ds_read_b64_tr_b16 v[84:85], v8 offset:8192\n" \
"ds_read_b64_tr_b16 v[86:87], v8 offset:12288\n" \
"buffer_load_dwordx4 v14, s[36:39], s64 offen offset:0 sc1 lds\n" \
"v_mfma_f32_16x16x32_bf16 v[216:219], v[100:103], v[60:63], v[216:219]\n" \
"ds_read_b128 v[16:19], v126 offset:32768\n" \
"ds_read_b128 v[20:23], v126 offset:33792\n" \
"v_mfma_f32_16x16x32_bf16 v[216:219], v[96:99], v[56:59], v[216:219]\n" \
"ds_read_b128 v[24:27], v126 offset:34816\n" \
"ds_read_b128 v[28:31], v126 offset:35840\n" \
"v_mfma_f32_16x16x32_bf16 v[220:223], v[104:107], v[56:59], v[220:223]\n" \
"ds_read_b64_tr_b16 v[88:89], v9 offset:0\n" \
"v_mfma_f32_16x16x32_bf16 v[220:223], v[108:111], v[60:63], v[220:223]\n" \
"ds_read_b64_tr_b16 v[90:91], v9 offset:4096\n" \
"buffer_load_dwordx4 v14, s[36:39], s64 offen offset:1024 sc1 lds\n" \
"s_add_u32 s64, s64, 16384\n" \
"v_mfma_f32_16x16x32_bf16 v[204:207], v[108:111], v[52:55], v[204:207]\n" \
"ds_read_b64_tr_b16 v[92:93], v9 offset:8192\n" \
"v_mfma_f32_16x16x32_bf16 v[204:207], v[104:107], v[48:51], v[204:207]\n" \
"ds_read_b64_tr_b16 v[94:95], v9 offset:12288\n" \
"s_waitcnt lgkmcnt(4)\n" \
"v_mfma_f32_16x16x32_bf16 v[192:195], v[80:83], v[16:19], v[192:195]\n" \
"ds_read_b64_tr_b16 v[96:97], v10 offset:0\n" \
"s_add_u32 m0, s70, 16384\n" \
"s_mov_b32 s65, 1573888\n" \
"v_mfma_f32_16x16x32_bf16 v[192:195], v[84:87], v[20:23], v[192:195]\n" \
"ds_read_b64_tr_b16 v[98:99], v10 offset:4096\n" \
"buffer_load_dwordx4 v13, s[44:47], s65 offen offset:0 lds\n" \
"v_mfma_f32_16x16x32_bf16 v[208:211], v[84:87], v[28:31], v[208:211]\n" \
"ds_read_b64_tr_b16 v[100:101], v10 offset:8192\n" \
"s_mov_b32 s66, 1572992\n" \
"v_mfma_f32_16x16x32_bf16 v[208:211], v[80:83], v[24:27], v[208:211]\n" \
"ds_read_b64_tr_b16 v[102:103], v10 offset:12288\n" \
"buffer_load_dwordx4 v13, s[44:47], s66 offen offset:1024 lds\n" \
"s_waitcnt lgkmcnt(4)\n" \
"v_mfma_f32_16x16x32_bf16 v[212:215], v[88:91], v[24:27], v[212:215]\n" \
"ds_read_b64_tr_b16 v[104:105], v11 offset:0\n" \
"s_mov_b32 s67, 1588224\n" \
"v_mfma_f32_16x16x32_bf16 v[212:215], v[92:95], v[28:31], v[212:215]\n" \
"ds_read_b64_tr_b16 v[106:107], v11 offset:4096\n" \
"buffer_load_dwordx4 v13, s[44:47], s67 offen offset:2048 lds\n" \
"v_mfma_f32_16x16x32_bf16 v[196:199], v[92:95], v[20:23], v[196:199]\n" \
"ds_read_b64_tr_b16 v[108:109], v11 offset:8192\n" \
"s_mov_b32 s68, 1587328\n" \
"v_mfma_f32_16x16x32_bf16 v[196:199], v[88:91], v[16:19], v[196:199]\n" \
"ds_read_b64_tr_b16 v[110:111], v11 offset:12288\n" \
"buffer_load_dwordx4 v13, s[44:47], s68 offen offset:3072 lds\n" \
"s_waitcnt vmcnt(33) lgkmcnt(0)\n" \
"s_barrier\n" \
"s_cmp_lg_u32 s62, 0\n" \
"s_cbranch_scc1 L_w0px_%=\n" \
"buffer_load_dwordx2 v[112:113], v122, s[80:83], s60 offen offset:1024 sc1\n" \
"L_w0px_%=:\n" \
"v_mfma_f32_16x16x32_bf16 v[200:203], v[96:99], v[16:19], v[200:203]\n" \
"ds_read_b128 v[48:51], v126 offset:40960\n" \
"ds_read_b128 v[52:55], v126 offset:41984\n" \
"v_mfma_f32_16x16x32_bf16 v[200:203], v[100:103], v[20:23], v[200:203]\n" \
"ds_read_b128 v[56:59], v126 offset:43008\n" \
"ds_read_b128 v[60:63], v126 offset:44032\n" \
"v_mfma_f32_16x16x32_bf16 v[216:219], v[100:103], v[28:31], v[216:219]\n" \
"v_mfma_f32_16x16x32_bf16 v[216:219], v[96:99], v[24:27], v[216:219]\n" \
"v_mfma_f32_16x16x32_bf16 v[220:223], v[104:107], v[24:27], v[220:223]\n" \
"v_mfma_f32_16x16x32_bf16 v[220:223], v[108:111], v[28:31], v[220:223]\n" \
"v_mfma_f32_16x16x32_bf16 v[204:207], v[108:111], v[20:23], v[204:207]\n" \
"v_mfma_f32_16x16x32_bf16 v[204:207], v[104:107], v[16:19], v[204:207]\n" \
"s_waitcnt lgkmcnt(0)\n" \
"v_mfma_f32_16x16x32_bf16 v[192:195], a[64:67], v[48:51], v[192:195]\n" \
"s_add_u32 m0, s70, 65536\n" \
"s_mov_b32 s65, 1836032\n" \
"v_mfma_f32_16x16x32_bf16 v[192:195], a[68:71], v[52:55], v[192:195]\n" \
"buffer_load_dwordx4 v13, s[44:47], s65 offen offset:0 lds\n" \
"v_mfma_f32_16x16x32_bf16 v[208:211], a[68:71], v[60:63], v[208:211]\n" \
"s_mov_b32 s66, 1835136\n" \
"v_mfma_f32_16x16x32_bf16 v[208:211], a[64:67], v[56:59], v[208:211]\n" \
"buffer_load_dwordx4 v13, s[44:47], s66 offen offset:1024 lds\n" \
"v_mfma_f32_16x16x32_bf16 v[212:215], a[72:75], v[56:59], v[212:215]\n" \
"s_mov_b32 s67, 1850368\n" \
"v_mfma_f32_16x16x32_bf16 v[212:215], a[76:79], v[60:63], v[212:215]\n" \
"buffer_load_dwordx4 v13, s[44:47], s67 offen offset:2048 lds\n" \
"v_mfma_f32_16x16x32_bf16 v[196:199], a[76:79], v[52:55], v[196:199]\n" \
"s_mov_b32 s68, 1849472\n" \
"v_mfma_f32_16x16x32_bf16 v[196:199], a[72:75], v[48:51], v[196:199]\n" \
"buffer_load_dwordx4 v13, s[44:47], s68 offen offset:3072 lds\n" \
"s_waitcnt vmcnt(18) lgkmcnt(0)\n" \
"s_barrier\n" \
"v_mfma_f32_16x16x32_bf16 v[200:203], a[80:83], v[48:51], v[200:203]\n" \
"ds_read_b64_tr_b16 v[80:81], v8 offset:16384\n" \
"ds_read_b64_tr_b16 v[82:83], v8 offset:20480\n" \
"v_mfma_f32_16x16x32_bf16 v[200:203], a[84:87], v[52:55], v[200:203]\n" \
"ds_read_b64_tr_b16 v[84:85], v8 offset:24576\n" \
"ds_read_b64_tr_b16 v[86:87], v8 offset:28672\n" \
"v_mfma_f32_16x16x32_bf16 v[216:219], a[84:87], v[60:63], v[216:219]\n" \
"ds_read_b128 v[16:19], v126 offset:49152\n" \
"ds_read_b128 v[20:23], v126 offset:50176\n" \
"v_mfma_f32_16x16x32_bf16 v[216:219], a[80:83], v[56:59], v[216:219]\n" \
"ds_read_b128 v[24:27], v126 offset:51200\n" \
"ds_read_b128 v[28:31], v126 offset:52224\n" \
"v_mfma_f32_16x16x32_bf16 v[220:223], a[88:91], v[56:59], v[220:223]\n" \
"ds_read_b64_tr_b16 v[88:89], v9 offset:16384\n" \
"v_mfma_f32_16x16x32_bf16 v[220:223], a[92:95], v[60:63], v[220:223]\n" \
"ds_read_b64_tr_b16 v[90:91], v9 offset:20480\n" \
"v_mfma_f32_16x16x32_bf16 v[204:207], a[92:95], v[52:55], v[204:207]\n" \
"ds_read_b64_tr_b16 v[92:93], v9 offset:24576\n" \
"v_mfma_f32_16x16x32_bf16 v[204:207], a[88:91], v[48:51], v[204:207]\n" \
"ds_read_b64_tr_b16 v[94:95], v9 offset:28672\n" \
"s_waitcnt lgkmcnt(4)\n" \
"v_mfma_f32_16x16x32_bf16 v[192:195], v[80:83], v[16:19], v[192:195]\n" \
"ds_read_b64_tr_b16 v[96:97], v10 offset:16384\n" \
"v_mfma_f32_16x16x32_bf16 v[192:195], v[84:87], v[20:23], v[192:195]\n" \
"ds_read_b64_tr_b16 v[98:99], v10 offset:20480\n" \
"v_mfma_f32_16x16x32_bf16 v[208:211], v[84:87], v[28:31], v[208:211]\n" \
"ds_read_b64_tr_b16 v[100:101], v10 offset:24576\n" \
"v_mfma_f32_16x16x32_bf16 v[208:211], v[80:83], v[24:27], v[208:211]\n" \
"ds_read_b64_tr_b16 v[102:103], v10 offset:28672\n" \
"s_waitcnt lgkmcnt(4)\n" \
"v_mfma_f32_16x16x32_bf16 v[212:215], v[88:91], v[24:27], v[212:215]\n" \
"ds_read_b64_tr_b16 v[104:105], v11 offset:16384\n" \
"v_mfma_f32_16x16x32_bf16 v[212:215], v[92:95], v[28:31], v[212:215]\n" \
"ds_read_b64_tr_b16 v[106:107], v11 offset:20480\n" \
"v_mfma_f32_16x16x32_bf16 v[196:199], v[92:95], v[20:23], v[196:199]\n" \
"ds_read_b64_tr_b16 v[108:109], v11 offset:24576\n" \
"v_mfma_f32_16x16x32_bf16 v[196:199], v[88:91], v[16:19], v[196:199]\n" \
"ds_read_b64_tr_b16 v[110:111], v11 offset:28672\n" \
"s_waitcnt vmcnt(10) lgkmcnt(0)\n" \
"s_barrier\n" \
"v_mfma_f32_16x16x32_bf16 v[200:203], v[96:99], v[16:19], v[200:203]\n" \
"ds_read_b64_tr_b16 v[80:81], v4 offset:0\n" \
"ds_read_b64_tr_b16 v[82:83], v4 offset:4096\n" \
"v_mfma_f32_16x16x32_bf16 v[200:203], v[100:103], v[20:23], v[200:203]\n" \
"ds_read_b64_tr_b16 v[84:85], v4 offset:8192\n" \
"ds_read_b64_tr_b16 v[86:87], v4 offset:12288\n" \
"v_mfma_f32_16x16x32_bf16 v[216:219], v[100:103], v[28:31], v[216:219]\n" \
"ds_read_b128 v[48:51], v126 offset:57344\n" \
"ds_read_b128 v[52:55], v126 offset:58368\n" \
"v_mfma_f32_16x16x32_bf16 v[216:219], v[96:99], v[24:27], v[216:219]\n" \
"ds_read_b128 v[56:59], v126 offset:59392\n" \
"ds_read_b128 v[60:63], v126 offset:60416\n" \
"v_mfma_f32_16x16x32_bf16 v[220:223], v[104:107], v[24:27], v[220:223]\n" \
"ds_read_b64_tr_b16 v[88:89], v5 offset:0\n" \
"v_mfma_f32_16x16x32_bf16 v[220:223], v[108:111], v[28:31], v[220:223]\n" \
"ds_read_b64_tr_b16 v[90:91], v5 offset:4096\n" \
"v_mfma_f32_16x16x32_bf16 v[204:207], v[108:111], v[20:23], v[204:207]\n" \
"ds_read_b64_tr_b16 v[92:93], v5 offset:8192\n" \
"v_mfma_f32_16x16x32_bf16 v[204:207], v[104:107], v[16:19], v[204:207]\n" \
"ds_read_b64_tr_b16 v[94:95], v5 offset:12288\n" \
"s_waitcnt lgkmcnt(4)\n" \
"v_mfma_f32_16x16x32_bf16 v[192:195], v[80:83], v[48:51], v[192:195]\n" \
"ds_read_b64_tr_b16 v[96:97], v6 offset:0\n" \
"s_add_u32 m0, s70, 81920\n" \
"s_mov_b32 s65, 1967104\n" \
"v_mfma_f32_16x16x32_bf16 v[192:195], v[84:87], v[52:55], v[192:195]\n" \
"ds_read_b64_tr_b16 v[98:99], v6 offset:4096\n" \
"buffer_load_dwordx4 v13, s[44:47], s65 offen offset:0 lds\n" \
"v_mfma_f32_16x16x32_bf16 v[208:211], v[84:87], v[60:63], v[208:211]\n" \
"ds_read_b64_tr_b16 v[100:101], v6 offset:8192\n" \
"s_mov_b32 s66, 1966208\n" \
"v_mfma_f32_16x16x32_bf16 v[208:211], v[80:83], v[56:59], v[208:211]\n" \
"ds_read_b64_tr_b16 v[102:103], v6 offset:12288\n" \
"buffer_load_dwordx4 v13, s[44:47], s66 offen offset:1024 lds\n" \
"s_waitcnt lgkmcnt(4)\n" \
"v_mfma_f32_16x16x32_bf16 v[212:215], v[88:91], v[56:59], v[212:215]\n" \
"ds_read_b64_tr_b16 v[104:105], v7 offset:0\n" \
"s_mov_b32 s67, 1981440\n" \
"v_mfma_f32_16x16x32_bf16 v[212:215], v[92:95], v[60:63], v[212:215]\n" \
"ds_read_b64_tr_b16 v[106:107], v7 offset:4096\n" \
"buffer_load_dwordx4 v13, s[44:47], s67 offen offset:2048 lds\n" \
"v_mfma_f32_16x16x32_bf16 v[196:199], v[92:95], v[52:55], v[196:199]\n" \
"ds_read_b64_tr_b16 v[108:109], v7 offset:8192\n" \
"s_mov_b32 s68, 1980544\n" \
"v_mfma_f32_16x16x32_bf16 v[196:199], v[88:91], v[48:51], v[196:199]\n" \
"ds_read_b64_tr_b16 v[110:111], v7 offset:12288\n" \
"buffer_load_dwordx4 v13, s[44:47], s68 offen offset:3072 lds\n" \
"s_waitcnt vmcnt(8) lgkmcnt(0)\n" \
"s_barrier\n" \
"v_mfma_f32_16x16x32_bf16 v[200:203], v[96:99], v[48:51], v[200:203]\n" \
"ds_read_b64_tr_b16 v[80:81], v4 offset:16384\n" \
"ds_read_b64_tr_b16 v[82:83], v4 offset:20480\n" \
"v_mfma_f32_16x16x32_bf16 v[200:203], v[100:103], v[52:55], v[200:203]\n" \
"ds_read_b64_tr_b16 v[84:85], v4 offset:24576\n" \
"ds_read_b64_tr_b16 v[86:87], v4 offset:28672\n" \
"v_mfma_f32_16x16x32_bf16 v[216:219], v[100:103], v[60:63], v[216:219]\n" \
"ds_read_b128 v[16:19], v127 offset:32768\n" \
"ds_read_b128 v[20:23], v127 offset:33792\n" \
"v_mfma_f32_16x16x32_bf16 v[216:219], v[96:99], v[56:59], v[216:219]\n" \
"ds_read_b128 v[24:27], v127 offset:34816\n" \
"ds_read_b128 v[28:31], v127 offset:35840\n" \
"v_mfma_f32_16x16x32_bf16 v[220:223], v[104:107], v[56:59], v[220:223]\n" \
"ds_read_b64_tr_b16 v[88:89], v5 offset:16384\n" \
"v_mfma_f32_16x16x32_bf16 v[220:223], v[108:111], v[60:63], v[220:223]\n" \
"ds_read_b64_tr_b16 v[90:91], v5 offset:20480\n" \
"v_mfma_f32_16x16x32_bf16 v[204:207], v[108:111], v[52:55], v[204:207]\n" \
"ds_read_b64_tr_b16 v[92:93], v5 offset:24576\n" \
"v_mfma_f32_16x16x32_bf16 v[204:207], v[104:107], v[48:51], v[204:207]\n" \
"ds_read_b64_tr_b16 v[94:95], v5 offset:28672\n" \
"s_waitcnt lgkmcnt(4)\n" \
"v_mfma_f32_16x16x32_bf16 v[192:195], v[80:83], v[16:19], v[192:195]\n" \
"ds_read_b64_tr_b16 v[96:97], v6 offset:16384\n" \
"v_mfma_f32_16x16x32_bf16 v[192:195], v[84:87], v[20:23], v[192:195]\n" \
"ds_read_b64_tr_b16 v[98:99], v6 offset:20480\n" \
"v_mfma_f32_16x16x32_bf16 v[208:211], v[84:87], v[28:31], v[208:211]\n" \
"ds_read_b64_tr_b16 v[100:101], v6 offset:24576\n" \
"v_mfma_f32_16x16x32_bf16 v[208:211], v[80:83], v[24:27], v[208:211]\n" \
"ds_read_b64_tr_b16 v[102:103], v6 offset:28672\n" \
"s_waitcnt lgkmcnt(4)\n" \
"v_mfma_f32_16x16x32_bf16 v[212:215], v[88:91], v[24:27], v[212:215]\n" \
"ds_read_b64_tr_b16 v[104:105], v7 offset:16384\n" \
"v_mfma_f32_16x16x32_bf16 v[212:215], v[92:95], v[28:31], v[212:215]\n" \
"ds_read_b64_tr_b16 v[106:107], v7 offset:20480\n" \
"v_mfma_f32_16x16x32_bf16 v[196:199], v[92:95], v[20:23], v[196:199]\n" \
"ds_read_b64_tr_b16 v[108:109], v7 offset:24576\n" \
"v_mfma_f32_16x16x32_bf16 v[196:199], v[88:91], v[16:19], v[196:199]\n" \
"ds_read_b64_tr_b16 v[110:111], v7 offset:28672\n" \
"s_waitcnt vmcnt(8) lgkmcnt(0)\n" \
"s_cmp_lg_u32 s62, 0\n" \
"s_cbranch_scc1 L_w0sx_%=\n" \
"v_cmp_eq_u64 vcc, s[28:29], v[112:113]\n" \
"s_nop 1\n" \
"s_cmp_eq_u64 vcc, -1\n" \
"s_cbranch_scc1 L_okx_%=\n" \
"s_mov_b32 s76, 0x40000\n" \
"L_spx_%=:\n" \
"buffer_load_dwordx2 v[112:113], v122, s[80:83], s60 offen offset:1024 sc1\n" \
"s_waitcnt vmcnt(0)\n" \
"v_cmp_eq_u64 vcc, s[28:29], v[112:113]\n" \
"s_nop 1\n" \
"s_cmp_eq_u64 vcc, -1\n" \
"s_cbranch_scc1 L_okx_%=\n" \
"s_sleep 1\n" \
"s_sub_u32 s76, s76, 1\n" \
"s_cmp_lg_u32 s76, 0\n" \
"s_cbranch_scc1 L_spx_%=\n" \
"s_mov_b32 s77, 1\n" \
"L_okx_%=:\n" \
"buffer_store_dwordx2 v[122:123], v122, s[80:83], s60 offen offset:1024 sc1\n" \
"L_w0sx_%=:\n" \
"s_barrier\n" \
"buffer_load_dwordx4 v[164:167], v15, s[52:55], 0 offen offset:0 sc1\n" \
"buffer_load_dwordx4 v[172:175], v15, s[52:55], 0 offen offset:1024 sc1\n" \
"buffer_load_dwordx4 v[180:183], v15, s[52:55], 0 offen offset:2048 sc1\n" \
"buffer_load_dwordx4 v[188:191], v15, s[52:55], 0 offen offset:3072 sc1\n" \
"v_mfma_f32_16x16x32_bf16 v[200:203], v[96:99], v[16:19], v[200:203]\n" \
"ds_read_b128 v[48:51], v127 offset:40960\n" \
"ds_read_b128 v[52:55], v127 offset:41984\n" \
"v_mfma_f32_16x16x32_bf16 v[200:203], v[100:103], v[20:23], v[200:203]\n" \
"ds_read_b128 v[56:59], v127 offset:43008\n" \
"ds_read_b128 v[60:63], v127 offset:44032\n" \
"v_mfma_f32_16x16x32_bf16 v[216:219], v[100:103], v[28:31], v[216:219]\n" \
"v_mfma_f32_16x16x32_bf16 v[216:219], v[96:99], v[24:27], v[216:219]\n" \
"v_mfma_f32_16x16x32_bf16 v[220:223], v[104:107], v[24:27], v[220:223]\n" \
"v_mfma_f32_16x16x32_bf16 v[220:223], v[108:111], v[28:31], v[220:223]\n" \
"v_mfma_f32_16x16x32_bf16 v[204:207], v[108:111], v[20:23], v[204:207]\n" \
"v_mfma_f32_16x16x32_bf16 v[204:207], v[104:107], v[16:19], v[204:207]\n" \
"s_waitcnt lgkmcnt(0)\n" \
"v_mfma_f32_16x16x32_bf16 v[192:195], a[96:99], v[48:51], v[192:195]\n" \
"v_mfma_f32_16x16x32_bf16 v[192:195], a[100:103], v[52:55], v[192:195]\n" \
"v_mfma_f32_16x16x32_bf16 v[208:211], a[100:103], v[60:63], v[208:211]\n" \
"v_mfma_f32_16x16x32_bf16 v[208:211], a[96:99], v[56:59], v[208:211]\n" \
"v_mfma_f32_16x16x32_bf16 v[212:215], a[104:107], v[56:59], v[212:215]\n" \
"v_mfma_f32_16x16x32_bf16 v[212:215], a[108:111], v[60:63], v[212:215]\n" \
"v_mfma_f32_16x16x32_bf16 v[196:199], a[108:111], v[52:55], v[196:199]\n" \
"v_mfma_f32_16x16x32_bf16 v[196:199], a[104:107], v[48:51], v[196:199]\n" \
"s_waitcnt vmcnt(8) lgkmcnt(0)\n" \
"s_barrier\n" \
"v_mfma_f32_16x16x32_bf16 v[200:203], a[112:115], v[48:51], v[200:203]\n" \
"ds_read_b64_tr_b16 v[80:81], v8 offset:0\n" \
"ds_read_b64_tr_b16 v[82:83], v8 offset:4096\n" \
"v_mfma_f32_16x16x32_bf16 v[200:203], a[116:119], v[52:55], v[200:203]\n" \
"ds_read_b64_tr_b16 v[84:85], v8 offset:8192\n" \
"ds_read_b64_tr_b16 v[86:87], v8 offset:12288\n" \
"v_mfma_f32_16x16x32_bf16 v[216:219], a[116:119], v[60:63], v[216:219]\n" \
"ds_read_b128 v[16:19], v127 offset:49152\n" \
"ds_read_b128 v[20:23], v127 offset:50176\n" \
"v_mfma_f32_16x16x32_bf16 v[216:219], a[112:115], v[56:59], v[216:219]\n" \
"ds_read_b128 v[24:27], v127 offset:51200\n" \
"ds_read_b128 v[28:31], v127 offset:52224\n" \
"v_mfma_f32_16x16x32_bf16 v[220:223], a[120:123], v[56:59], v[220:223]\n" \
"ds_read_b64_tr_b16 v[88:89], v9 offset:0\n" \
"v_mfma_f32_16x16x32_bf16 v[220:223], a[124:127], v[60:63], v[220:223]\n" \
"ds_read_b64_tr_b16 v[90:91], v9 offset:4096\n" \
"v_mfma_f32_16x16x32_bf16 v[204:207], a[124:127], v[52:55], v[204:207]\n" \
"ds_read_b64_tr_b16 v[92:93], v9 offset:8192\n" \
"v_mfma_f32_16x16x32_bf16 v[204:207], a[120:123], v[48:51], v[204:207]\n" \
"ds_read_b64_tr_b16 v[94:95], v9 offset:12288\n" \
"s_waitcnt lgkmcnt(4)\n" \
"v_mfma_f32_16x16x32_bf16 v[192:195], v[80:83], v[16:19], v[192:195]\n" \
"ds_read_b64_tr_b16 v[96:97], v10 offset:0\n" \
"v_mfma_f32_16x16x32_bf16 v[192:195], v[84:87], v[20:23], v[192:195]\n" \
"ds_read_b64_tr_b16 v[98:99], v10 offset:4096\n" \
"v_mfma_f32_16x16x32_bf16 v[208:211], v[84:87], v[28:31], v[208:211]\n" \
"ds_read_b64_tr_b16 v[100:101], v10 offset:8192\n" \
"v_mfma_f32_16x16x32_bf16 v[208:211], v[80:83], v[24:27], v[208:211]\n" \
"ds_read_b64_tr_b16 v[102:103], v10 offset:12288\n" \
"s_waitcnt lgkmcnt(4)\n" \
"v_mfma_f32_16x16x32_bf16 v[212:215], v[88:91], v[24:27], v[212:215]\n" \
"ds_read_b64_tr_b16 v[104:105], v11 offset:0\n" \
"v_mfma_f32_16x16x32_bf16 v[212:215], v[92:95], v[28:31], v[212:215]\n" \
"ds_read_b64_tr_b16 v[106:107], v11 offset:4096\n" \
"v_mfma_f32_16x16x32_bf16 v[196:199], v[92:95], v[20:23], v[196:199]\n" \
"ds_read_b64_tr_b16 v[108:109], v11 offset:8192\n" \
"v_mfma_f32_16x16x32_bf16 v[196:199], v[88:91], v[16:19], v[196:199]\n" \
"ds_read_b64_tr_b16 v[110:111], v11 offset:12288\n" \
"s_waitcnt vmcnt(4) lgkmcnt(0)\n" \
"s_barrier\n" \
"v_mfma_f32_16x16x32_bf16 v[200:203], v[96:99], v[16:19], v[200:203]\n" \
"ds_read_b64_tr_b16 v[80:81], v8 offset:16384\n" \
"ds_read_b64_tr_b16 v[82:83], v8 offset:20480\n" \
"v_mfma_f32_16x16x32_bf16 v[200:203], v[100:103], v[20:23], v[200:203]\n" \
"ds_read_b64_tr_b16 v[84:85], v8 offset:24576\n" \
"ds_read_b64_tr_b16 v[86:87], v8 offset:28672\n" \
"v_mfma_f32_16x16x32_bf16 v[216:219], v[100:103], v[28:31], v[216:219]\n" \
"ds_read_b128 v[48:51], v127 offset:57344\n" \
"ds_read_b128 v[52:55], v127 offset:58368\n" \
"v_mfma_f32_16x16x32_bf16 v[216:219], v[96:99], v[24:27], v[216:219]\n" \
"ds_read_b128 v[56:59], v127 offset:59392\n" \
"ds_read_b128 v[60:63], v127 offset:60416\n" \
"v_mfma_f32_16x16x32_bf16 v[220:223], v[104:107], v[24:27], v[220:223]\n" \
"ds_read_b64_tr_b16 v[88:89], v9 offset:16384\n" \
"v_mfma_f32_16x16x32_bf16 v[220:223], v[108:111], v[28:31], v[220:223]\n" \
"ds_read_b64_tr_b16 v[90:91], v9 offset:20480\n" \
"v_mfma_f32_16x16x32_bf16 v[204:207], v[108:111], v[20:23], v[204:207]\n" \
"ds_read_b64_tr_b16 v[92:93], v9 offset:24576\n" \
"v_mfma_f32_16x16x32_bf16 v[204:207], v[104:107], v[16:19], v[204:207]\n" \
"ds_read_b64_tr_b16 v[94:95], v9 offset:28672\n" \
"s_waitcnt lgkmcnt(4)\n" \
"v_mfma_f32_16x16x32_bf16 v[192:195], v[80:83], v[48:51], v[192:195]\n" \
"ds_read_b64_tr_b16 v[96:97], v10 offset:16384\n" \
"v_mfma_f32_16x16x32_bf16 v[192:195], v[84:87], v[52:55], v[192:195]\n" \
"ds_read_b64_tr_b16 v[98:99], v10 offset:20480\n" \
"v_mfma_f32_16x16x32_bf16 v[208:211], v[84:87], v[60:63], v[208:211]\n" \
"ds_read_b64_tr_b16 v[100:101], v10 offset:24576\n" \
"v_mfma_f32_16x16x32_bf16 v[208:211], v[80:83], v[56:59], v[208:211]\n" \
"ds_read_b64_tr_b16 v[102:103], v10 offset:28672\n" \
"s_waitcnt lgkmcnt(4)\n" \
"v_mfma_f32_16x16x32_bf16 v[212:215], v[88:91], v[56:59], v[212:215]\n" \
"ds_read_b64_tr_b16 v[104:105], v11 offset:16384\n" \
"v_mfma_f32_16x16x32_bf16 v[212:215], v[92:95], v[60:63], v[212:215]\n" \
"ds_read_b64_tr_b16 v[106:107], v11 offset:20480\n" \
"v_mfma_f32_16x16x32_bf16 v[196:199], v[92:95], v[52:55], v[196:199]\n" \
"ds_read_b64_tr_b16 v[108:109], v11 offset:24576\n" \
"v_mfma_f32_16x16x32_bf16 v[196:199], v[88:91], v[48:51], v[196:199]\n" \
"ds_read_b64_tr_b16 v[110:111], v11 offset:28672\n" \
"s_waitcnt lgkmcnt(0)\n" \
"v_mfma_f32_16x16x32_bf16 v[200:203], v[96:99], v[48:51], v[200:203]\n" \
"v_mfma_f32_16x16x32_bf16 v[200:203], v[100:103], v[52:55], v[200:203]\n" \
"v_mfma_f32_16x16x32_bf16 v[216:219], v[100:103], v[60:63], v[216:219]\n" \
"v_mfma_f32_16x16x32_bf16 v[216:219], v[96:99], v[56:59], v[216:219]\n" \
"v_mfma_f32_16x16x32_bf16 v[220:223], v[104:107], v[56:59], v[220:223]\n" \
"v_mfma_f32_16x16x32_bf16 v[220:223], v[108:111], v[60:63], v[220:223]\n" \
"v_mfma_f32_16x16x32_bf16 v[204:207], v[108:111], v[52:55], v[204:207]\n" \
"v_mfma_f32_16x16x32_bf16 v[204:207], v[104:107], v[48:51], v[204:207]\n" \
"v_accvgpr_read_b32 v64, a224\n" \
"v_accvgpr_read_b32 v65, a225\n" \
"v_accvgpr_read_b32 v66, a226\n" \
"v_accvgpr_read_b32 v67, a227\n" \
"v_accvgpr_read_b32 v68, a228\n" \
"v_accvgpr_read_b32 v69, a229\n" \
"v_accvgpr_read_b32 v70, a230\n" \
"v_accvgpr_read_b32 v71, a231\n" \
"v_accvgpr_read_b32 v72, a232\n" \
"v_accvgpr_read_b32 v73, a233\n" \
"v_accvgpr_read_b32 v74, a234\n" \
"v_accvgpr_read_b32 v75, a235\n" \
"v_accvgpr_read_b32 v76, a236\n" \
"v_accvgpr_read_b32 v77, a237\n" \
"v_accvgpr_read_b32 v78, a238\n" \
"v_accvgpr_read_b32 v79, a239\n" \
"s_mov_b32 s36, 0xbfb8aa3b\n" \
"s_mov_b32 s37, 0xbfb8aa3b\n" \
"s_mov_b32 s38, 1.0\n" \
"s_mov_b32 s39, 1.0\n" \
"s_mov_b32 s40, 0xffff0000\n" \
"v_permlane16_swap_b32 v64, v66\n" \
"v_permlane16_swap_b32 v65, v67\n" \
"v_permlane16_swap_b32 v68, v70\n" \
"v_permlane16_swap_b32 v69, v71\n" \
"v_permlane16_swap_b32 v72, v74\n" \
"v_permlane16_swap_b32 v73, v75\n" \
"v_permlane16_swap_b32 v76, v78\n" \
"v_permlane16_swap_b32 v77, v79\n" \
"v_lshlrev_b32 v16, 16, v64\n" \
"v_and_b32 v17, 0xffff0000, v64\n" \
"v_lshlrev_b32 v18, 16, v65\n" \
"v_and_b32 v19, 0xffff0000, v65\n" \
"v_lshlrev_b32 v20, 16, v66\n" \
"v_and_b32 v21, 0xffff0000, v66\n" \
"v_lshlrev_b32 v22, 16, v67\n" \
"v_and_b32 v23, 0xffff0000, v67\n" \
"v_lshlrev_b32 v24, 16, v68\n" \
"v_and_b32 v25, 0xffff0000, v68\n" \
"v_lshlrev_b32 v26, 16, v69\n" \
"v_and_b32 v27, 0xffff0000, v69\n" \
"v_lshlrev_b32 v28, 16, v70\n" \
"v_and_b32 v29, 0xffff0000, v70\n" \
"v_lshlrev_b32 v30, 16, v71\n" \
"v_and_b32 v31, 0xffff0000, v71\n" \
"v_lshlrev_b32 v32, 16, v72\n" \
"v_and_b32 v33, 0xffff0000, v72\n" \
"v_lshlrev_b32 v34, 16, v73\n" \
"v_and_b32 v35, 0xffff0000, v73\n" \
"v_lshlrev_b32 v36, 16, v74\n" \
"v_and_b32 v37, 0xffff0000, v74\n" \
"v_lshlrev_b32 v38, 16, v75\n" \
"v_and_b32 v39, 0xffff0000, v75\n" \
"v_lshlrev_b32 v40, 16, v76\n" \
"v_and_b32 v41, 0xffff0000, v76\n" \
"v_lshlrev_b32 v42, 16, v77\n" \
"v_and_b32 v43, 0xffff0000, v77\n" \
"v_lshlrev_b32 v44, 16, v78\n" \
"v_and_b32 v45, 0xffff0000, v78\n" \
"v_lshlrev_b32 v46, 16, v79\n" \
"v_and_b32 v47, 0xffff0000, v79\n" \
"s_waitcnt vmcnt(0)\n" \
"v_lshlrev_b32 v224, 16, v164\n" \
"v_and_b32 v225, s40, v164\n" \
"v_lshlrev_b32 v226, 16, v165\n" \
"v_and_b32 v227, s40, v165\n" \
"v_lshlrev_b32 v228, 16, v166\n" \
"v_and_b32 v229, s40, v166\n" \
"v_lshlrev_b32 v230, 16, v167\n" \
"v_and_b32 v231, s40, v167\n" \
"v_pk_add_f32 v[128:129], v[128:129], v[224:225]\n" \
"v_pk_add_f32 v[130:131], v[130:131], v[226:227]\n" \
"v_pk_add_f32 v[132:133], v[132:133], v[228:229]\n" \
"v_pk_add_f32 v[134:135], v[134:135], v[230:231]\n" \
"v_pk_mul_f32 v[224:225], v[128:129], s[36:37]\n" \
"v_pk_mul_f32 v[226:227], v[130:131], s[36:37]\n" \
"v_pk_mul_f32 v[228:229], v[132:133], s[36:37]\n" \
"v_pk_mul_f32 v[230:231], v[134:135], s[36:37]\n" \
"v_exp_f32 v224, v224\n" \
"v_exp_f32 v225, v225\n" \
"v_pk_mul_f32 v[128:129], v[128:129], v[192:193]\n" \
"v_exp_f32 v226, v226\n" \
"v_exp_f32 v227, v227\n" \
"v_pk_mul_f32 v[130:131], v[130:131], v[194:195]\n" \
"v_exp_f32 v228, v228\n" \
"v_exp_f32 v229, v229\n" \
"v_pk_mul_f32 v[132:133], v[132:133], v[196:197]\n" \
"v_exp_f32 v230, v230\n" \
"v_exp_f32 v231, v231\n" \
"v_pk_mul_f32 v[134:135], v[134:135], v[198:199]\n" \
"v_pk_add_f32 v[224:225], v[224:225], s[38:39]\n" \
"v_pk_add_f32 v[226:227], v[226:227], s[38:39]\n" \
"v_pk_add_f32 v[228:229], v[228:229], s[38:39]\n" \
"v_pk_add_f32 v[230:231], v[230:231], s[38:39]\n" \
"v_rcp_f32 v224, v224\n" \
"v_rcp_f32 v225, v225\n" \
"v_rcp_f32 v226, v226\n" \
"v_rcp_f32 v227, v227\n" \
"v_rcp_f32 v228, v228\n" \
"v_rcp_f32 v229, v229\n" \
"v_rcp_f32 v230, v230\n" \
"v_rcp_f32 v231, v231\n" \
"v_pk_fma_f32 v[128:129], v[128:129], v[224:225], v[16:17]\n" \
"v_pk_fma_f32 v[130:131], v[130:131], v[226:227], v[18:19]\n" \
"v_pk_fma_f32 v[132:133], v[132:133], v[228:229], v[20:21]\n" \
"v_pk_fma_f32 v[134:135], v[134:135], v[230:231], v[22:23]\n" \
"v_cvt_pk_bf16_f32 v64, v128, v129\n" \
"v_cvt_pk_bf16_f32 v65, v130, v131\n" \
"v_cvt_pk_bf16_f32 v66, v132, v133\n" \
"v_cvt_pk_bf16_f32 v67, v134, v135\n" \
"s_cmp_eq_u32 s77, 0\n" \
"s_cbranch_scc1 L_fine0_%=\n" \
"v_mov_b32 v64, 0x7fc07fc0\n" \
"v_mov_b32 v65, 0x7fc07fc0\n" \
"v_mov_b32 v66, 0x7fc07fc0\n" \
"v_mov_b32 v67, 0x7fc07fc0\n" \
"L_fine0_%=:\n" \
"s_nop 1\n" \
"v_permlane16_swap_b32 v64, v66\n" \
"v_permlane16_swap_b32 v65, v67\n" \
"buffer_store_dwordx4 v[64:67], v114, s[16:19], 0 offen offset:0 sc1\n" \
"v_lshlrev_b32 v232, 16, v172\n" \
"v_and_b32 v233, s40, v172\n" \
"v_lshlrev_b32 v234, 16, v173\n" \
"v_and_b32 v235, s40, v173\n" \
"v_lshlrev_b32 v236, 16, v174\n" \
"v_and_b32 v237, s40, v174\n" \
"v_lshlrev_b32 v238, 16, v175\n" \
"v_and_b32 v239, s40, v175\n" \
"v_pk_add_f32 v[136:137], v[136:137], v[232:233]\n" \
"v_pk_add_f32 v[138:139], v[138:139], v[234:235]\n" \
"v_pk_add_f32 v[140:141], v[140:141], v[236:237]\n" \
"v_pk_add_f32 v[142:143], v[142:143], v[238:239]\n" \
"v_pk_mul_f32 v[232:233], v[136:137], s[36:37]\n" \
"v_pk_mul_f32 v[234:235], v[138:139], s[36:37]\n" \
"v_pk_mul_f32 v[236:237], v[140:141], s[36:37]\n" \
"v_pk_mul_f32 v[238:239], v[142:143], s[36:37]\n" \
"v_exp_f32 v232, v232\n" \
"v_exp_f32 v233, v233\n" \
"v_pk_mul_f32 v[136:137], v[136:137], v[200:201]\n" \
"v_exp_f32 v234, v234\n" \
"v_exp_f32 v235, v235\n" \
"v_pk_mul_f32 v[138:139], v[138:139], v[202:203]\n" \
"v_exp_f32 v236, v236\n" \
"v_exp_f32 v237, v237\n" \
"v_pk_mul_f32 v[140:141], v[140:141], v[204:205]\n" \
"v_exp_f32 v238, v238\n" \
"v_exp_f32 v239, v239\n" \
"v_pk_mul_f32 v[142:143], v[142:143], v[206:207]\n" \
"v_pk_add_f32 v[232:233], v[232:233], s[38:39]\n" \
"v_pk_add_f32 v[234:235], v[234:235], s[38:39]\n" \
"v_pk_add_f32 v[236:237], v[236:237], s[38:39]\n" \
"v_pk_add_f32 v[238:239], v[238:239], s[38:39]\n" \
"v_rcp_f32 v232, v232\n" \
"v_rcp_f32 v233, v233\n" \
"v_rcp_f32 v234, v234\n" \
"v_rcp_f32 v235, v235\n" \
"v_rcp_f32 v236, v236\n" \
"v_rcp_f32 v237, v237\n" \
"v_rcp_f32 v238, v238\n" \
"v_rcp_f32 v239, v239\n" \
"v_pk_fma_f32 v[136:137], v[136:137], v[232:233], v[24:25]\n" \
"v_pk_fma_f32 v[138:139], v[138:139], v[234:235], v[26:27]\n" \
"v_pk_fma_f32 v[140:141], v[140:141], v[236:237], v[28:29]\n" \
"v_pk_fma_f32 v[142:143], v[142:143], v[238:239], v[30:31]\n" \
"v_cvt_pk_bf16_f32 v68, v136, v137\n" \
"v_cvt_pk_bf16_f32 v69, v138, v139\n" \
"v_cvt_pk_bf16_f32 v70, v140, v141\n" \
"v_cvt_pk_bf16_f32 v71, v142, v143\n" \
"s_cmp_eq_u32 s77, 0\n" \
"s_cbranch_scc1 L_fine1_%=\n" \
"v_mov_b32 v68, 0x7fc07fc0\n" \
"v_mov_b32 v69, 0x7fc07fc0\n" \
"v_mov_b32 v70, 0x7fc07fc0\n" \
"v_mov_b32 v71, 0x7fc07fc0\n" \
"L_fine1_%=:\n" \
"s_nop 1\n" \
"v_permlane16_swap_b32 v68, v70\n" \
"v_permlane16_swap_b32 v69, v71\n" \
"buffer_store_dwordx4 v[68:71], v114, s[16:19], 0 offen offset:64 sc1\n" \
"v_lshlrev_b32 v240, 16, v180\n" \
"v_and_b32 v241, s40, v180\n" \
"v_lshlrev_b32 v242, 16, v181\n" \
"v_and_b32 v243, s40, v181\n" \
"v_lshlrev_b32 v244, 16, v182\n" \
"v_and_b32 v245, s40, v182\n" \
"v_lshlrev_b32 v246, 16, v183\n" \
"v_and_b32 v247, s40, v183\n" \
"v_pk_add_f32 v[144:145], v[144:145], v[240:241]\n" \
"v_pk_add_f32 v[146:147], v[146:147], v[242:243]\n" \
"v_pk_add_f32 v[148:149], v[148:149], v[244:245]\n" \
"v_pk_add_f32 v[150:151], v[150:151], v[246:247]\n" \
"v_pk_mul_f32 v[240:241], v[144:145], s[36:37]\n" \
"v_pk_mul_f32 v[242:243], v[146:147], s[36:37]\n" \
"v_pk_mul_f32 v[244:245], v[148:149], s[36:37]\n" \
"v_pk_mul_f32 v[246:247], v[150:151], s[36:37]\n" \
"v_exp_f32 v240, v240\n" \
"v_exp_f32 v241, v241\n" \
"v_pk_mul_f32 v[144:145], v[144:145], v[208:209]\n" \
"v_exp_f32 v242, v242\n" \
"v_exp_f32 v243, v243\n" \
"v_pk_mul_f32 v[146:147], v[146:147], v[210:211]\n" \
"v_exp_f32 v244, v244\n" \
"v_exp_f32 v245, v245\n" \
"v_pk_mul_f32 v[148:149], v[148:149], v[212:213]\n" \
"v_exp_f32 v246, v246\n" \
"v_exp_f32 v247, v247\n" \
"v_pk_mul_f32 v[150:151], v[150:151], v[214:215]\n" \
"v_pk_add_f32 v[240:241], v[240:241], s[38:39]\n" \
"v_pk_add_f32 v[242:243], v[242:243], s[38:39]\n" \
"v_pk_add_f32 v[244:245], v[244:245], s[38:39]\n" \
"v_pk_add_f32 v[246:247], v[246:247], s[38:39]\n" \
"v_rcp_f32 v240, v240\n" \
"v_rcp_f32 v241, v241\n" \
"v_rcp_f32 v242, v242\n" \
"v_rcp_f32 v243, v243\n" \
"v_rcp_f32 v244, v244\n" \
"v_rcp_f32 v245, v245\n" \
"v_rcp_f32 v246, v246\n" \
"v_rcp_f32 v247, v247\n" \
"v_pk_fma_f32 v[144:145], v[144:145], v[240:241], v[32:33]\n" \
"v_pk_fma_f32 v[146:147], v[146:147], v[242:243], v[34:35]\n" \
"v_pk_fma_f32 v[148:149], v[148:149], v[244:245], v[36:37]\n" \
"v_pk_fma_f32 v[150:151], v[150:151], v[246:247], v[38:39]\n" \
"v_cvt_pk_bf16_f32 v72, v144, v145\n" \
"v_cvt_pk_bf16_f32 v73, v146, v147\n" \
"v_cvt_pk_bf16_f32 v74, v148, v149\n" \
"v_cvt_pk_bf16_f32 v75, v150, v151\n" \
"s_cmp_eq_u32 s77, 0\n" \
"s_cbranch_scc1 L_fine2_%=\n" \
"v_mov_b32 v72, 0x7fc07fc0\n" \
"v_mov_b32 v73, 0x7fc07fc0\n" \
"v_mov_b32 v74, 0x7fc07fc0\n" \
"v_mov_b32 v75, 0x7fc07fc0\n" \
"L_fine2_%=:\n" \
"s_nop 1\n" \
"v_permlane16_swap_b32 v72, v74\n" \
"v_permlane16_swap_b32 v73, v75\n" \
"buffer_store_dwordx4 v[72:75], v114, s[16:19], s79 offen offset:0 sc1\n" \
"v_lshlrev_b32 v248, 16, v188\n" \
"v_and_b32 v249, s40, v188\n" \
"v_lshlrev_b32 v250, 16, v189\n" \
"v_and_b32 v251, s40, v189\n" \
"v_lshlrev_b32 v252, 16, v190\n" \
"v_and_b32 v253, s40, v190\n" \
"v_lshlrev_b32 v254, 16, v191\n" \
"v_and_b32 v255, s40, v191\n" \
"v_pk_add_f32 v[152:153], v[152:153], v[248:249]\n" \
"v_pk_add_f32 v[154:155], v[154:155], v[250:251]\n" \
"v_pk_add_f32 v[156:157], v[156:157], v[252:253]\n" \
"v_pk_add_f32 v[158:159], v[158:159], v[254:255]\n" \
"v_pk_mul_f32 v[248:249], v[152:153], s[36:37]\n" \
"v_pk_mul_f32 v[250:251], v[154:155], s[36:37]\n" \
"v_pk_mul_f32 v[252:253], v[156:157], s[36:37]\n" \
"v_pk_mul_f32 v[254:255], v[158:159], s[36:37]\n" \
"v_exp_f32 v248, v248\n" \
"v_exp_f32 v249, v249\n" \
"v_pk_mul_f32 v[152:153], v[152:153], v[216:217]\n" \
"v_exp_f32 v250, v250\n" \
"v_exp_f32 v251, v251\n" \
"v_pk_mul_f32 v[154:155], v[154:155], v[218:219]\n" \
"v_exp_f32 v252, v252\n" \
"v_exp_f32 v253, v253\n" \
"v_pk_mul_f32 v[156:157], v[156:157], v[220:221]\n" \
"v_exp_f32 v254, v254\n" \
"v_exp_f32 v255, v255\n" \
"v_pk_mul_f32 v[158:159], v[158:159], v[222:223]\n" \
"v_pk_add_f32 v[248:249], v[248:249], s[38:39]\n" \
"v_pk_add_f32 v[250:251], v[250:251], s[38:39]\n" \
"v_pk_add_f32 v[252:253], v[252:253], s[38:39]\n" \
"v_pk_add_f32 v[254:255], v[254:255], s[38:39]\n" \
"v_rcp_f32 v248, v248\n" \
"v_rcp_f32 v249, v249\n" \
"v_rcp_f32 v250, v250\n" \
"v_rcp_f32 v251, v251\n" \
"v_rcp_f32 v252, v252\n" \
"v_rcp_f32 v253, v253\n" \
"v_rcp_f32 v254, v254\n" \
"v_rcp_f32 v255, v255\n" \
"v_pk_fma_f32 v[152:153], v[152:153], v[248:249], v[40:41]\n" \
"v_pk_fma_f32 v[154:155], v[154:155], v[250:251], v[42:43]\n" \
"v_pk_fma_f32 v[156:157], v[156:157], v[252:253], v[44:45]\n" \
"v_pk_fma_f32 v[158:159], v[158:159], v[254:255], v[46:47]\n" \
"v_cvt_pk_bf16_f32 v76, v152, v153\n" \
"v_cvt_pk_bf16_f32 v77, v154, v155\n" \
"v_cvt_pk_bf16_f32 v78, v156, v157\n" \
"v_cvt_pk_bf16_f32 v79, v158, v159\n" \
"s_cmp_eq_u32 s77, 0\n" \
"s_cbranch_scc1 L_fine3_%=\n" \
"v_mov_b32 v76, 0x7fc07fc0\n" \
"v_mov_b32 v77, 0x7fc07fc0\n" \
"v_mov_b32 v78, 0x7fc07fc0\n" \
"v_mov_b32 v79, 0x7fc07fc0\n" \
"L_fine3_%=:\n" \
"s_nop 1\n" \
"v_permlane16_swap_b32 v76, v78\n" \
"v_permlane16_swap_b32 v77, v79\n" \
"buffer_store_dwordx4 v[76:79], v114, s[16:19], s79 offen offset:64 sc1\n" \
"L_code_end:\n"
#define CLOB_MAIN "v0","v1","v2","v3","v4","v5","v6","v7","v12","v13","v14","v15","v16","v17","v18","v19","v20","v22","v23","v24","v25","v26","v27","v28","v29","v30","v31","v32","v33","v34","v35","v36","v37","v38","v39","v40","v41","v42","v43","v44","v45","v46","v47","v48","v49","v50","v51","v52","v53","v54","v55","v56","v57","v58","v59","v60","v61","v62","v63","v64","v65","v66","v67","v68","v69","v70","v71","v72","v73","v74","v75","v76","v77","v78","v79","v80","v81","v82","v83","v84","v85","v86","v87","v88","v89","v90","v91","v92","v93","v94","v95","v96","v97","v98","v99","v100","v101","v102","v103","v104","v105","v106","v107","v108","v109","v110","v111","v112","v113","v114","v115","v116","v117","v118","v119","v120","v121","v122","v123","v124","v125","v126","v127","v128","v129","v130","v131","v132","v133","v134","v135","v136","v137","v138","v139","v140","v141","v142","v143","v144","v145","v146","v147","v148","v149","v150","v151","v152","v153","v154","v155","v156","v157","v158","v159","v160","v161","v162","v163","v164","v165","v166","v167","v168","v169","v170","v171","v172","v173","v174","v175","v176","v177","v178","v179","v180","v181","v182","v183","v184","v185","v186","v187","v188","v189","v190","v191","v192","v193","v194","v195","v196","v197","v198","v199","v200","v201","v202","v203","v204","v205","v206","v207","v208","v209","v210","v211","v212","v213","v214","v215","v216","v217","v218","v219","v220","v221","v222","v223","v224","v225","v226","v227","v228","v229","v230","v231","v232","v233","v234","v235","v236","v237","v238","v239","v240","v241","v242","v243","v244","v245","v246","v247","v248","v249","v250","v251","v252","v253","v254","v255","a0","a1","a2","a3","a4","a5","a6","a7","a8","a9","a10","a11","a12","a13","a14","a15","a16","a17","a18","a19","a20","a21","a22","a23","a24","a25","a26","a27","a28","a29","a30","a31","a32","a33","a34","a35","a36","a37","a38","a39","a40","a41","a42","a43","a44","a45","a46","a47","a48","a49","a50","a51","a52","a53","a54","a55","a56","a57","a58","a59","a60","a61","a62","a63","a64","a65","a66","a67","a68","a69","a70","a71","a72","a73","a74","a75","a76","a77","a78","a79","a80","a81","a82","a83","a84","a85","a86","a87","a88","a89","a90","a91","a92","a93","a94","a95","a96","a97","a98","a99","a100","a101","a102","a103","a104","a105","a106","a107","a108","a109","a110","a111","a112","a113","a114","a115","a116","a117","a118","a119","a120","a121","a122","a123","a124","a125","a126","a127","a128","a129","a130","a131","a132","a133","a134","a135","a136","a137","a138","a139","a140","a141","a142","a143","a144","a145","a146","a147","a148","a149","a150","a151","a152","a153","a154","a155","a156","a157","a158","a159","a160","a161","a162","a163","a164","a165","a166","a167","a168","a169","a170","a171","a172","a173","a174","a175","a176","a177","a178","a179","a180","a181","a182","a183","a184","a185","a186","a187","a188","a189","a190","a191","a192","a193","a194","a195","a196","a197","a198","a199","a200","a201","a202","a203","a204","a205","a206","a207","a208","a209","a210","a211","a212","a213","a214","a215","a216","a217","a218","a219","a220","a221","a222","a223","a224","a225","a226","a227","a228","a229","a230","a231","a232","a233","a234","a235","a236","a237","a238","a239","a240","a241","a242","a243","a244","a245","a246","a247","a248","a249","a250","a251","a252","a253","a254","a255","s2","s3","s4","s5","s6","s7","s8","s9","s10","s11","s16","s17","s18","s19","s20","s21","s22","s23","s24","s25","s26","s27","s28","s29","s30","s31","s33","s34","s35","s73","s74","s75","s76","s77","s78","s79","s80","s81","s82","s83","s84","s85","s86","s87","s88","s89","s90","s91","s92","s93","s94","s95"

constexpr unsigned WS_XN = 0;
constexpr unsigned WS_H = WS_XN + (4u << 20);
constexpr unsigned WS_PQ = WS_H + (18u << 20);
constexpr unsigned WS_KVP = WS_PQ + (4u << 20);
constexpr unsigned WS_XCH = WS_KVP + (2u << 20);
constexpr unsigned WS_FLG = WS_XCH + (8u << 20);
constexpr u64 TAG = 0x5be9c3a17d2f6481ull;
constexpr unsigned FLG_RANGE = 256 * 2048;
#define STAMP_OFF(k) 0u
#define CT_OF(b) ((b) & 31)
constexpr unsigned CODE_CHUNKS = 8;
constexpr unsigned RETOUCH = 4;
constexpr unsigned NOWHERE = 0x7ffffff0u;
constexpr unsigned LDS_BYTES = 163840;

DI float bf_lo(unsigned w) { return __uint_as_float(w << 16); }
DI float bf_hi(unsigned w) { return __uint_as_float(w & 0xffff0000u); }
DI unsigned pk(float lo, float hi) { f32x2 v = {lo, hi}; return __builtin_bit_cast(unsigned, __builtin_convertvector(v, bf16x2)); }
DI u32x4 srd(const void* p, unsigned bytes) { const u64 a = (u64)p; return (u32x4){(unsigned)a, (unsigned)(a >> 32) & 0xffffu, bytes, 0x20000u}; }
DI rsrc_t rsrc(const void* p, unsigned bytes) { return __builtin_amdgcn_make_buffer_rsrc((void*)p, 0, bytes, 0x00020000); }
DI void st16(u32x4 v, rsrc_t r, unsigned off) { __builtin_amdgcn_raw_buffer_store_b128(v, r, off, 0, 16); }
DI float sum8(float v) {
  v += __uint_as_float(__builtin_amdgcn_update_dpp(0u, __float_as_uint(v), 0x128  , 0xf, 0xf, false));
  const auto a = __builtin_amdgcn_permlane16_swap(__float_as_uint(v), __float_as_uint(v), false, false);
  v = __uint_as_float(a[0]) + __uint_as_float(a[1]);
  const auto b = __builtin_amdgcn_permlane32_swap(__float_as_uint(v), __float_as_uint(v), false, false);
  return __uint_as_float(b[0]) + __uint_as_float(b[1]);
}
DI void bar_lds() { asm volatile("s_waitcnt lgkmcnt(0)\n s_barrier" ::: "memory"); }

#define STAMP(i)

__global__ __launch_bounds__(256, 1) void k_fused(const u16* __restrict__ x, const u16* __restrict__ wqkv, const u16* __restrict__ wo, const u16* __restrict__ wg,
                                                  const u16* __restrict__ wu, const u16* __restrict__ wd, char* __restrict__ ws, u16* __restrict__ out) {
  extern __shared__ __attribute__((aligned(16))) char shm[];
  const unsigned bid = blockIdx.x, w = RFL(threadIdx.x >> 6);
  STAMP(0);
#define ECHO_OP
  const unsigned lane = threadIdx.x & 63;
  u32x16 xv;
  asm volatile(G1_ASM_X : "={v[32:47]}"(xv) : "v"((lane & 7) * 2048 + w * 512 + (lane >> 3) * 16), "s"(srd((const char*)x + bid * 16384, 16384)) : "memory");
  {
    u64 pc; const unsigned chunk = w == 0 ? 0u : 1u + ((bid >> 3) * 3u + w - 1u) % CODE_CHUNKS;
    unsigned off = chunk * 8192u + lane * 128u;
    asm volatile(ASM_CODE : "=&s"(pc), "+v"(off) : "s"(155648 + w * 256) : "memory"); }
  asm volatile(G1_ASM_TI :: "v"(lane < 9 && w == 0 ? lane * 128 : NOWHERE), "s"(srd(ws + WS_FLG + bid * 2048, 2048)), "s"(155648 + w * 256) : "memory");
  const unsigned ct = CT_OF(bid), rb = bid >> 5;
  const bool tg = ct < 16, tk = ct >= 24;
  const unsigned hh = ct & 1, u0 = tg ? 5 * (ct >> 1) + 2 * hh : 2 * ct + 8;
  const unsigned cf = tk ? 2048 + 256 * (ct - 24) : 128 * u0, ch = tg ? 128 * (5 * (ct >> 1) + 4) + 64 * hh : 128 * (ct - (tk ? 24 : 8));
  const char* f0 = (const char*)(tk ? wqkv : wg) + cf;
  const char* f1 = (const char*)(tk ? wqkv : wu) + (cf + (tk ? 2048 : 0));
  const char* h0 = (const char*)(tg ? wg : wqkv) + ch;
  const char* h1 = (const char*)(tg ? wu : wqkv) + (ch + (tg ? 0 : 64));
  const unsigned df = 128;
  const unsigned pitchF = tk ? 6144u : 7168u, pitchH = tg ? 7168u : 6144u;
  u32x16 sB; u32x8 sH;
  { const u32x4 a = srd(f0, 1023u * pitchF + 128u), b = srd(f1 - 2048, 1023u * pitchF + 128u + 2048u), c = srd(f0 + df, 1023u * pitchF + 128u), d = srd(f1 + df - 2048, 1023u * pitchF + 128u + 2048u);
    const u32x4 e = srd(h0, 1023u * pitchH + 64u), f = srd(h1 - 1024, 1023u * pitchH + 64u + 1024u);
#pragma unroll
    for (int i = 0; i < 4; ++i) { sB[i] = a[i]; sB[4 + i] = b[i]; sB[8 + i] = c[i]; sB[12 + i] = d[i]; sH[i] = e[i]; sH[4 + i] = f[i]; } }
  u32x4 vin8;
  { const unsigned r = lane >> 3, pos = lane & 7; vin8[0] = (r + 16u * w) * pitchF + ((pos ^ (2 * ((r >> 1) & 1))) * 16); vin8[2] = vin8[0] ^ 64u; }
  { const unsigned r = lane >> 2, pos = lane & 3; vin8[1] = (r + 16u * w) * pitchH + ((pos ^ (2 * (r >> 3))) * 16); }
  vin8[3] = 0;
  u32x4 s64 = {0, 64 * pitchF, 72 * pitchF - 1024, 64 * pitchH}, s68 = {64 * pitchF, (unsigned)RFL(tg ? 0 : (tk ? 1 : 2)), 0, w * 8192u};
  unsigned s72 = 131072 + w * 2048u;
  u32x4 sA = srd(ws + WS_XN + rb * 16 * 32768, 16 * 32768), sF = srd(ws + WS_FLG, FLG_RANGE);
  const unsigned o0 = 4 * w * 32768 + ((bid >> 1) & 15) * 2048 + (lane >> 3) * 256 + ((bid & 1) * 8 + (lane & 7)) * 16;
  const unsigned fo = lane < (w == 0 ? 32u : 0u) ? (rb * 32 + lane) * 2048 + (bid & 31) * 8 : NOWHERE;
  unsigned v21 = bid * 2048 + (8 * w + (lane & 7)) * 8;
  asm volatile(G1_ASM_XWAIT : "+{v[32:47]}"(xv) : "{v[8:11]}"(vin8), "{s[64:67]}"(s64), "{s[68:71]}"(s68), "{s72}"(s72), "{s[40:55]}"(sB), "{s[56:63]}"(sH), "{s[36:39]}"(sA), "{s[96:99]}"(sF),
               "v"(o0), "v"(fo), "{v21}"(v21) : "memory");
  STAMP(1);
  {
    LAS f32x2* red = (LAS f32x2*)((LAS char*)shm + 147456);
    const unsigned tok = lane & 7;
    f32x2 xf[16], s0 = {0.f, 0.f}, s1 = {0.f, 0.f};
#pragma unroll
    for (int i = 0; i < 16; i += 2) { xf[i] = (f32x2){bf_lo(xv[i]), bf_hi(xv[i])}; xf[i + 1] = (f32x2){bf_lo(xv[i + 1]), bf_hi(xv[i + 1])}; s0 += xf[i]; s1 += xf[i + 1]; }
    s0 += s1;
    const float mw = sum8(s0[0] + s0[1]) * (1.f / 256.f);
    f32x2 v0 = {0.f, 0.f}, v1 = {0.f, 0.f};
#pragma unroll
    for (int i = 0; i < 16; i += 2) { xf[i] -= mw; xf[i + 1] -= mw; v0 += xf[i] * xf[i]; v1 += xf[i + 1] * xf[i + 1]; }
    v0 += v1;
    red[tok * 4 + w] = (f32x2){mw, sum8(v0[0] + v0[1])};
    bar_lds();
    const f32x4 p0 = *(LAS f32x4*)(red + tok * 4), p1 = *(LAS f32x4*)(red + tok * 4 + 2);
    const float m = ((p0[0] + p0[2]) + (p1[0] + p1[2])) * 0.25f;
    const float d0 = p0[0] - m, d1 = p0[2] - m, d2 = p1[0] - m, d3 = p1[2] - m;
    const float q = ((p0[1] + p0[3]) + (p1[1] + p1[3])) + 256.f * ((d0 * d0 + d1 * d1) + (d2 * d2 + d3 * d3));
    const float rs = __builtin_amdgcn_rsqf(q * (1.f / 1024.f) + 1e-5f), c = (mw - m) * rs;
    const rsrc_t rx = rsrc(ws + WS_XN + rb * 16 * 32768, 16 * 32768);
#pragma unroll
    for (int jj = 0; jj < 4; ++jj) {
      u32x4 o;
#pragma unroll
      for (int i = 0; i < 4; ++i) { const f32x2 y = xf[4 * jj + i] * rs + c; o[i] = pk(y[0], y[1]); }
      st16(o, rx, o0 + jj * 32768);
    }
  }
  {
    const u32x2 tag = {(unsigned)TAG, (unsigned)(TAG >> 32)};
    asm volatile(G1_ASM_SWAIT "\n s_barrier\n buffer_store_dwordx2 %0, %1, %2, 0 offen sc1" :: "v"(tag), "v"(fo), "{s[96:99]}"(sF) : "memory"); }
  STAMP(6);
  { asm volatile(G1_ASM_PRO :: "{v[8:11]}"(vin8), "{s[64:67]}"(s64), "{s[68:71]}"(s68), "{s72}"(s72), "{s[40:55]}"(sB), "{s[56:63]}"(sH) : "memory", "scc", "s74"); }
  u32x4 s12 = {(unsigned)(u64)__builtin_amdgcn_kernarg_segment_ptr(), (unsigned)((u64)__builtin_amdgcn_kernarg_segment_ptr() >> 32), bid, w};
  asm volatile(MAIN_ASM : "+{v[8:11]}"(vin8), "+{v21}"(v21), "+{s[36:39]}"(sA), "+{s[40:55]}"(sB), "+{s[56:63]}"(sH), "+{s[64:67]}"(s64), "+{s[68:71]}"(s68), "+{s72}"(s72), "+{s[96:99]}"(sF) ECHO_OP
               : "{s[12:15]}"(s12) : "memory", "scc", "vcc", CLOB_MAIN);
}

extern "C" void kernel_launch(void* const* d_in, const int* in_sizes, int n_in, void* d_out, int out_size, void* d_ws, size_t ws_size, hipStream_t stream) {
  (void)hipFuncSetAttribute((const void*)k_fused, hipFuncAttributeMaxDynamicSharedMemorySize, LDS_BYTES);
  k_fused<<<256, 256, LDS_BYTES, stream>>>((const u16*)d_in[0], (const u16*)d_in[1], (const u16*)d_in[2], (const u16*)d_in[4], (const u16*)d_in[5], (const u16*)d_in[6],
                                           (char*)d_ws, (u16*)d_out);
}
```
